```python
import torch

def _softmax(x, axis=-1):
    return torch.softmax(x, dim=axis)
def _gelu(x):
    return torch.nn.functional.gelu(x, approximate="tanh")
def _silu(x):
    return torch.nn.functional.silu(x)
def _sigmoid(x):
    return torch.sigmoid(x)
def _layernorm(x, axis=-1, eps=1e-5):
    m = x.mean(dim=axis, keepdim=True)
    v = x.var(dim=axis, keepdim=True, unbiased=False)
    return (x - m) / torch.sqrt(v + eps)
def _scatter_add(tgt, idx, src, axis=0):
    return tgt.clone().index_add_(axis, idx, src)
def _segment_sum(src, seg, n):
    out = torch.zeros((n,) + src.shape[1:], dtype=src.dtype)
    return out.index_add_(0, seg, src)
def _pad(x, pw):
    flat = tuple(v for pair in reversed(pw) for v in pair)
    return torch.nn.functional.pad(x, flat)

kernel_name = 'recurrent_scan__spectral_fnet__geglu_ffn__prompt'

def setup_inputs():

    _w = (lambda a: (lambda t: t.to(torch.bfloat16) if t.is_floating_point() else t)(torch.as_tensor(a)))
    def _A_setup():
        B, S, D = 1, 2048, 448
        ns = torch.arange(S)[:, None] * torch.arange(S)[None, :]
        nd = torch.arange(D)[:, None] * torch.arange(D)[None, :]
        Ws_re = torch.cos(-2.0 * torch.pi * ns / S).to(float)
        Ws_im = torch.sin(-2.0 * torch.pi * ns / S).to(float)
        Wd_re = torch.cos(-2.0 * torch.pi * nd / D).to(float)
        Wd_im = torch.sin(-2.0 * torch.pi * nd / D).to(float)
        return {
            "x": _w((lambda *s: torch.randn(*s, dtype=torch.bfloat16) * 0.05)(B, S, D)),
            "dft_seq_re": _w(Ws_re),
            "dft_seq_im": _w(Ws_im),
            "dft_hid_re": _w(Wd_re),
            "dft_hid_im": _w(Wd_im),
        }
    _A_in = _A_setup()

    def _B_setup():
        B, S, D, F = 1, 2048, 448, 1792
        return {
            "x": _w((lambda *s: torch.randn(*s, dtype=torch.bfloat16) * 0.05)(B, S, D)),
            "w_gate": _w((lambda *s: torch.randn(*s, dtype=torch.bfloat16) * 0.05)(D, F)),
            "w_up": _w((lambda *s: torch.randn(*s, dtype=torch.bfloat16) * 0.05)(D, F)),
            "w_down": _w((lambda *s: torch.randn(*s, dtype=torch.bfloat16) * 0.05)(F, D)),
        }
    _B_in = _B_setup()

    return {
        "A_x": _A_in["x"],
        "A_dft_seq_re": _A_in["dft_seq_re"],
        "A_dft_seq_im": _A_in["dft_seq_im"],
        "A_dft_hid_re": _A_in["dft_hid_re"],
        "A_dft_hid_im": _A_in["dft_hid_im"],
        "B_x": _B_in["x"],
        "B_w_gate": _B_in["w_gate"],
        "B_w_up": _B_in["w_up"],
        "B_w_down": _B_in["w_down"],
    }

def reference(A_x, A_dft_seq_re, A_dft_seq_im, A_dft_hid_re, A_dft_hid_im, B_x, B_w_gate, B_w_up, B_w_down):
    def _A_ref(x, dft_seq_re, dft_seq_im, dft_hid_re, dft_hid_im):
        a = torch.einsum("bsd,ed->bse", x, dft_hid_re)
        b = torch.einsum("bsd,ed->bse", x, dft_hid_im)
        y = torch.einsum("ts,bse->bte", dft_seq_re, a) - torch.einsum("ts,bse->bte", dft_seq_im, b)
        return y

    def _B_ref(x, w_gate, w_up, w_down):
        return (_gelu(x @ w_gate) * (x @ w_up)) @ w_down

    x = A_x
    xn = _layernorm(x, axis=-1)
    s = _A_ref(xn, A_dft_seq_re, A_dft_seq_im, A_dft_hid_re, A_dft_hid_im)
    g = _silu(_B_ref(xn, B_w_gate, B_w_up, B_w_down))
    return x + s * g
```

```cpp
#include <hip/hip_runtime.h>
#include <hip/hip_bf16.h>
typedef unsigned short u16;
__device__ __forceinline__ float bf2f(u16 v){ return __uint_as_float(((unsigned)v)<<16); }
__device__ __forceinline__ u16 f2bf(float f){ __hip_bfloat16 b = __float2bfloat16(f); return *(u16*)&b; }
__device__ __forceinline__ float rbf(float f){ return bf2f(f2bf(f)); }
constexpr int S=2048, D=448, F=1792;

__global__ void ln_k(const u16* x, u16* xn){
  int row=blockIdx.x; int t=threadIdx.x;
  float v[7]; float s=0;
  for(int i=0;i<7;i++){ v[i]=bf2f(x[row*D+t+64*i]); s+=v[i]; }
  for(int o=32;o>0;o>>=1) s+=__shfl_xor(s,o);
  float m=s/D;
  float q=0; for(int i=0;i<7;i++){ float d=v[i]-m; q+=d*d; }
  for(int o=32;o>0;o>>=1) q+=__shfl_xor(q,o);
  float var=rbf(q/D); float mb=rbf(m);
  float sd=rbf(sqrtf(rbf(var+1e-5f)));
  for(int i=0;i<7;i++){ xn[row*D+t+64*i]=f2bf(rbf(v[i]-mb)/sd); }
}
__global__ void gemm_naive(const u16* A,int lda,const u16* B,int sbk,int sbn,float* C,int ldc,int M,int N,int K,float alpha,int accum){
  int n=blockIdx.x*64+threadIdx.x; int m=blockIdx.y;
  if(n>=N) return;
  float s=0;
  for(int k=0;k<K;k++) s+=bf2f(A[(size_t)m*lda+k])*bf2f(B[(size_t)k*sbk+(size_t)n*sbn]);
  s=rbf(s)*alpha;
  if(accum) s=rbf(s+C[(size_t)m*ldc+n]);
  C[(size_t)m*ldc+n]=s;
}
__global__ void cvt(const float* a,u16* b,int n){ int i=blockIdx.x*256+threadIdx.x; if(i<n) b[i]=f2bf(a[i]); }
__device__ float gelu_t(float x){ float u=0.7978845608028654f*(x+0.044715f*x*x*x); return 0.5f*x*(1.f+tanhf(u)); }
__global__ void geglu(const float* g,const float* u,u16* h,int n){ int i=blockIdx.x*256+threadIdx.x; if(i<n) h[i]=f2bf(rbf(gelu_t(g[i]))*u[i]); }
__global__ void fin(const u16* x,const float* y,const float* d,u16* out,int n){ int i=blockIdx.x*256+threadIdx.x; if(i<n){ float z=d[i]; float g=rbf(z/(1.f+expf(-z))); out[i]=f2bf(bf2f(x[i])+rbf(y[i]*g)); } }

extern "C" void kernel_launch(void* const* d_in, const int* in_sizes, int n_in, void* d_out, int out_size, void* d_ws, size_t ws_size, hipStream_t stream){
  const u16* x=(const u16*)d_in[0]; const u16* Cr=(const u16*)d_in[1]; const u16* Ci=(const u16*)d_in[2];
  const u16* Dr=(const u16*)d_in[3]; const u16* Di=(const u16*)d_in[4];
  const u16* Wg=(const u16*)d_in[6]; const u16* Wu=(const u16*)d_in[7]; const u16* Wd=(const u16*)d_in[8];
  char* ws=(char*)d_ws;
  u16* xn=(u16*)ws; ws+=S*D*2;
  float* af=(float*)ws; ws+=S*D*4;
  float* bf=(float*)ws; ws+=S*D*4;
  u16* a=(u16*)ws; ws+=S*D*2;
  u16* b=(u16*)ws; ws+=S*D*2;
  float* g=(float*)ws; ws+=S*F*4;
  float* u=(float*)ws; ws+=S*F*4;
  u16* h=(u16*)ws; ws+=S*F*2;
  float* y=(float*)ws; ws+=S*D*4;
  float* dn=(float*)ws; ws+=S*D*4;
  ln_k<<<S,64,0,stream>>>(x,xn);
  gemm_naive<<<dim3(7,S),64,0,stream>>>(xn,D,Dr,1,D,af,D,S,D,D,1.f,0);
  gemm_naive<<<dim3(7,S),64,0,stream>>>(xn,D,Di,1,D,bf,D,S,D,D,1.f,0);
  cvt<<<(S*D+255)/256,256,0,stream>>>(af,a,S*D);
  cvt<<<(S*D+255)/256,256,0,stream>>>(bf,b,S*D);
  gemm_naive<<<dim3(7,S),64,0,stream>>>(Cr,S,a,D,1,y,D,S,D,S,1.f,0);
  gemm_naive<<<dim3(7,S),64,0,stream>>>(Ci,S,b,D,1,y,D,S,D,S,-1.f,1);
  gemm_naive<<<dim3(28,S),64,0,stream>>>(xn,D,Wg,F,1,g,F,S,F,D,1.f,0);
  gemm_naive<<<dim3(28,S),64,0,stream>>>(xn,D,Wu,F,1,u,F,S,F,D,1.f,0);
  geglu<<<(S*F+255)/256,256,0,stream>>>(g,u,h,S*F);
  gemm_naive<<<dim3(7,S),64,0,stream>>>(h,F,Wd,D,1,dn,D,S,D,F,1.f,0);
  fin<<<(S*D+255)/256,256,0,stream>>>(x,y,dn,(u16*)d_out,S*D);
}
```

```cpp
#include <hip/hip_runtime.h>
#include <hip/hip_bf16.h>
#include <stdint.h>

typedef unsigned short u16;
typedef __bf16 bf16x8 __attribute__((ext_vector_type(8)));
typedef short s16x4 __attribute__((ext_vector_type(4)));
typedef short s16x8 __attribute__((ext_vector_type(8)));
typedef float f32x4 __attribute__((ext_vector_type(4)));
typedef unsigned u32x4 __attribute__((ext_vector_type(4)));
typedef unsigned u32x2 __attribute__((ext_vector_type(2)));
#define LAS __attribute__((address_space(3)))
typedef LAS char* lptr;
typedef __amdgpu_buffer_rsrc_t rsrc_t;

constexpr int S = 2048, D = 448, F = 1792;

constexpr size_t WS_FLAGS = 0;
constexpr size_t WS_XN    = 65536;
constexpr size_t WS_AB    = WS_XN + (size_t)S * D * 2;
constexpr size_t WS_H     = WS_AB + (size_t)2 * 28 * 64 * 1024;
constexpr size_t WS_G     = WS_H + (size_t)128 * 56 * 1024;

#define SBAR() __builtin_amdgcn_sched_barrier(0)
#define BAR() __builtin_amdgcn_s_barrier()
#define WAITV(n) asm volatile("s_waitcnt vmcnt(%0)" ::"n"(n) : "memory")
#define WAITL0() asm volatile("s_waitcnt lgkmcnt(0)" ::: "memory")

__device__ __forceinline__ rsrc_t mk_rsrc(const void* p, unsigned bytes) {
  return __builtin_amdgcn_make_buffer_rsrc((void*)p, 0, bytes, 0x00020000);
}
__device__ __forceinline__ void dma16(rsrc_t r, lptr dst, int voff, int soff) {
  __builtin_amdgcn_raw_ptr_buffer_load_lds(r, (LAS void*)dst, 16, voff, soff, 0, 0);
}
__device__ __forceinline__ bf16x8 ldsr128(lptr p) { return *(const LAS bf16x8*)p; }
__device__ __forceinline__ bf16x8 ldstr(lptr p0, lptr p1) {
  typedef short v4i16_t __attribute__((ext_vector_type(4)));
  v4i16_t lo = __builtin_amdgcn_ds_read_tr16_b64_v4i16((LAS v4i16_t*)p0);
  v4i16_t hi = __builtin_amdgcn_ds_read_tr16_b64_v4i16((LAS v4i16_t*)p1);
  s16x8 v = __builtin_shufflevector(lo, hi, 0, 1, 2, 3, 4, 5, 6, 7);
  return __builtin_bit_cast(bf16x8, v);
}
__device__ __forceinline__ f32x4 mfma(bf16x8 a, bf16x8 b, f32x4 c) { return __builtin_amdgcn_mfma_f32_16x16x32_bf16(a, b, c, 0, 0, 0); }
__device__ __forceinline__ unsigned pk_bf16(float lo, float hi) {
  typedef __bf16 bf16x2 __attribute__((ext_vector_type(2)));
  typedef float f32x2 __attribute__((ext_vector_type(2)));
  f32x2 v = {lo, hi};
  bf16x2 b = __builtin_convertvector(v, bf16x2);
  return __builtin_bit_cast(unsigned, b);
}
__device__ __forceinline__ float bf_lo(unsigned v) { return __uint_as_float(v << 16); }
__device__ __forceinline__ float bf_hi(unsigned v) { return __uint_as_float(v & 0xffff0000u); }
__device__ __forceinline__ float gelu_t(float x) {
  float p = __builtin_fmaf(x * x, -0.1029432397f, -2.302208198f);
  float e = __builtin_amdgcn_exp2f(x * p);
  return x * __builtin_amdgcn_rcpf(1.0f + e);
}
__device__ __forceinline__ float silu_f(float x) {
  float e = __builtin_amdgcn_exp2f(x * -1.4426950409f);
  return x * __builtin_amdgcn_rcpf(1.0f + e);
}

__global__ __launch_bounds__(512) void k0_ln(const u16* __restrict__ x, char* __restrict__ ws) {
  const int lane = threadIdx.x & 63, row = blockIdx.x * 8 + (threadIdx.x >> 6);
  if (blockIdx.x == 0 && threadIdx.x < 64) ((unsigned*)(ws + WS_FLAGS))[threadIdx.x * 64] = 0u;
  u32x4 v = {0, 0, 0, 0};
  if (lane < 56) v = *(const u32x4*)(x + (size_t)row * D + lane * 8);
  float f[8];
  for (int i = 0; i < 4; ++i) { f[2 * i] = bf_lo(v[i]); f[2 * i + 1] = bf_hi(v[i]); }
  float s = 0;
  for (int i = 0; i < 8; ++i) s += f[i];
  for (int o = 32; o > 0; o >>= 1) s += __shfl_xor(s, o);
  const float m = s * (1.0f / D);
  float q = 0;
  if (lane < 56) for (int i = 0; i < 8; ++i) { float d = f[i] - m; q += d * d; }
  for (int o = 32; o > 0; o >>= 1) q += __shfl_xor(q, o);
  const float r = __builtin_amdgcn_rsqf(q * (1.0f / D) + 1e-5f);
  u32x4 o4;
  for (int i = 0; i < 4; ++i) o4[i] = pk_bf16((f[2 * i] - m) * r, (f[2 * i + 1] - m) * r);
  if (lane < 56) *(u32x4*)(ws + WS_XN + ((size_t)row * D + lane * 8) * 2) = o4;
}

constexpr int K1_STAGE = 53248, K1_XO = 0, K1_GO = 16384, K1_UO = 30720, K1_DO = 45056, K1_NST = 7;

template <int NG, int NA>
__device__ __forceinline__ void k1_consumer(lptr lds, char* __restrict__ ws, int mt, int ng, int wm, int wn, int lane) {
  const int i16 = lane & 15, g = lane >> 4, q = i16 >> 2, p = i16 & 3;
  const int odd = ng & 1;
  const int rowx = 64 * wm + 8 * (i16 >> 2) + (i16 & 3);
  const int vx0 = K1_XO + rowx * 128 + ((g) ^ (i16 & 7)) * 16;
  const int vx1 = K1_XO + rowx * 128 + ((g + 4) ^ (i16 & 7)) * 16;
  const int wrow = (q ^ ((g & 1) << 2));
  const int vw0 = g * 1792 + wrow * 32 + p * 8;
  const int vw1 = g * 1792 + (wrow ^ 4) * 32 + p * 8;
  int nb[NG];
  if (NG == 4) { for (int j = 0; j < NG; ++j) nb[j] = odd + j; }
  else { nb[0] = 4 + odd; nb[1] = 5 + odd; nb[NG - 1] = odd ? 0 : 6; }
  const int au0 = (NA == 1) ? 0 : 1;
  const int vd0 = K1_DO + (16 * au0 + i16) * 128 + ((g) ^ (i16 & 7)) * 16;
  const int vd1 = K1_DO + (16 * au0 + i16) * 128 + ((g + 4) ^ (i16 & 7)) * 16;

  f32x4 G[NG][4], U[NG][4], AB[NA][4];
#pragma unroll
  for (int j = 0; j < NG; ++j)
#pragma unroll
    for (int t = 0; t < 4; ++t) { G[j][t] = f32x4{0, 0, 0, 0}; U[j][t] = f32x4{0, 0, 0, 0}; }
#pragma unroll
  for (int j = 0; j < NA; ++j)
#pragma unroll
    for (int t = 0; t < 4; ++t) AB[j][t] = f32x4{0, 0, 0, 0};

  bf16x8 X0[4], X1[4], WG[NG], WU[NG], WD[NA];
  auto ldx = [&](bf16x8 (&X)[4], lptr st, int ks) {
#pragma unroll
    for (int t = 0; t < 4; ++t) X[t] = ldsr128(st + (ks ? vx1 : vx0) + (32 * (t >> 1) + 4 * (t & 1)) * 128);
  };
  auto ldg = [&](int j, lptr st, int ks) { lptr b = st + K1_GO + nb[j] * 256 + ks * 4 * 1792; WG[j] = ldstr(b + vw0, b + vw1); };
  auto ldu = [&](int j, lptr st, int ks) { lptr b = st + K1_UO + nb[j] * 256 + ks * 4 * 1792; WU[j] = ldstr(b + vw0, b + vw1); };
  auto ldd = [&](int j, lptr st, int ks) { WD[j] = ldsr128(st + (ks ? vd1 : vd0) + j * 16 * 128); };

  BAR();
  {
    lptr st = lds;
    ldx(X0, st, 0);
#pragma unroll
    for (int j = 0; j < NG; ++j) { ldg(j, st, 0); ldu(j, st, 0); }
#pragma unroll
    for (int j = 0; j < NA; ++j) ldd(j, st, 0);
  }
  int slot = 0;
  for (int s = 0; s < K1_NST; ++s) {
    lptr st = lds + slot * K1_STAGE;
    slot = (slot == 2) ? 0 : slot + 1;
    lptr sn = lds + slot * K1_STAGE;
    ldx(X1, st, 1);
    SBAR();
#pragma unroll
    for (int j = 0; j < NG; ++j) {
#pragma unroll
      for (int t = 0; t < 4; ++t) G[j][t] = mfma(WG[j], X0[t], G[j][t]);
      ldg(j, st, 1);
      SBAR();
#pragma unroll
      for (int t = 0; t < 4; ++t) U[j][t] = mfma(WU[j], X0[t], U[j][t]);
      ldu(j, st, 1);
      SBAR();
    }
#pragma unroll
    for (int j = 0; j < NA; ++j) {
#pragma unroll
      for (int t = 0; t < 4; ++t) AB[j][t] = mfma(X0[t], WD[j], AB[j][t]);
      ldd(j, st, 1);
      SBAR();
    }
#pragma unroll
    for (int t = 0; t < 4; ++t) G[0][t] = mfma(WG[0], X1[t], G[0][t]);
#pragma unroll
    for (int t = 0; t < 4; ++t) U[0][t] = mfma(WU[0], X1[t], U[0][t]);
    SBAR();
    WAITL0();
    BAR();
    SBAR();
    ldg(0, sn, 0); ldu(0, sn, 0);
    ldx(X0, sn, 0);
    SBAR();
#pragma unroll
    for (int j = 1; j < NG; ++j) {
#pragma unroll
      for (int t = 0; t < 4; ++t) G[j][t] = mfma(WG[j], X1[t], G[j][t]);
      ldg(j, sn, 0);
      SBAR();
#pragma unroll
      for (int t = 0; t < 4; ++t) U[j][t] = mfma(WU[j], X1[t], U[j][t]);
      ldu(j, sn, 0);
      SBAR();
    }
#pragma unroll
    for (int j = 0; j < NA; ++j) {
#pragma unroll
      for (int t = 0; t < 4; ++t) AB[j][t] = mfma(X1[t], WD[j], AB[j][t]);
      ldd(j, sn, 0);
      SBAR();
    }
  }
  char* hbase = ws + WS_H;
  const int tt0 = mt * 8 + wm * 4;
  auto hval = [&](int j, int t, unsigned& lo, unsigned& hi) {
    float h0 = gelu_t(G[j][t][0]) * U[j][t][0], h1 = gelu_t(G[j][t][1]) * U[j][t][1];
    float h2 = gelu_t(G[j][t][2]) * U[j][t][2], h3 = gelu_t(G[j][t][3]) * U[j][t][3];
    lo = pk_bf16(h0, h1); hi = pk_bf16(h2, h3);
  };
  {
    constexpr int NP = NG / 2;
#pragma unroll
    for (int pp = 0; pp < NP; ++pp) {
      const int ks = (7 * ng + nb[2 * pp]) >> 1;
#pragma unroll
      for (int t = 0; t < 4; ++t) {
        unsigned o0, o1, o2, o3;
        hval(2 * pp, t, o0, o1); hval(2 * pp + 1, t, o2, o3);
        *(u32x4*)(hbase + ((size_t)(tt0 + t) * 56 + ks) * 1024 + lane * 16) = u32x4{o0, o1, o2, o3};
      }
    }
    if (NG & 1) {
      const int U_ = 7 * ng + nb[NG - 1];
      const int ks = U_ >> 1, half = U_ & 1;
#pragma unroll
      for (int t = 0; t < 4; ++t) {
        unsigned o0, o1;
        hval(NG - 1, t, o0, o1);
        *(u32x2*)(hbase + ((size_t)(tt0 + t) * 56 + ks) * 1024 + lane * 16 + half * 8) = u32x2{o0, o1};
      }
    }
  }
  if (ng < 14) {
    const int part = ng >= 7 ? 1 : 0;
    const int u0 = (ng - 7 * part) * 4 + au0;
    char* abase = ws + WS_AB;
#pragma unroll
    for (int j = 0; j < NA; ++j)
#pragma unroll
      for (int pi = 0; pi < 2; ++pi) {
        const f32x4 a = AB[j][2 * pi], b = AB[j][2 * pi + 1];
        const int ks = mt * 4 + wm * 2 + pi;
        *(u32x4*)(abase + ((size_t)(part * 28 + u0 + j) * 64 + ks) * 1024 + lane * 16) =
            u32x4{pk_bf16(a[0], a[1]), pk_bf16(a[2], a[3]), pk_bf16(b[0], b[1]), pk_bf16(b[2], b[3])};
      }
  }
}

__global__ __launch_bounds__(512, 2) void k1_gemm(const u16* __restrict__ Wg, const u16* __restrict__ Wu, const u16* __restrict__ Dr,
                                                  const u16* __restrict__ Di, char* __restrict__ ws) {
  extern __shared__ __attribute__((aligned(1024))) char smem[];
  lptr lds = (lptr)smem;
  const int lane = threadIdx.x & 63;
  const int wid = __builtin_amdgcn_readfirstlane(threadIdx.x >> 6);
  const int xcd = blockIdx.x & 7, idx = blockIdx.x >> 3;
  const int mt = (xcd & 1) * 8 + (idx & 7), ng = (xcd >> 1) * 4 + (idx >> 3);
  if (wid >= 4) {
    const int lw = wid - 4;
    const rsrc_t rx = mk_rsrc(ws + WS_XN, S * D * 2);
    const rsrc_t rw = mk_rsrc(lw < 2 ? Wg : Wu, D * F * 2);
    const int ngd = ng < 14 ? ng : 13;
    const rsrc_t rd = mk_rsrc(ngd >= 7 ? Di : Dr, D * D * 2);
    const int e0 = (ngd >= 7 ? ngd - 7 : ngd) * 64;
    const int r8 = lane >> 3, c8 = lane & 7;
    int vx[2];
    vx[0] = r8 * 896 + (c8 ^ (r8 & 3)) * 16;
    vx[1] = r8 * 896 + (c8 ^ ((r8 & 3) | 4)) * 16;
    const int vd = r8 * 896 + (c8 ^ r8) * 16;
    int vw[7];
#pragma unroll
    for (int j = 0; j < 7; ++j) {
      const int B = 4 * (7 * (lw & 1) + j) + (lane >> 4);
      const int kb = B / 7, nbk = B % 7, rs = (lane & 15) >> 1, half = lane & 1;
      const int k = 8 * kb + (rs ^ ((kb & 1) << 2));
      vw[j] = k * (F * 2) + (ng * 112 + 16 * nbk + 8 * half) * 2;
    }
    const int wdst = (lw < 2 ? K1_GO : K1_UO) + 7 * (lw & 1) * 1024;
    auto issue = [&](int s, int slot) {
      lptr st = lds + slot * K1_STAGE;
#pragma unroll
      for (int i = 0; i < 4; ++i) { const int rb = 4 * lw + i; dma16(rx, st + K1_XO + rb * 1024, vx[i & 1], (mt * 128 + 8 * rb) * 896 + s * 128); }
#pragma unroll
      for (int j = 0; j < 7; ++j) dma16(rw, st + wdst + j * 1024, vw[j], s * 64 * (F * 2));
#pragma unroll
      for (int i = 0; i < 2; ++i) { const int rb = 2 * lw + i; dma16(rd, st + K1_DO + rb * 1024, vd, (e0 + 8 * rb) * 896 + s * 128); }
    };
    issue(0, 0); issue(1, 1); issue(2, 2);
    WAITV(26); BAR();
    WAITV(13); BAR();
    int slot = 0;
    for (int s = 1; s < K1_NST; ++s) {
      if (s + 2 < K1_NST) { issue(s + 2, slot); WAITV(13); } else { WAITV(0); }
      slot = (slot == 2) ? 0 : slot + 1;
      BAR();
    }
  } else {
    const int wm = wid & 1, wn = wid >> 1;
    if (wn == 0) k1_consumer<4, 1>(lds, ws, mt, ng, wm, 0, lane);
    else k1_consumer<3, 3>(lds, ws, mt, ng, wm, 1, lane);
  }
}

constexpr int Y_STAGE = 36864, Y_NSLOT = 4, Y_NST = 32;
constexpr int D_STAGE = 30720, D_NSLOT = 5, D_NST = 28;
constexpr int Y_RS = 84, D_RS = 116;

template <int NU>
__device__ __forceinline__ void y_loader(lptr lds, const u16* Cr, const u16* Ci, char* ws, int t0, int u0, int lw, int lane) {
  const rsrc_t rc0 = mk_rsrc(Cr, S * S * 2), rc1 = mk_rsrc(Ci, S * S * 2);
  const rsrc_t rb = mk_rsrc(ws + WS_AB, 2 * 28 * 64 * 1024);
  const int r8 = lane >> 3, c8 = lane & 7;
  const int va = r8 * 4096 + (c8 ^ r8) * 16;
  const int vb = lane * 16;
  constexpr int PER = 4 + NU;
  auto issue = [&](int s, int slot) {
    lptr st = lds + slot * Y_STAGE;
    const int part = s >> 4, sk = s & 15;
    const rsrc_t rc = part ? rc1 : rc0;
#pragma unroll
    for (int i = 0; i < 2; ++i)
#pragma unroll
      for (int kh = 0; kh < 2; ++kh) { const int rbk = 2 * lw + i; dma16(rc, st + kh * 8192 + rbk * 1024, va, (t0 + 8 * rbk) * 4096 + sk * 256 + kh * 128); }
#pragma unroll
    for (int u = 0; u < NU; ++u) dma16(rb, st + 16384 + u * 4096 + lw * 1024, vb, ((part * 28 + u0 + u) * 64 + sk * 4 + lw) * 1024);
  };
  issue(0, 0); issue(1, 1); issue(2, 2); issue(3, 3);
  WAITV(3 * PER); BAR();
  WAITV(2 * PER); BAR();
  int slot = 0;
  for (int s = 1; s < Y_NST; ++s) {
    if (s + 3 < Y_NST) { issue(s + 3, slot); WAITV(2 * PER); }
    else if (s + 2 < Y_NST) { WAITV(PER); } else { WAITV(0); }
    slot = (slot + 1) & 3;
    BAR();
  }
}

template <int NU>
__device__ __forceinline__ void y_consumer(lptr lds, int w, int lane) {
  const int i16 = lane & 15, g = lane >> 4;
  const int va = (w >> 1) * 8192 + i16 * 128 + ((g + 4 * (w & 1)) ^ (i16 & 7)) * 16;
  const int vb = 16384 + w * 1024 + lane * 16;
  f32x4 acc[NU][4];
#pragma unroll
  for (int u = 0; u < NU; ++u)
#pragma unroll
    for (int t = 0; t < 4; ++t) acc[u][t] = f32x4{0, 0, 0, 0};
  bf16x8 A0[4], A1[4], B[NU];
  BAR();
#pragma unroll
  for (int t = 0; t < 4; ++t) A0[t] = ldsr128(lds + va + t * 2048);
#pragma unroll
  for (int u = 0; u < NU; ++u) B[u] = ldsr128(lds + vb + u * 4096);
  int slot = 0;
  auto step = [&](bf16x8 (&Ac)[4], bf16x8 (&An)[4]) {
    WAITL0();
    BAR();
    slot = (slot + 1) & 3;
    lptr sn = lds + slot * Y_STAGE;
    SBAR();
#pragma unroll
    for (int t = 0; t < 4; ++t) An[t] = ldsr128(sn + va + t * 2048);
    SBAR();
#pragma unroll
    for (int u = 0; u < NU; ++u) {
#pragma unroll
      for (int t = 0; t < 4; ++t) acc[u][t] = mfma(B[u], Ac[t], acc[u][t]);
      B[u] = ldsr128(sn + vb + u * 4096);
      SBAR();
    }
  };
  for (int s = 0; s < Y_NST; s += 2) {
    if (s == 16) {
#pragma unroll
      for (int u = 0; u < NU; ++u)
#pragma unroll
        for (int t = 0; t < 4; ++t) acc[u][t] = -acc[u][t];
    }
    step(A0, A1);
    step(A1, A0);
  }
  WAITL0();
  BAR();
  lptr pb = lds + w * (64 * Y_RS * 4);
#pragma unroll
  for (int u = 0; u < NU; ++u)
#pragma unroll
    for (int t = 0; t < 4; ++t) *(LAS f32x4*)(pb + ((16 * t + i16) * Y_RS + 16 * u + 4 * g) * 4) = acc[u][t];
}

template <int PER>
__device__ __forceinline__ void d_loader(lptr lds, const u16* Wd, char* ws, int mtD, int n0, int lw, int lane) {
  const rsrc_t rh = mk_rsrc(ws + WS_H, 128 * 56 * 1024);
  const rsrc_t rw = mk_rsrc(Wd, F * D * 2);
  const int vh = lane * 16;
  constexpr int NB = PER - 4;
  const int j0 = (lw < 2) ? 4 * lw : 8 + 3 * (lw - 2);
  int vw[NB];
#pragma unroll
  for (int j = 0; j < NB; ++j) {
    const int B = 4 * (j0 + j) + (lane >> 4);
    const int kb = B / 7, nbk = B % 7, rs = (lane & 15) >> 1, half = lane & 1;
    vw[j] = (8 * kb + rs) * 896 + (n0 + 16 * nbk + 8 * half) * 2;
  }
  auto issue = [&](int s, int slot) {
    lptr st = lds + slot * D_STAGE;
#pragma unroll
    for (int i = 0; i < 2; ++i)
#pragma unroll
      for (int k = 0; k < 2; ++k) { const int tt = 2 * lw + i; dma16(rh, st + (tt * 2 + k) * 1024, vh, ((mtD * 8 + tt) * 56 + 2 * s + k) * 1024); }
#pragma unroll
    for (int j = 0; j < NB; ++j) dma16(rw, st + 16384 + (j0 + j) * 1024, vw[j], s * 64 * 896);
  };
  issue(0, 0); issue(1, 1); issue(2, 2); issue(3, 3); issue(4, 4);
  WAITV(4 * PER); BAR();
  WAITV(3 * PER); BAR();
  int slot = 0;
  for (int s = 1; s < D_NST; ++s) {
    if (s + 4 < D_NST) { issue(s + 4, slot); WAITV(3 * PER); }
    else if (s + 3 < D_NST) { WAITV(2 * PER); } else if (s + 2 < D_NST) { WAITV(PER); } else { WAITV(0); }
    slot = (slot == 4) ? 0 : slot + 1;
    BAR();
  }
}

__device__ __forceinline__ void d_consumer(lptr lds, int w, int lane) {
  const int i16 = lane & 15, g = lane >> 4, q = i16 >> 2, p = i16 & 3;
  const int wm = w & 1, wk = w >> 1;
  const int va = (wm * 4 * 2 + wk) * 1024 + lane * 16;
  const int vb = 16384 + wk * 4 * 1792 + (g >> 1) * 1792 + ((g & 1) * 4 + q) * 32 + p * 8;
  f32x4 acc[7][4];
#pragma unroll
  for (int u = 0; u < 7; ++u)
#pragma unroll
    for (int t = 0; t < 4; ++t) acc[u][t] = f32x4{0, 0, 0, 0};
  bf16x8 A0[4], A1[4], B[7];
  BAR();
#pragma unroll
  for (int t = 0; t < 4; ++t) A0[t] = ldsr128(lds + va + t * 2048);
#pragma unroll
  for (int u = 0; u < 7; ++u) B[u] = ldstr(lds + vb + u * 256, lds + vb + u * 256 + 2 * 1792);
  int slot = 0;
  auto step = [&](bf16x8 (&Ac)[4], bf16x8 (&An)[4]) {
    WAITL0();
    BAR();
    slot = (slot == 4) ? 0 : slot + 1;
    lptr sn = lds + slot * D_STAGE;
    SBAR();
#pragma unroll
    for (int t = 0; t < 4; ++t) An[t] = ldsr128(sn + va + t * 2048);
    SBAR();
#pragma unroll
    for (int u = 0; u < 7; ++u) {
#pragma unroll
      for (int t = 0; t < 4; ++t) acc[u][t] = mfma(B[u], Ac[t], acc[u][t]);
      B[u] = ldstr(sn + vb + u * 256, sn + vb + u * 256 + 2 * 1792);
      SBAR();
    }
  };
  for (int s = 0; s < D_NST; s += 2) { step(A0, A1); step(A1, A0); }
  WAITL0();
  BAR();
  lptr pb = lds + wk * (128 * D_RS * 4);
#pragma unroll
  for (int u = 0; u < 7; ++u)
#pragma unroll
    for (int t = 0; t < 4; ++t) *(LAS f32x4*)(pb + ((64 * wm + 16 * t + i16) * D_RS + 16 * u + 4 * g) * 4) = acc[u][t];
}

__global__ __launch_bounds__(512, 2) void k2_main(const u16* __restrict__ x, const u16* __restrict__ Cr, const u16* __restrict__ Ci,
                                                  const u16* __restrict__ Wd, char* __restrict__ ws, u16* __restrict__ out) {
  extern __shared__ __attribute__((aligned(1024))) char smem[];
  lptr lds = (lptr)smem;
  const int tid = threadIdx.x, lane = tid & 63;
  const int wid = __builtin_amdgcn_readfirstlane(tid >> 6);
  const int xcd = blockIdx.x & 7, idx = blockIdx.x >> 3;
  unsigned* flags = (unsigned*)(ws + WS_FLAGS);
  if (idx < 8) {
    const int mtD = 2 * xcd + (idx >> 2), ntD = idx & 3, n0 = ntD * 112;
    if (wid >= 4) {
      if (wid < 6) d_loader<8>(lds, Wd, ws, mtD, n0, wid - 4, lane);
      else d_loader<7>(lds, Wd, ws, mtD, n0, wid - 4, lane);
      BAR();
    } else d_consumer(lds, wid, lane);
    WAITL0();
    BAR();
    const rsrc_t rg = mk_rsrc(ws + WS_G + (size_t)(mtD * 4 + ntD) * (128 * 112 * 2), 128 * 112 * 2);
#pragma unroll
    for (int it = 0; it < 4; ++it) {
      const int item = it * 512 + tid;
      if (item < 128 * 14) {
        const int r = item / 14, c = item % 14;
        lptr p0 = lds + (r * D_RS + 8 * c) * 4;
        const f32x4 a0 = *(LAS f32x4*)p0, a1 = *(LAS f32x4*)(p0 + 16);
        const f32x4 b0 = *(LAS f32x4*)(p0 + 128 * D_RS * 4), b1 = *(LAS f32x4*)(p0 + 128 * D_RS * 4 + 16);
        const f32x4 d0 = a0 + b0, d1 = a1 + b1;
        u32x4 o = {pk_bf16(silu_f(d0[0]), silu_f(d0[1])), pk_bf16(silu_f(d0[2]), silu_f(d0[3])),
                   pk_bf16(silu_f(d1[0]), silu_f(d1[1])), pk_bf16(silu_f(d1[2]), silu_f(d1[3]))};
        const int tt = r >> 4, i = r & 15;
        const int tok = 32 * (tt >> 1) + 8 * (i >> 2) + 4 * (tt & 1) + (i & 3);
        __builtin_amdgcn_raw_buffer_store_b128(o, rg, (tok * 112 + 8 * c) * 2, 0, 16);
      }
    }
    WAITV(0);
    __syncthreads();
    if (tid == 0) __hip_atomic_store(flags + (mtD * 4 + ntD) * 64, 1u, __ATOMIC_RELAXED, __HIP_MEMORY_SCOPE_AGENT);
  } else {
    const int j = idx - 8;
    const int mt = 4 * xcd + j / 6, nt = j % 6;
    const int t0 = mt * 64;
    const int u0 = nt < 4 ? 5 * nt : 20 + 4 * (nt - 4);
    const int nu = nt < 4 ? 5 : 4;
    if (wid >= 4) {
      if (nu == 5) y_loader<5>(lds, Cr, Ci, ws, t0, u0, wid - 4, lane);
      else y_loader<4>(lds, Cr, Ci, ws, t0, u0, wid - 4, lane);
      BAR();
    } else {
      if (nu == 5) y_consumer<5>(lds, wid, lane);
      else y_consumer<4>(lds, wid, lane);
    }
    const int e0 = 16 * u0, mtD = mt >> 1;
    const int nA = e0 / 112, nB = (e0 + 16 * nu - 1) / 112;
    if (tid == 64 * 4) {
      unsigned sp = 0;
      while (__hip_atomic_load(flags + (mtD * 4 + nA) * 64, __ATOMIC_RELAXED, __HIP_MEMORY_SCOPE_AGENT) == 0u ||
             __hip_atomic_load(flags + (mtD * 4 + nB) * 64, __ATOMIC_RELAXED, __HIP_MEMORY_SCOPE_AGENT) == 0u) {
        __builtin_amdgcn_s_sleep(2);
        if (++sp > (1u << 20)) break;
      }
    }
    WAITL0();
    __syncthreads();
    const rsrc_t rg = mk_rsrc(ws + WS_G, 16 * 4 * 128 * 112 * 2);
    const int ngrp = 2 * nu;
    for (int item = tid; item < 64 * ngrp; item += 512) {
      const int r = item / ngrp, c = item % ngrp;
      const int t = t0 + r, e = e0 + 8 * c;
      const int nD = e / 112, eo = e - nD * 112;
      const u32x4 gv = __builtin_amdgcn_raw_buffer_load_b128(rg, ((mtD * 4 + nD) * 128 * 112 + (t - mtD * 128) * 112 + eo) * 2, 0, 16);
      const u32x4 xv = *(const u32x4*)(x + (size_t)t * D + e);
      f32x4 s0 = {0, 0, 0, 0}, s1 = {0, 0, 0, 0};
#pragma unroll
      for (int w = 0; w < 4; ++w) {
        lptr p0 = lds + w * (64 * Y_RS * 4) + (r * Y_RS + 8 * c) * 4;
        s0 += *(LAS f32x4*)p0; s1 += *(LAS f32x4*)(p0 + 16);
      }
      u32x4 o;
      o[0] = pk_bf16(bf_lo(xv[0]) - s0[0] * bf_lo(gv[0]), bf_hi(xv[0]) - s0[1] * bf_hi(gv[0]));
      o[1] = pk_bf16(bf_lo(xv[1]) - s0[2] * bf_lo(gv[1]), bf_hi(xv[1]) - s0[3] * bf_hi(gv[1]));
      o[2] = pk_bf16(bf_lo(xv[2]) - s1[0] * bf_lo(gv[2]), bf_hi(xv[2]) - s1[1] * bf_hi(gv[2]));
      o[3] = pk_bf16(bf_lo(xv[3]) - s1[2] * bf_lo(gv[3]), bf_hi(xv[3]) - s1[3] * bf_hi(gv[3]));
      *(u32x4*)(out + (size_t)t * D + e) = o;
    }
  }
}

extern "C" void kernel_launch(void* const* d_in, const int* in_sizes, int n_in, void* d_out, int out_size, void* d_ws, size_t ws_size,
                              hipStream_t stream) {
  const u16* x = (const u16*)d_in[0];
  const u16* Cr = (const u16*)d_in[1];
  const u16* Ci = (const u16*)d_in[2];
  const u16* Dr = (const u16*)d_in[3];
  const u16* Di = (const u16*)d_in[4];
  const u16* Wg = (const u16*)d_in[6];
  const u16* Wu = (const u16*)d_in[7];
  const u16* Wd = (const u16*)d_in[8];
  char* ws = (char*)d_ws;
  constexpr int LDS1 = 3 * K1_STAGE;
  constexpr int LDS2 = 5 * D_STAGE > 4 * Y_STAGE ? 5 * D_STAGE : 4 * Y_STAGE;
  hipFuncSetAttribute((const void*)k1_gemm, hipFuncAttributeMaxDynamicSharedMemorySize, LDS1);
  hipFuncSetAttribute((const void*)k2_main, hipFuncAttributeMaxDynamicSharedMemorySize, LDS2);
  k0_ln<<<S / 8, 512, 0, stream>>>(x, ws);
  k1_gemm<<<256, 512, LDS1, stream>>>(Wg, Wu, Dr, Di, ws);
  k2_main<<<256, 512, LDS2, stream>>>(x, Cr, Ci, Wd, ws, (u16*)d_out);
}
```

```cpp
#include <hip/hip_runtime.h>
#include <hip/hip_bf16.h>
#include <stdint.h>

typedef unsigned short u16;
typedef __bf16 bf16x8 __attribute__((ext_vector_type(8)));
typedef short s16x8 __attribute__((ext_vector_type(8)));
typedef float f32x4 __attribute__((ext_vector_type(4)));
typedef float f32x2 __attribute__((ext_vector_type(2)));
typedef unsigned u32x4 __attribute__((ext_vector_type(4)));
typedef unsigned u32x2 __attribute__((ext_vector_type(2)));
#define LAS __attribute__((address_space(3)))
#define GAS __attribute__((address_space(1)))
typedef LAS char* lptr;
typedef __amdgpu_buffer_rsrc_t rsrc_t;

constexpr int S = 2048, D = 448, F = 1792;

constexpr size_t WS_NONCE  = 0;
constexpr size_t WS_ARRIVE = 4096;
constexpr size_t WS_FAB    = 32768;
constexpr size_t WS_FH     = 49152;
constexpr size_t WS_FWD    = 65536;
constexpr size_t WS_FY     = 81920;
constexpr size_t WS_FD     = 147456;
constexpr size_t WS_ST     = 212992;
constexpr size_t WS_AB     = 262144;
constexpr size_t WS_H      = WS_AB + (size_t)2 * 28 * 64 * 1024;
constexpr size_t WS_WDT    = WS_H + (size_t)128 * 56 * 1024;
constexpr size_t WS_PY     = WS_WDT + (size_t)28 * 56 * 1024;
constexpr size_t WS_PD     = WS_PY + (size_t)64 * 16 * 7 * 1024;

#define STAMP(i)
#define STAMPR(i)
#define SBAR() __builtin_amdgcn_sched_barrier(0)
#define BAR() __builtin_amdgcn_s_barrier()
#define WAITV(n) asm volatile("s_waitcnt vmcnt(%0)" ::"n"(n) : "memory")
#define WAITL0() __builtin_amdgcn_s_waitcnt(0xC07F)
#define PF0 2
#define STAUX 16
#define SPIN_MAX (1u << 18)

__device__ __forceinline__ rsrc_t mk_rsrc(const void* p, unsigned bytes) {
  return __builtin_amdgcn_make_buffer_rsrc((void*)p, 0, bytes, 0x00020000);
}
__device__ __forceinline__ u32x4 mk_srd(const void* p, unsigned bytes) {
  const unsigned long long a = (unsigned long long)p;
  return u32x4{(unsigned)a, (unsigned)(a >> 32) & 0xffffu, bytes, 0x00020000u};
}
__device__ __forceinline__ unsigned lds_addr(lptr p) { return (unsigned)(size_t)p; }
__device__ __forceinline__ void dma16(const u32x4& srd, unsigned dst, int voff, int soff) {
  asm volatile("s_mov_b32 m0, %0\n\ts_nop 0\n\tbuffer_load_dwordx4 %1, %2, %3 offen lds" ::"s"(dst), "v"(voff), "s"(srd), "s"(soff) : "memory");
}
__device__ __forceinline__ void pf4(const char* p, unsigned dst) {
  asm volatile("s_mov_b32 m0, %0\n\ts_nop 0\n\tglobal_load_lds_dword %1, off" ::"s"(dst), "v"(p) : "memory");
}
__device__ __forceinline__ bf16x8 ldsr128(lptr p) { return *(const LAS bf16x8*)p; }
__device__ __forceinline__ bf16x8 ldstr(lptr p0, lptr p1) {
  typedef short v4i16_t __attribute__((ext_vector_type(4)));
  v4i16_t lo = __builtin_amdgcn_ds_read_tr16_b64_v4i16((LAS v4i16_t*)p0);
  v4i16_t hi = __builtin_amdgcn_ds_read_tr16_b64_v4i16((LAS v4i16_t*)p1);
  s16x8 v = __builtin_shufflevector(lo, hi, 0, 1, 2, 3, 4, 5, 6, 7);
  return __builtin_bit_cast(bf16x8, v);
}
__device__ __forceinline__ f32x4 mfma(bf16x8 a, bf16x8 b, f32x4 c) { return __builtin_amdgcn_mfma_f32_16x16x32_bf16(a, b, c, 0, 0, 0); }
__device__ __forceinline__ void mfma_ip(f32x4& c, const bf16x8& a, const bf16x8& b) {
  asm volatile("v_mfma_f32_16x16x32_bf16 %0, %1, %2, %0" : "+v"(c) : "v"(a), "v"(b));
}
#define FILL() asm volatile("s_nop 0")
#define PAD_MFMA_OUT() asm volatile("s_nop 7\n\ts_nop 3")
#define PAD_MFMA_IN() asm volatile("s_nop 3")
__device__ __forceinline__ unsigned pk_bf16(float lo, float hi) {
  typedef __bf16 bf16x2 __attribute__((ext_vector_type(2)));
  f32x2 v = {lo, hi};
  bf16x2 b = __builtin_convertvector(v, bf16x2);
  return __builtin_bit_cast(unsigned, b);
}
__device__ __forceinline__ float bf_lo(unsigned v) { return __uint_as_float(v << 16); }
__device__ __forceinline__ float bf_hi(unsigned v) { return __uint_as_float(v & 0xffff0000u); }
__device__ __forceinline__ float rbf(float f) { return bf_lo(pk_bf16(f, 0.f)); }
__device__ __forceinline__ float silu_f(float x) {
  float e = __builtin_amdgcn_exp2f(x * -1.4426950409f);
  return x * __builtin_amdgcn_rcpf(1.0f + e);
}
__device__ __forceinline__ float wave_sum(float v) {
  v += __builtin_bit_cast(float, __builtin_amdgcn_update_dpp(0, __builtin_bit_cast(int, v), 0x111, 0xf, 0xf, true));
  v += __builtin_bit_cast(float, __builtin_amdgcn_update_dpp(0, __builtin_bit_cast(int, v), 0x112, 0xf, 0xf, true));
  v += __builtin_bit_cast(float, __builtin_amdgcn_update_dpp(0, __builtin_bit_cast(int, v), 0x114, 0xf, 0xf, true));
  v += __builtin_bit_cast(float, __builtin_amdgcn_update_dpp(0, __builtin_bit_cast(int, v), 0x118, 0xf, 0xf, true));
  v += __builtin_bit_cast(float, __builtin_amdgcn_update_dpp(0, __builtin_bit_cast(int, v), 0x142, 0xa, 0xf, true));
  v += __builtin_bit_cast(float, __builtin_amdgcn_update_dpp(0, __builtin_bit_cast(int, v), 0x143, 0xc, 0xf, true));
  return __builtin_bit_cast(float, __builtin_amdgcn_readlane(__builtin_bit_cast(int, v), 63));
}
__device__ __forceinline__ unsigned ld_flag(const void* p) { return __hip_atomic_load((const GAS unsigned*)p, __ATOMIC_RELAXED, __HIP_MEMORY_SCOPE_AGENT); }
__device__ __forceinline__ void st_flag(void* p, unsigned v) { __hip_atomic_store((GAS unsigned*)p, v, __ATOMIC_RELAXED, __HIP_MEMORY_SCOPE_AGENT); }
__device__ __forceinline__ unsigned get_tag(lptr lds);
__device__ __forceinline__ void poll_all(const char* p, bool active, unsigned tag) {
  for (unsigned sp = 0; sp < SPIN_MAX; ++sp) {
    const unsigned v = active ? ld_flag(p) : tag;
    if (__all(v == tag)) break;
    __builtin_amdgcn_s_sleep(2);
  }
}
__device__ __forceinline__ void lds_spin(lptr p) {
  for (unsigned sp = 0; sp < SPIN_MAX * 4; ++sp) {
    if (*(volatile LAS unsigned*)p != 0u) break;
    __builtin_amdgcn_s_sleep(2);
  }
}

constexpr int K1_STAGE = 53248, K1_NST = 7, K1_WO = 16384;
constexpr int L_MISC = 3 * K1_STAGE;
constexpr int L_STATS = L_MISC;
constexpr int L_CS = L_MISC + 1024;
constexpr int L_WORDS = L_CS + 18 * 64;
constexpr int L_DUMMY = L_WORDS + 128;
constexpr int L_TOTAL = L_DUMMY + 512;
#define YREP 1
constexpr int P_STAGE = 30720, P_NY = 16 * YREP, P_ND = 7, P_NST = P_NY + P_ND;
static_assert(L_TOTAL <= 163840 && 5 * P_STAGE <= L_MISC, "LDS");

__device__ __forceinline__ unsigned get_tag(lptr lds) { return *(volatile LAS unsigned*)(lds + L_WORDS + 16); }

struct Args {
  const u16 *x, *Cr, *Ci, *Dr, *Di, *Wg, *Wu, *Wd;
  char* ws;
  u16* out;
};

template <int MODE, int NG, int NA>
__device__ __forceinline__ void p1_consumer(lptr lds, char* __restrict__ ws, int mt, int grp, int f0, int a0, int wm, int wn, int lane, const char* cp) {
  constexpr int NBK = 2 * NG, NCQ = NBK / 4, WSZ = NBK * 8 * 256, KSO = 4 * NCQ * 1024;
  constexpr int GO = K1_WO, UO = K1_WO + WSZ, DO = K1_WO + 2 * WSZ;
  const int i16 = lane & 15, g = lane >> 4, q = i16 >> 2, p = i16 & 3;
  const int rowx = 64 * wm + 8 * (i16 >> 2) + (i16 & 3);
  const int vx0 = rowx * 128 + ((g) ^ (i16 & 7)) * 16;
  const int vx1 = rowx * 128 + ((g + 4) ^ (i16 & 7)) * 16;
  const int fw = ((q >> 1) & 1) | ((g & 1) << 1);
  int vw[NG];
#pragma unroll
  for (int j = 0; j < NG; ++j) { const int nb = NG * wn + j; vw[j] = g * NCQ * 1024 + (nb >> 2) * 1024 + q * 128 + (((nb & 3) ^ fw) * 32) + p * 8; }
  const int vd0 = DO + (16 * NA * wn + i16) * 128 + ((g) ^ (i16 & 7)) * 16;
  const int vd1 = DO + (16 * NA * wn + i16) * 128 + ((g + 4) ^ (i16 & 7)) * 16;

  f32x4 G[NG][4], U[NG][4], AB[NA][4];
#pragma unroll
  for (int j = 0; j < NG; ++j)
#pragma unroll
    for (int t = 0; t < 4; ++t) { G[j][t] = f32x4{0, 0, 0, 0}; U[j][t] = f32x4{0, 0, 0, 0}; }
#pragma unroll
  for (int j = 0; j < NA; ++j)
#pragma unroll
    for (int t = 0; t < 4; ++t) AB[j][t] = f32x4{0, 0, 0, 0};

  bf16x8 X0[4], X1[4], WG[NG], WU[NG], WD[NA];
  auto ldx = [&](bf16x8 (&X)[4], lptr st, int ks) {
#pragma unroll
    for (int t = 0; t < 4; ++t) X[t] = ldsr128(st + (ks ? vx1 : vx0) + (32 * (t >> 1) + 4 * (t & 1)) * 128);
  };
  auto ldg = [&](int j, lptr st, int ks) { lptr b = st + GO + ks * KSO + vw[j]; WG[j] = ldstr(b, b + 512); };
  auto ldu = [&](int j, lptr st, int ks) { lptr b = st + UO + ks * KSO + vw[j]; WU[j] = ldstr(b, b + 512); };
  auto ldd = [&](int j, lptr st, int ks) { WD[j] = ldsr128(st + (ks ? vd1 : vd0) + j * 16 * 128); };
  auto grp4 = [&](f32x4 (&C)[4], const bf16x8& W, bf16x8 (&X)[4], bool wfirst, auto mid) {
    if (wfirst) { mfma_ip(C[0], W, X[0]); mfma_ip(C[1], W, X[1]); } else { mfma_ip(C[0], X[0], W); mfma_ip(C[1], X[1], W); }
    SBAR(); mid(); SBAR();
    if (wfirst) { mfma_ip(C[2], W, X[2]); mfma_ip(C[3], W, X[3]); } else { mfma_ip(C[2], X[2], W); mfma_ip(C[3], X[3], W); }
    SBAR();
  };
  auto fill = [&]() { FILL(); };
  auto ldx1 = [&](bf16x8 (&X)[4], lptr st, int ks, int t) { X[t] = ldsr128(st + (ks ? vx1 : vx0) + (32 * (t >> 1) + 4 * (t & 1)) * 128); };
  auto blockA = [&](lptr st) {
#pragma unroll
    for (int j = 0; j < NG; ++j) {
      grp4(G[j], WG[j], X0, true, [&]() { if (2 * j < 4) ldx1(X1, st, 1, 2 * j); else FILL(); }); ldg(j, st, 1); SBAR();
      grp4(U[j], WU[j], X0, true, [&]() { if (2 * j + 1 < 4) ldx1(X1, st, 1, 2 * j + 1); else FILL(); }); ldu(j, st, 1); SBAR();
    }
#pragma unroll
    for (int j = 0; j < NA; ++j) { grp4(AB[j], WD[j], X0, false, fill); ldd(j, st, 1); SBAR(); }
  };
  auto blockB_head = [&]() { grp4(G[0], WG[0], X1, true, fill); FILL(); grp4(U[0], WU[0], X1, true, fill); };
  auto blockB_rest = [&](lptr sn, bool reload) {
    int k = 0;
    auto mid = [&]() {
      if (!reload) FILL();
      else if (k == 0) ldg(0, sn, 0);
      else if (k == 1) ldu(0, sn, 0);
      else if (k < 6) ldx1(X0, sn, 0, k - 2);
      else FILL();
      ++k;
    };
#pragma unroll
    for (int j = 1; j < NG; ++j) {
      grp4(G[j], WG[j], X1, true, mid); if (reload) { ldg(j, sn, 0); SBAR(); } else FILL();
      grp4(U[j], WU[j], X1, true, mid); if (reload) { ldu(j, sn, 0); SBAR(); } else FILL();
    }
#pragma unroll
    for (int j = 0; j < NA; ++j) { grp4(AB[j], WD[j], X1, false, mid); if (reload) { ldd(j, sn, 0); SBAR(); } else FILL(); }
  };

#pragma unroll
  for (int j = 0; j < NG; ++j)
#pragma unroll
    for (int t = 0; t < 4; ++t) { asm volatile("" : "+v"(G[j][t])); asm volatile("" : "+v"(U[j][t])); }
#pragma unroll
  for (int j = 0; j < NA; ++j)
#pragma unroll
    for (int t = 0; t < 4; ++t) asm volatile("" : "+v"(AB[j][t]));
  PAD_MFMA_IN();
  BAR();
  STAMP(2);
  ldx(X0, lds, 0);
#pragma unroll
  for (int j = 0; j < NG; ++j) { ldg(j, lds, 0); ldu(j, lds, 0); }
#pragma unroll
  for (int j = 0; j < NA; ++j) ldd(j, lds, 0);
  SBAR();
  blockA(lds);
  blockB_head();
  WAITL0(); BAR();
  int slot = 1;
#pragma unroll 1
  for (int s = 0; s < K1_NST - 1; ++s) {
    lptr sn = lds + slot * K1_STAGE;
    slot = (slot == 2) ? 0 : slot + 1;
    if (s >= PF0 && s < PF0 + 4) { pf4(cp, lds_addr(lds) + L_DUMMY); cp += 2 * 4096; }
    blockB_rest(sn, true);
    blockA(sn);
    blockB_head();
    WAITL0();
    if (s == 2) { STAMP(8); }
    BAR();
    if (s == 2) { STAMP(9); }
    if (s == 1) { STAMP(10); }
  }
  blockB_rest(lds, false);
  PAD_MFMA_OUT();
  STAMP(3);
  BAR();
  const lptr cs = lds + L_CS;
  const lptr stats = lds + L_STATS;
  {
    const rsrc_t rab = mk_rsrc(ws + WS_AB, 2 * 28 * 64 * 1024);
#pragma unroll
    for (int pi = 0; pi < 2; ++pi) {
      const lptr sp = stats + (64 * wm + 32 * pi + 8 * g) * 8;
      const f32x4 s0 = *(LAS f32x4*)(sp), s1 = *(LAS f32x4*)(sp + 16), s2 = *(LAS f32x4*)(sp + 32), s3 = *(LAS f32x4*)(sp + 48);
#pragma unroll
      for (int j = 0; j < NA; ++j) {
        const float c = *(LAS float*)(cs + (2 * NBK + NA * wn + j) * 64 + i16 * 4);
        const f32x4 a = AB[j][2 * pi], b = AB[j][2 * pi + 1];
        const int ks = mt * 4 + wm * 2 + pi;
        u32x4 o;
        o[0] = pk_bf16((a[0] - s0[0] * c) * s0[1], (a[1] - s0[2] * c) * s0[3]);
        o[1] = pk_bf16((a[2] - s1[0] * c) * s1[1], (a[3] - s1[2] * c) * s1[3]);
        o[2] = pk_bf16((b[0] - s2[0] * c) * s2[1], (b[1] - s2[2] * c) * s2[3]);
        o[3] = pk_bf16((b[2] - s3[0] * c) * s3[1], (b[3] - s3[2] * c) * s3[3]);
        __builtin_amdgcn_raw_buffer_store_b128(o, rab, lane * 16 + ((a0 + NA * wn + j) * 64 + ks) * 1024, 0, STAUX);
      }
    }
  }
  const rsrc_t rh = mk_rsrc(ws + WS_H, 128 * 56 * 1024);
  const int tt0 = mt * 8 + wm * 4;
  f32x2 st[4];
#pragma unroll
  for (int t = 0; t < 4; ++t) st[t] = *(LAS f32x2*)(stats + (64 * wm + 32 * (t >> 1) + 8 * (i16 >> 2) + 4 * (t & 1) + (i16 & 3)) * 8);
#pragma unroll
  for (int pp = 0; pp < NG / 2; ++pp) {
    const int ks = ((f0 >> 4) + NG * wn + 2 * pp) >> 1;
    f32x4 cg[2], cu[2];
#pragma unroll
    for (int j = 0; j < 2; ++j) {
      cg[j] = *(LAS f32x4*)(cs + (NG * wn + 2 * pp + j) * 64 + g * 16);
      cu[j] = *(LAS f32x4*)(cs + (NBK + NG * wn + 2 * pp + j) * 64 + g * 16);
    }
#pragma unroll
    for (int th = 0; th < 2; ++th) {
      f32x2 gg[8], uu[8], z[8];
#pragma unroll
      for (int i = 0; i < 8; ++i) {
        const int t = 2 * th + (i >> 2), j = (i >> 1) & 1, hh = i & 1;
        const f32x2 c2 = {cg[j][2 * hh], cg[j][2 * hh + 1]};
        const f32x2 a2 = {G[2 * pp + j][t][2 * hh], G[2 * pp + j][t][2 * hh + 1]};
        gg[i] = (a2 - c2 * st[t].x) * st[t].y;
      }
#pragma unroll
      for (int i = 0; i < 8; ++i) z[i] = gg[i] * ((gg[i] * gg[i]) * -0.1029432397f + -2.302208198f);
#pragma unroll
      for (int i = 0; i < 8; ++i) { z[i].x = __builtin_amdgcn_exp2f(z[i].x); z[i].y = __builtin_amdgcn_exp2f(z[i].y); }
#pragma unroll
      for (int i = 0; i < 8; ++i) {
        const int t = 2 * th + (i >> 2), j = (i >> 1) & 1, hh = i & 1;
        const f32x2 c2 = {cu[j][2 * hh], cu[j][2 * hh + 1]};
        const f32x2 a2 = {U[2 * pp + j][t][2 * hh], U[2 * pp + j][t][2 * hh + 1]};
        uu[i] = ((a2 - c2 * st[t].x) * st[t].y) * gg[i];
        z[i] = z[i] + 1.0f;
      }
#pragma unroll
      for (int i = 0; i < 8; ++i) { z[i].x = __builtin_amdgcn_rcpf(z[i].x); z[i].y = __builtin_amdgcn_rcpf(z[i].y); }
#pragma unroll
      for (int i = 0; i < 8; ++i) uu[i] = uu[i] * z[i];
#pragma unroll
      for (int tl = 0; tl < 2; ++tl) {
        const u32x4 o = {pk_bf16(uu[4 * tl].x, uu[4 * tl].y), pk_bf16(uu[4 * tl + 1].x, uu[4 * tl + 1].y),
                         pk_bf16(uu[4 * tl + 2].x, uu[4 * tl + 2].y), pk_bf16(uu[4 * tl + 3].x, uu[4 * tl + 3].y)};
        __builtin_amdgcn_raw_buffer_store_b128(o, rh, lane * 16 + ((tt0 + 2 * th + tl) * 56 + ks) * 1024, 0, STAUX);
      }
      if (pp == 0 && th == 0) {
        WAITV(2);
        if (lane == 0) {
          const unsigned n = __hip_atomic_fetch_add((LAS unsigned*)(lds + L_WORDS), 1u, __ATOMIC_RELAXED, __HIP_MEMORY_SCOPE_WORKGROUP);
          if (n == 3u) st_flag(ws + WS_FAB + (mt * 16 + grp) * 64, get_tag(lds));
        }
        STAMP(5);
      }
    }
  }
  STAMP(4);
  if (MODE == 1) {
    WAITV(0);
    if (lane == 0) {
      const unsigned n = __hip_atomic_fetch_add((LAS unsigned*)(lds + L_WORDS + 4), 1u, __ATOMIC_RELAXED, __HIP_MEMORY_SCOPE_WORKGROUP);
      if (n == 3u) st_flag(ws + WS_FH + (mt * 16 + grp) * 64, get_tag(lds));
    }
  }
}

template <int NBK, int NWU, int NDU>
struct ColSum {
  f32x4 cw[NWU > 0 ? NWU : 1], cd[NDU > 0 ? NDU : 1];
  __device__ __forceinline__ void init() {
#pragma unroll
    for (int j = 0; j < (NWU > 0 ? NWU : 1); ++j) cw[j] = f32x4{0, 0, 0, 0};
#pragma unroll
    for (int j = 0; j < (NDU > 0 ? NDU : 1); ++j) cd[j] = f32x4{0, 0, 0, 0};
  }
  bf16x8 w[2][NWU > 0 ? NWU : 1], d[2][NDU > 0 ? NDU : 1];
  __device__ __forceinline__ void load(lptr st, int up, int w0, int d0, int lane, int ks) {
    constexpr int NCQ = NBK / 4, WSZ = NBK * 8 * 256, KSO = 4 * NCQ * 1024, DO = K1_WO + 2 * WSZ;
    const int i16 = lane & 15, g = lane >> 4, q = i16 >> 2, p = i16 & 3;
    const int fw = ((q >> 1) & 1) | ((g & 1) << 1);
    const int vb = K1_WO + up * WSZ + (w0 >> 2) * 1024 + g * NCQ * 1024 + q * 128 + p * 8;
#pragma unroll
    for (int j = 0; j < NWU; ++j) { lptr b = st + vb + ((j ^ fw) * 32) + ks * KSO; w[ks][j] = ldstr(b, b + 512); }
#pragma unroll
    for (int j = 0; j < NDU; ++j) d[ks][j] = ldsr128(st + DO + (16 * (d0 + j) + i16) * 128 + ((g + 4 * ks) ^ (i16 & 7)) * 16);
  }
  __device__ __forceinline__ void mma(int ks) {
    const u32x4 ones_u = {0x3f803f80u, 0x3f803f80u, 0x3f803f80u, 0x3f803f80u};
    const bf16x8 ONES = __builtin_bit_cast(bf16x8, ones_u);
#pragma unroll
    for (int j = 0; j < NWU; ++j) cw[j] = mfma(w[ks][j], ONES, cw[j]);
#pragma unroll
    for (int j = 0; j < NDU; ++j) cd[j] = mfma(ONES, d[ks][j], cd[j]);
  }
  __device__ __forceinline__ void store(lptr lds, int up, int w0, int d0, int lane) {
    const int i16 = lane & 15, g = lane >> 4;
#pragma unroll
    for (int j = 0; j < NWU; ++j)
      if (i16 == 0) *(LAS f32x4*)(lds + L_CS + (up * NBK + w0 + j) * 64 + g * 16) = cw[j];
#pragma unroll
    for (int j = 0; j < NDU; ++j)
      if (g == 0) *(LAS float*)(lds + L_CS + (2 * NBK + d0 + j) * 64 + i16 * 4) = cd[j][0];
  }
};

template <int MODE, bool TB, int NWU, int NDU>
__device__ __forceinline__ void p1_helper(lptr lds, const Args& a, int xcd, int idx, int mt, int grp, int f0, int a0, int h, int lane, unsigned nonce) {
  char* ws = a.ws;
  constexpr int NBK = TB ? 4 : 8, WSZ = NBK * 8 * 256;
  constexpr int NWP = TB ? 4 : 8, NDP = TB ? 4 : 1, PER = 4 + NWP + NDP;
  const int mh = xcd & 1, nq = xcd >> 1;
  const int up = h >> 1;
  const u32x4 rx = mk_srd(a.x, S * D * 2);
  const u32x4 rw = mk_srd(up ? a.Wu : a.Wg, D * F * 2);
  const u32x4 rd = mk_srd(a0 >= 28 ? a.Di : a.Dr, D * D * 2);
  const int e0 = (a0 >= 28 ? a0 - 28 : a0) * 16;
  const int r8 = lane >> 3, c8 = lane & 7;
  int vx[2];
  vx[0] = r8 * 896 + (c8 ^ (r8 & 3)) * 16;
  vx[1] = r8 * 896 + (c8 ^ ((r8 & 3) | 4)) * 16;
  const int vd = r8 * 896 + (c8 ^ r8) * 16;
  int vw[2];
  {
    const int f0_ = (r8 >> 1) & 1;
    vw[0] = r8 * (F * 2) + f0 * 2 + ((((c8 >> 1) ^ f0_) * 2) + (c8 & 1)) * 16;
    vw[1] = r8 * (F * 2) + f0 * 2 + ((((c8 >> 1) ^ (f0_ | 2)) * 2) + (c8 & 1)) * 16;
  }
  const unsigned l0 = lds_addr(lds);
  const unsigned dummy = l0 + L_DUMMY + (h & 1) * 256;
  auto piece = [&](int s, int slot, int i) {
    const unsigned st = l0 + slot * K1_STAGE;
    if (i < 4) { const int pc = 4 * h + i; dma16(rx, st + pc * 1024, vx[i & 1], (mt * 128 + 8 * pc) * 896 + s * 128); }
    else if (i < 4 + NWP) {
      const int j = i - 4, pj = NWP * (h & 1) + j, kb = TB ? pj : pj >> 1, cq = TB ? 0 : pj & 1;
      dma16(rw, st + K1_WO + up * WSZ + pj * 1024, vw[TB ? (j & 1) : ((j >> 1) & 1)], (s * 64 + 8 * kb) * (F * 2) + cq * 128);
    } else { const int pc = NDP * h + (i - 4 - NWP); dma16(rd, st + K1_WO + 2 * WSZ + pc * 1024, vd, (e0 + 8 * pc) * 896 + s * 128); }
  };
  auto issue_a = [&](int s, int slot) {
#pragma unroll
    for (int i = 0; i < 8; ++i) piece(s, slot, i);
  };
  auto issue_b = [&](int s, int slot) {
#pragma unroll
    for (int i = 8; i < PER; ++i) piece(s, slot, i);
  };
  const int ln2 = lane >> 1, hb = (lane & 1) * 64;
  const char* pfx = (const char*)a.x + (size_t)(1024 * mh + 32 * idx + ln2) * 896 + hb;
  const char* pfw;
  {
    const int j = ln2 < 28 ? ln2 : 27;
    const int mat = j >= 14, jj = j - 14 * mat, rr = jj >= 7, ln = jj - 7 * rr;
    pfw = (const char*)(mat ? a.Wu : a.Wg) + (size_t)(2 * idx + rr) * (F * 2) + 896 * nq + 128 * ln + hb;
  }
  const int srow = mt * 128 + 8 * grp + 2 * h;
  u32x4 sx0, sx1;
  {
    const u16* sp = a.x + (size_t)srow * D + (lane < 56 ? lane : 55) * 8;
    asm volatile("global_load_dwordx4 %0, %2, off\n\tglobal_load_dwordx4 %1, %2, off offset:896" : "=&v"(sx0), "=&v"(sx1) : "v"(sp) : "memory");
  }
  STAMP(1);
  if (h < 3) { pf4(pfx + h * 128, dummy); pf4(pfw + (size_t)h * (64 * F * 2), dummy); }
  else {
    const char* db = (const char*)(nq >= 2 ? a.Di : a.Dr) + (size_t)(224 * (nq & 1) + 7 * idx) * 896 + hb;
    pf4(db + ln2 * 128, dummy);
    pf4(db + (ln2 < 17 ? 32 + ln2 : 48) * 128, dummy);
  }
  STAMP(3);
  issue_a(0, 0); issue_b(0, 0);
  STAMP(5);
  constexpr int WD_SCR = 2 * K1_STAGE + (TB ? 45056 : 40960);
  const bool wdjob = blockIdx.x < 196;
  const int wd_kp = blockIdx.x % 28, wd_cg = blockIdx.x / 28;
  if (wdjob) {
    const u32x4 rwd = mk_srd(a.Wd, F * D * 2);
    const int rs = (lane & 15) >> 1, half = lane & 1, b = lane >> 4;
    const int vq = rs * 896 + (64 * wd_cg + 16 * b + 8 * half) * 2;
#pragma unroll
    for (int i = 0; i < 2; ++i) dma16(rwd, l0 + WD_SCR + (2 * h + i) * 1024, vq, (64 * wd_kp + 8 * (2 * h + i)) * 896);
  }
  STAMP(4);
  if (h == 3 && lane < 4) ((LAS unsigned*)(lds + L_WORDS))[lane] = 0u;
  ColSum<NBK, NWU, NDU> cs;
  cs.init();
  const int cup = TB ? (h & 1) : up, w0 = TB ? 0 : 4 * (h & 1), d0 = TB ? 4 * (h & 1) : (h & 1);
  auto slice = [&](int k, lptr st) {
    SBAR();
    if (k < 2) cs.load(st, cup, w0, d0, lane, k); else cs.mma(k - 2);
    SBAR();
  };
  STAMP(6);
  if (wdjob) { WAITV(PER + 4); } else { WAITV(PER + 2); }
  asm volatile("" : "+v"(sx0), "+v"(sx1));
  auto row_stats = [&](const u32x4& v) {
    float f[8];
#pragma unroll
    for (int i = 0; i < 4; ++i) { f[2 * i] = lane < 56 ? bf_lo(v[i]) : 0.f; f[2 * i + 1] = lane < 56 ? bf_hi(v[i]) : 0.f; }
    const float mean = wave_sum(((f[0] + f[1]) + (f[2] + f[3])) + ((f[4] + f[5]) + (f[6] + f[7]))) * (1.0f / D);
    float q = 0.f;
#pragma unroll
    for (int i = 0; i < 8; ++i) { const float d_ = f[i] - mean; q = __builtin_fmaf(d_, d_, q); }
    const float var = wave_sum(lane < 56 ? q : 0.f) * (1.0f / D);
    const float sd = rbf(__builtin_sqrtf(rbf(rbf(var) + 1e-5f)));
    return pk_bf16(mean, sd);
  };
  unsigned pay0 = row_stats(sx0), pay1 = row_stats(sx1);
  asm volatile("" : "+v"(pay0), "+v"(pay1));
  WAITL0(); WAITV(0);
  STAMP(7); BAR();
  STAMP(2);
  const unsigned tag1 = get_tag(lds);
  if (lane < 2)
    __hip_atomic_store((GAS unsigned long long*)(ws + WS_ST + (size_t)(srow + lane) * 8), ((unsigned long long)tag1 << 32) | (lane ? pay1 : pay0), __ATOMIC_RELAXED, __HIP_MEMORY_SCOPE_AGENT);
  slice(0, lds); slice(1, lds);
#pragma unroll
  for (int i = 0; i < PER; ++i) { piece(1, 1, i); if (i == 2 || i == 3) slice(i, lds); }
  if (h == 3 && wdjob) {
    const int i16 = lane & 15, g = lane >> 4, q = i16 >> 2, p = i16 & 3;
    const rsrc_t rt = mk_rsrc(ws + WS_WDT, 28 * 56 * 1024);
    lptr b0 = lds + WD_SCR + (g >> 1) * 1024 + ((g & 1) * 4 + q) * 32 + p * 8;
#pragma unroll
    for (int kk = 0; kk < 2; ++kk)
#pragma unroll
      for (int u = 0; u < 4; ++u) {
        const bf16x8 f = ldstr(b0 + kk * 4096 + u * 256, b0 + kk * 4096 + u * 256 + 2048);
        __builtin_amdgcn_raw_buffer_store_b128(__builtin_bit_cast(u32x4, f), rt, lane * 16 + ((4 * wd_cg + u) * 56 + 2 * wd_kp + kk) * 1024, 0, STAUX);
      }
    WAITL0();
  }
  issue_a(2, 2);
  pf4(pfx + (3 + h) * 128, dummy); pf4(pfw + (size_t)(3 + h) * (64 * F * 2), dummy);

  WAITL0(); WAITV(10); BAR();
  int slot = 1;
#pragma unroll 1
  for (int s = 1; s < K1_NST; ++s) {
    const int s1_ = (slot == 2) ? 0 : slot + 1, s2_ = (slot == 0) ? 2 : slot - 1;
    lptr st = lds + slot * K1_STAGE;
    slice(0, st);
    if (s + 1 < K1_NST) {
#pragma unroll
      for (int i = 8; i < PER; ++i) { piece(s + 1, s1_, i); if (i == 8) slice(1, st); else if (i == 9) slice(2, st); else if (i == 10) slice(3, st); }
    } else { slice(1, st); slice(2, st); slice(3, st); }
    if (s + 2 < K1_NST) issue_a(s + 2, s2_);
    if (s == 4) {
      if (h == 0 && lane == 0) st_flag(ws + WS_ARRIVE + blockIdx.x * 64, tag1);
      if (h == 3 && wdjob && lane == 0) st_flag(ws + WS_FWD + blockIdx.x * 64, tag1);
    }
    if (s == 5) {
      const GAS unsigned long long* gp = (const GAS unsigned long long*)(ws + WS_ST + (size_t)(mt * 128 + 32 * h + (lane & 31)) * 8);
      unsigned long long gv = 0;
      for (unsigned sp = 0; sp < SPIN_MAX; ++sp) {
        gv = __hip_atomic_load(gp, __ATOMIC_RELAXED, __HIP_MEMORY_SCOPE_AGENT);
        if (__all((unsigned)(gv >> 32) == tag1)) break;
        __builtin_amdgcn_s_sleep(2);
      }
      if (lane < 32) *(LAS f32x2*)(lds + L_STATS + (32 * h + lane) * 8) = f32x2{bf_lo((unsigned)gv), 1.0f / bf_hi((unsigned)gv)};
    }
    WAITL0();
    if (s + 2 < K1_NST) { WAITV(8); }
    if (s == 3) { STAMP(8); }
    BAR();
    if (s == 3) { STAMP(9); }
    if (s == 2) { STAMP(10); }
    slot = s1_;
  }
  cs.store(lds, cup, w0, d0, lane);
  WAITL0(); BAR();
}

template <int MODE, int NB>
__device__ __forceinline__ void p2_loader(lptr lds, const Args& a, int xcd, int idx, int r, int m, int n, int lw, int lane, unsigned nonce) {
  char* ws = a.ws;
  constexpr int PER = 4 + NB;
  const u32x4 rc = mk_srd((r >> 1) ? a.Ci : a.Cr, S * S * 2);
  const u32x4 rb = mk_srd(ws + WS_AB, 2 * 28 * 64 * 1024);
  const u32x4 rh = mk_srd(ws + WS_H, 128 * 56 * 1024);
  const u32x4 rw = mk_srd(ws + WS_WDT, 28 * 56 * 1024);
  const int r8 = lane >> 3, c8 = lane & 7;
  int va[2];
  va[0] = r8 * 4096 + (c8 ^ (r8 & 3)) * 16;
  va[1] = r8 * 4096 + (c8 ^ ((r8 & 3) | 4)) * 16;
  const int vl = lane * 16;
  const int k0 = 1024 * (r & 1), p = r >> 1;
  const int j0 = (lw < 2) ? 4 * lw : 8 + 3 * (lw - 2);
  const unsigned l0 = lds_addr(lds);
  const unsigned dummy = l0 + L_DUMMY + (lw & 1) * 256;
  auto issueA = [&](int s, int slot) {
    const unsigned st = l0 + slot * P_STAGE;
    if (s < P_NY) {
#pragma unroll
      for (int i = 0; i < 4; ++i) { const int pc = 4 * lw + i; dma16(rc, st + pc * 1024, va[i & 1], (128 * m + 8 * pc) * 4096 + (k0 + 64 * (s & 15)) * 2); }
    } else {
#pragma unroll
      for (int i = 0; i < 4; ++i) { const int pc = 4 * lw + i; dma16(rh, st + pc * 1024, vl, ((8 * m + (pc >> 1)) * 56 + 14 * r + 2 * (s - P_NY) + (pc & 1)) * 1024); }
    }
  };
  auto issueB = [&](int s, int slot) {
    const unsigned st = l0 + slot * P_STAGE + 16384;
    if (s < P_NY) {
#pragma unroll
      for (int j = 0; j < NB; ++j) { const int jj = j0 + j; dma16(rb, st + jj * 1024, vl, ((p * 28 + 7 * n + (jj >> 1)) * 64 + (k0 >> 5) + 2 * (s & 15) + (jj & 1)) * 1024); }
    } else {
#pragma unroll
      for (int j = 0; j < NB; ++j) { const int jj = j0 + j; dma16(rw, st + jj * 1024, vl, ((7 * n + (jj >> 1)) * 56 + 14 * r + 2 * (s - P_NY) + (jj & 1)) * 1024); }
    }
  };
  __builtin_amdgcn_s_setprio(0);
  const unsigned tag = get_tag(lds);
  issueA(0, 0); issueA(1, 1);
  if (lw == 3) {
    const int mtp = 8 * (r & 1) + (lane & 7), j = (lane >> 3) & 3;
    const bool act = lane < 32 && ((n & 1) == 0 || j == 3);
    poll_all(ws + WS_FAB + (mtp * 16 + (2 * p + (n >> 1)) * 4 + j) * 64, act, tag);
    if (lane == 0) *(volatile LAS unsigned*)(lds + L_WORDS + 8) = 1u;
  } else lds_spin(lds + L_WORDS + 8);
  STAMP(16);
  {
    const int l = (lane >> 1) < 28 ? (lane >> 1) : 27;
    const int sl = l >= 14, li = 14 * idx + l - 14 * sl;
    const char* bp = ws + WS_AB + ((size_t)(p * 28 + (li >> 4)) * 64 + 32 * (r & 1) + 2 * (4 * lw + sl) + ((li >> 3) & 1)) * 1024 + (li & 7) * 128 + (lane & 1) * 64;
    pf4(bp, dummy); pf4(bp + 4 * 1024, dummy);
  }
  issueB(0, 0); issueB(1, 1);
  __builtin_amdgcn_s_setprio(3);
  WAITV(NB); BAR();
  issueA(2, 2); issueB(2, 2);
  WAITV(PER); BAR();
  issueA(3, 3); issueB(3, 3);
  const int cg0 = (112 * n) >> 6, cg1 = (112 * n + 111) >> 6, jj = lane - 4;
  const bool fact = lane < 4 || (jj < 21 && cg0 + jj / 7 <= cg1);
  const char* fp = lane < 4 ? ws + WS_FH + (m * 16 + 4 * r + lane) * 64 : ws + WS_FWD + ((fact ? cg0 + jj / 7 : cg0) * 28 + 7 * r + (fact ? jj % 7 : 0)) * 64;
  unsigned fval = 0;
  int slot = 4;
#pragma unroll 1
  for (int s = 1; s < P_NST; ++s) {
    if (lw == 3 && s == 7) asm volatile("global_load_dword %0, %1, off sc1" : "+v"(fval) : "v"(fp) : "memory");
    if (s == 10) {
      if (lw == 3) {
        asm volatile("" : "+v"(fval));
        if (!__all(!fact || fval == tag)) poll_all(fp, fact, tag);
        if (lane == 0) *(volatile LAS unsigned*)(lds + L_WORDS + 12) = 1u;
      }
    }
    if (s + 3 == P_NY && lw != 3) lds_spin(lds + L_WORDS + 12);
    if (s + 3 < P_NST && s >= 2) { issueA(s + 3, slot); issueB(s + 3, slot); }
    if (s == 1) { issueA(4, 4); issueB(4, 4); }
    if (s + 3 < P_NST) { WAITV(2 * PER); } else if (s + 2 < P_NST) { WAITV(PER); } else { WAITV(0); }
    slot = (slot == 4) ? 0 : slot + 1;
    if (s + 1 < P_NST) BAR();
  }
}

template <int MODE>
__global__ __launch_bounds__(512, 2) void fused_kernel(Args a) {
  extern __shared__ __attribute__((aligned(1024))) char smem[];
  lptr lds = (lptr)smem;
  char* ws = a.ws;
  const int tid = threadIdx.x, lane = tid & 63;
  const int wid = __builtin_amdgcn_readfirstlane(tid >> 6);
  const int xcd = blockIdx.x & 7, idx = blockIdx.x >> 3;
  STAMP(0); STAMPR(30);
  const unsigned nonce = 0;
  if (wid == 0) {
    const unsigned tg = (MODE == 0) ? ld_flag(ws + WS_NONCE) * 0x9E3779B1u + 0x7F4A7C15u : 0x51u;
    if (lane == 0 && MODE != 2) *(volatile LAS unsigned*)(lds + L_WORDS + 16) = tg;
    WAITL0();
  }
  if (wid >= 4) __builtin_amdgcn_s_setprio(3);

  if (MODE != 2) {
    const int mh = xcd & 1, nq = xcd >> 1;
    const int mt = mh * 8 + (idx & 7), j = idx >> 3;
    const bool tb = j == 3;
    const int grp = nq * 4 + j;
    const int f0 = 448 * nq + 128 * j, a0 = 14 * nq + 2 * j;
    if (wid < 4) {
      const char* cp = (const char*)((nq >> 1) ? a.Ci : a.Cr) + (size_t)(1024 * mh + 32 * idx + 8 * wid + (lane >> 5)) * 4096 + 2048 * (nq & 1) + (lane & 31) * 64;
      if (tb) p1_consumer<MODE, 2, 4>(lds, ws, mt, grp, f0, a0, wid & 1, wid >> 1, lane, cp);
      else p1_consumer<MODE, 4, 1>(lds, ws, mt, grp, f0, a0, wid & 1, wid >> 1, lane, cp);
    } else {
      const int h = wid - 4;
      if (tb) {
        if (h < 2) p1_helper<MODE, true, 4, 0>(lds, a, xcd, idx, mt, grp, f0, a0, h, lane, nonce);
        else p1_helper<MODE, true, 0, 4>(lds, a, xcd, idx, mt, grp, f0, a0, h, lane, nonce);
      } else {
        if (h < 2) p1_helper<MODE, false, 4, 1>(lds, a, xcd, idx, mt, grp, f0, a0, h, lane, nonce);
        else p1_helper<MODE, false, 4, 0>(lds, a, xcd, idx, mt, grp, f0, a0, h, lane, nonce);
      }
    }
  }
  if (MODE == 1) return;
  if (MODE == 2) { if (tid < 4) ((LAS unsigned*)(lds + L_WORDS))[tid] = 0u; if (tid == 4) ((LAS unsigned*)(lds + L_WORDS))[4] = 0x51u; __syncthreads(); }

  const int p1_mt = (xcd & 1) * 8 + (idx & 7), p1_grp = (xcd >> 1) * 4 + (idx >> 3);
  const int r = xcd >> 1, m = 8 * (xcd & 1) + (idx & 7), n = idx >> 3;
  const int tile = m * 4 + n;
  const int i16 = lane & 15, g = lane >> 4;
  f32x4 accY[7][2], acc[7][2];
  if (wid >= 6) {
    p2_loader<MODE, 3>(lds, a, xcd, idx, r, m, n, wid - 4, lane, nonce);
  } else if (wid >= 4) {
    p2_loader<MODE, 4>(lds, a, xcd, idx, r, m, n, wid - 4, lane, nonce);
  } else {
    const int w = wid;
    const int rowt = 32 * w + 8 * (i16 >> 2) + (i16 & 3);
    const int vt0 = rowt * 128 + ((g) ^ (i16 & 7)) * 16, vt1 = rowt * 128 + ((g + 4) ^ (i16 & 7)) * 16;
    const int ve = 16384 + lane * 16;
    const int vh = (4 * w) * 1024 + lane * 16;
    bf16x8 T0[4], T1[4], E[14];
    auto ldTy = [&](bf16x8 (&T)[4], lptr st) {
      T[0] = ldsr128(st + vt0); T[1] = ldsr128(st + vt0 + 512); T[2] = ldsr128(st + vt1); T[3] = ldsr128(st + vt1 + 512);
    };
    auto ldTd = [&](bf16x8 (&T)[4], lptr st) {
      T[0] = ldsr128(st + vh); T[1] = ldsr128(st + vh + 2048); T[2] = ldsr128(st + vh + 1024); T[3] = ldsr128(st + vh + 3072);
    };
    auto ldE = [&](int j, lptr st) { E[j] = ldsr128(st + ve + ((j % 7) * 2 + j / 7) * 1024); };
    auto mm = [&](int j, bf16x8 (&T)[4]) {
      mfma_ip(acc[j % 7][0], E[j], T[(j / 7) * 2]);
      mfma_ip(acc[j % 7][1], E[j], T[(j / 7) * 2 + 1]);
    };
#define GHEAD 3
    constexpr int GH = GHEAD;
    auto ldT1 = [&](bf16x8 (&T)[4], lptr st, int i, int NT) {
      if (NT == 0) T[i] = ldsr128(st + (i >= 2 ? vt1 : vt0) + (i & 1) * 512);
      else T[i] = ldsr128(st + vh + (i & 1) * 2048 + (i >> 1) * 1024);
    };
    auto rest = [&](bf16x8 (&Tc)[4], bf16x8 (&Tn)[4], lptr sn, int NT) {
#pragma unroll
      for (int j = GH; j < 14; ++j) {
        const int k = j - GH;
        mfma_ip(acc[j % 7][0], E[j], Tc[(j / 7) * 2]); SBAR();
        if (NT != 2 && k < 4 + GH) { if (k < 4) ldT1(Tn, sn, k, NT); else ldE(k - 4, sn); SBAR(); }
        mfma_ip(acc[j % 7][1], E[j], Tc[(j / 7) * 2 + 1]); SBAR();
        if (NT != 2) ldE(j, sn); else FILL();
        SBAR();
      }
    };
    auto head = [&](bf16x8 (&Tc)[4]) {
#pragma unroll
      for (int j = 0; j < GH; ++j) { mm(j, Tc); FILL(); }
      SBAR();
    };
#pragma unroll
    for (int u = 0; u < 7; ++u) { acc[u][0] = f32x4{0, 0, 0, 0}; acc[u][1] = f32x4{0, 0, 0, 0}; }
#pragma unroll
    for (int u = 0; u < 7; ++u) { asm volatile("" : "+v"(acc[u][0])); asm volatile("" : "+v"(acc[u][1])); }
    PAD_MFMA_IN();
    if (w != r) {
      const rsrc_t rp = mk_rsrc(ws + WS_PD + (size_t)((tile * 4 + r) * 4 + w) * 7168, 7168);
      const u32x4 sent = {0xffffffffu, 0xffffffffu, 0xffffffffu, 0xffffffffu};
#pragma unroll
      for (int u = 0; u < 7; ++u) {
        __builtin_amdgcn_raw_buffer_store_b128(sent, rp, u * 1024 + lane * 16, 0, 16);
      }
    }
    STAMP(16); STAMPR(28);
    BAR();
    STAMP(17); STAMPR(27);
    ldTy(T0, lds);
#pragma unroll
    for (int j = 0; j < 14; ++j) ldE(j, lds);
    SBAR();
    head(T0);
    WAITL0(); BAR();
    int slot = 1;
#pragma unroll 1
    for (int s = 0; s < P_NY - 2; s += 2) {
      lptr sn = lds + slot * P_STAGE; slot = (slot == 4) ? 0 : slot + 1;
      rest(T0, T1, sn, 0); head(T1);
      WAITL0(); BAR();
      sn = lds + slot * P_STAGE; slot = (slot == 4) ? 0 : slot + 1;
      rest(T1, T0, sn, 0); head(T0);
      WAITL0(); BAR();
      if (YREP > 1 && s == 14) { STAMP(23); }
      if (MODE == 0 && s == 0) {
        WAITV(0);
        if (lane == 0) {
          const unsigned cnt = __hip_atomic_fetch_add((LAS unsigned*)(lds + L_WORDS + 4), 1u, __ATOMIC_RELAXED, __HIP_MEMORY_SCOPE_WORKGROUP);
          if (cnt == 3u) st_flag(ws + WS_FH + (p1_mt * 16 + p1_grp) * 64, get_tag(lds));
        }
      }
    }
    {
      lptr sn = lds + slot * P_STAGE; slot = (slot == 4) ? 0 : slot + 1;
      rest(T0, T1, sn, 0); head(T1);
      WAITL0(); BAR();
      sn = lds + slot * P_STAGE; slot = (slot == 4) ? 0 : slot + 1;
      rest(T1, T0, sn, 1);
    }
    PAD_MFMA_OUT();
    STAMP(18);
    {
      const float sg = (r >> 1) ? -1.0f : 1.0f;
#pragma unroll
      for (int u = 0; u < 7; ++u) { accY[u][0] = acc[u][0] * sg; accY[u][1] = acc[u][1] * sg; acc[u][0] = f32x4{0, 0, 0, 0}; acc[u][1] = f32x4{0, 0, 0, 0}; }
      if (w != r) {
        const rsrc_t rp = mk_rsrc(ws + WS_PY + (size_t)((tile * 4 + r) * 4 + w) * 7168, 7168);
#pragma unroll
        for (int u = 0; u < 7; ++u) {
          const u32x4 o = {pk_bf16(accY[u][0][0], accY[u][0][1]), pk_bf16(accY[u][0][2], accY[u][0][3]),
                           pk_bf16(accY[u][1][0], accY[u][1][1]), pk_bf16(accY[u][1][2], accY[u][1][3])};
          __builtin_amdgcn_raw_buffer_store_b128(o, rp, u * 1024 + lane * 16, 0, 16);
        }
      }
    }
#pragma unroll
    for (int u = 0; u < 7; ++u) { asm volatile("" : "+v"(acc[u][0])); asm volatile("" : "+v"(acc[u][1])); }
    PAD_MFMA_IN();
    head(T0);
    WAITL0(); BAR();
#pragma unroll 1
    for (int s = 0; s < P_ND - 1; s += 2) {
      lptr sn = lds + slot * P_STAGE; slot = (slot == 4) ? 0 : slot + 1;
      rest(T0, T1, sn, 1); head(T1);
      WAITL0(); BAR();
      if (s == 0 && w != r) {
        WAITV(0);
        const unsigned tag = get_tag(lds);
        if (lane == 0) st_flag(ws + WS_FY + ((tile * 4 + r) * 4 + w) * 64, tag);
      }
      sn = lds + slot * P_STAGE; slot = (slot == 4) ? 0 : slot + 1;
      rest(T1, T0, sn, 1); head(T0);
      WAITL0(); if (s + 2 < P_ND - 1) BAR();
    }
    rest(T0, T1, lds, 2);
    PAD_MFMA_OUT();
    STAMP(19); STAMPR(29);
    if (w != r) {
      const rsrc_t rp = mk_rsrc(ws + WS_PD + (size_t)((tile * 4 + r) * 4 + w) * 7168, 7168);
#pragma unroll
      for (int u = 0; u < 7; ++u) {
        u32x4 o = {pk_bf16(acc[u][0][0], acc[u][0][1]), pk_bf16(acc[u][0][2], acc[u][0][3]),
                   pk_bf16(acc[u][1][0], acc[u][1][1]), pk_bf16(acc[u][1][2], acc[u][1][3])};
#pragma unroll
        for (int k = 0; k < 4; ++k) o[k] = o[k] < 0xfffffffeu ? o[k] : 0xfffffffeu;
        __builtin_amdgcn_raw_buffer_store_b128(o, rp, u * 1024 + lane * 16, 0, 16);
      }
    } else {
#pragma unroll
      for (int uu = 0; uu < 7; ++uu) {
        *(LAS f32x4*)(lds + (uu * 4 + 0) * 1024 + lane * 16) = accY[uu][0];
        *(LAS f32x4*)(lds + (uu * 4 + 1) * 1024 + lane * 16) = accY[uu][1];
        *(LAS f32x4*)(lds + (uu * 4 + 2) * 1024 + lane * 16) = acc[uu][0];
        *(LAS f32x4*)(lds + (uu * 4 + 3) * 1024 + lane * 16) = acc[uu][1];
      }
      WAITL0();
    }
    BAR();
  }
  if (wid >= 4) {
    __builtin_amdgcn_s_setprio(0);
    const int hw = wid - 4;
    const int nu = hw < 3 ? 2 : 1;
    const unsigned tag = get_tag(lds);
    const int tok0 = 128 * m + 32 * r + 8 * (i16 >> 2) + (i16 & 3);
    const rsrc_t ry = mk_rsrc(ws + WS_PY, 64 * 16 * 7168), rd = mk_rsrc(ws + WS_PD, 64 * 16 * 7168);
    u32x2 x0[2], x1[2];
    u32x4 fy[2][3], fd[2][3];
#pragma unroll
    for (int k = 0; k < 2; ++k) {
      const int u = 2 * hw + (k < nu ? k : 0);
      const int ecol = 112 * n + 16 * u + 4 * g;
      x0[k] = *(const u32x2*)(a.x + (size_t)tok0 * D + ecol); x1[k] = *(const u32x2*)(a.x + (size_t)(tok0 + 4) * D + ecol);
    }
    {
      const int rr = (r + 1 + (lane % 3)) & 3;
      poll_all(ws + WS_FY + ((tile * 4 + rr) * 4 + r) * 64, lane < 3, tag);
    }
#pragma unroll
    for (int k = 0; k < 2; ++k)
#pragma unroll
      for (int i = 0; i < 3; ++i)
        fy[k][i] = __builtin_amdgcn_raw_buffer_load_b128(ry, ((tile * 4 + ((r + 1 + i) & 3)) * 4 + r) * 7168 + (2 * hw + (k < nu ? k : 0)) * 1024 + lane * 16, 0, 16);
    STAMP(20);
    if (MODE == 0 && blockIdx.x == 0 && hw == 3) {
#pragma unroll 1
      for (int i = 0; i < 4; ++i) poll_all(ws + WS_ARRIVE + (64 * i + lane) * 64, true, tag);
      if (lane == 0) st_flag(ws + WS_NONCE, tag);
    }
    for (unsigned sp = 0; sp < (SPIN_MAX >> 3); ++sp) {
      const unsigned v = lane < 3 ? __builtin_amdgcn_raw_buffer_load_b32(rd, ((tile * 4 + ((r + 1 + lane) & 3)) * 4 + r) * 7168 + 6 * 1024 + 63 * 16 + 12, 0, 16) : 0u;
      if (__all(v != 0xffffffffu)) break;
      __builtin_amdgcn_s_sleep(1);
    }
    for (unsigned sp = 0; sp < (SPIN_MAX >> 3); ++sp) {
      bool ok = true;
#pragma unroll
      for (int k = 0; k < 2; ++k)
#pragma unroll
        for (int i = 0; i < 3; ++i) {
          fd[k][i] = __builtin_amdgcn_raw_buffer_load_b128(rd, ((tile * 4 + ((r + 1 + i) & 3)) * 4 + r) * 7168 + (2 * hw + (k < nu ? k : 0)) * 1024 + lane * 16, 0, 16);
        }
#pragma unroll
      for (int k = 0; k < 2; ++k)
#pragma unroll
        for (int i = 0; i < 3; ++i)
          ok = ok && fd[k][i][0] != 0xffffffffu && fd[k][i][1] != 0xffffffffu && fd[k][i][2] != 0xffffffffu && fd[k][i][3] != 0xffffffffu;
      if (__all(ok)) break;
      __builtin_amdgcn_s_sleep(2);
    }
    STAMP(21); STAMPR(29);
    BAR();
#pragma unroll
    for (int k = 0; k < 2; ++k) {
      if (k < nu) {
        const int u = 2 * hw + k;
        const int ecol = 112 * n + 16 * u + 4 * g;
        f32x4 y0 = *(LAS f32x4*)(lds + (u * 4 + 0) * 1024 + lane * 16), y1 = *(LAS f32x4*)(lds + (u * 4 + 1) * 1024 + lane * 16);
        f32x4 d0 = *(LAS f32x4*)(lds + (u * 4 + 2) * 1024 + lane * 16), d1 = *(LAS f32x4*)(lds + (u * 4 + 3) * 1024 + lane * 16);
#pragma unroll
        for (int i = 0; i < 3; ++i) {
          y0 += f32x4{bf_lo(fy[k][i][0]), bf_hi(fy[k][i][0]), bf_lo(fy[k][i][1]), bf_hi(fy[k][i][1])};
          y1 += f32x4{bf_lo(fy[k][i][2]), bf_hi(fy[k][i][2]), bf_lo(fy[k][i][3]), bf_hi(fy[k][i][3])};
          d0 += f32x4{bf_lo(fd[k][i][0]), bf_hi(fd[k][i][0]), bf_lo(fd[k][i][1]), bf_hi(fd[k][i][1])};
          d1 += f32x4{bf_lo(fd[k][i][2]), bf_hi(fd[k][i][2]), bf_lo(fd[k][i][3]), bf_hi(fd[k][i][3])};
        }
        const u32x2 o0 = {pk_bf16(bf_lo(x0[k][0]) + y0[0] * silu_f(d0[0]), bf_hi(x0[k][0]) + y0[1] * silu_f(d0[1])),
                          pk_bf16(bf_lo(x0[k][1]) + y0[2] * silu_f(d0[2]), bf_hi(x0[k][1]) + y0[3] * silu_f(d0[3]))};
        const u32x2 o1 = {pk_bf16(bf_lo(x1[k][0]) + y1[0] * silu_f(d1[0]), bf_hi(x1[k][0]) + y1[1] * silu_f(d1[1])),
                          pk_bf16(bf_lo(x1[k][1]) + y1[2] * silu_f(d1[2]), bf_hi(x1[k][1]) + y1[3] * silu_f(d1[3]))};
        *(u32x2*)(a.out + (size_t)tok0 * D + ecol) = o0;
        *(u32x2*)(a.out + (size_t)(tok0 + 4) * D + ecol) = o1;
      }
    }
  }
}

#define SPLIT 0
extern "C" void kernel_launch(void* const* d_in, const int* in_sizes, int n_in, void* d_out, int out_size, void* d_ws, size_t ws_size,
                              hipStream_t stream) {
  Args a;
  a.x = (const u16*)d_in[0]; a.Cr = (const u16*)d_in[1]; a.Ci = (const u16*)d_in[2]; a.Dr = (const u16*)d_in[3]; a.Di = (const u16*)d_in[4];
  a.Wg = (const u16*)d_in[6]; a.Wu = (const u16*)d_in[7]; a.Wd = (const u16*)d_in[8];
  a.ws = (char*)d_ws; a.out = (u16*)d_out;
  (void)hipFuncSetAttribute((const void*)fused_kernel<0>, hipFuncAttributeMaxDynamicSharedMemorySize, L_TOTAL);
  fused_kernel<0><<<256, 512, L_TOTAL, stream>>>(a);
}
```

```cpp
#include <hip/hip_runtime.h>
#include <hip/hip_bf16.h>
#include <stdint.h>

typedef unsigned short u16;
typedef __bf16 bf16x8 __attribute__((ext_vector_type(8)));
typedef short s16x8 __attribute__((ext_vector_type(8)));
typedef float f32x4 __attribute__((ext_vector_type(4)));
typedef float f32x2 __attribute__((ext_vector_type(2)));
typedef unsigned u32x4 __attribute__((ext_vector_type(4)));
typedef unsigned u32x2 __attribute__((ext_vector_type(2)));
#define LAS __attribute__((address_space(3)))
#define GAS __attribute__((address_space(1)))
typedef LAS char* lptr;
typedef __amdgpu_buffer_rsrc_t rsrc_t;

constexpr int S = 2048, D = 448, F = 1792;

constexpr size_t WS_NONCE  = 0;
constexpr size_t WS_ARRIVE = 4096;
constexpr size_t WS_FAB    = 32768;
constexpr size_t WS_FH     = 49152;
constexpr size_t WS_FWD    = 65536;
constexpr size_t WS_FY     = 81920;
constexpr size_t WS_FD     = 147456;
constexpr size_t WS_ST     = 212992;
constexpr size_t WS_CW     = 229376;
constexpr size_t WS_CD     = 286720;
constexpr size_t WS_AB     = 327680;
constexpr size_t WS_H      = WS_AB + (size_t)2 * 28 * 64 * 1024;
constexpr size_t WS_WDT    = WS_H + (size_t)128 * 56 * 1024;
constexpr size_t WS_PY     = WS_WDT + (size_t)28 * 56 * 1024;
constexpr size_t WS_PD     = WS_PY + (size_t)64 * 16 * 7 * 1024;

#define STAMP(i)
#define STAMPR(i)
#define SBAR() __builtin_amdgcn_sched_barrier(0)
#define BAR() __builtin_amdgcn_s_barrier()
#define WAITV(n) asm volatile("s_waitcnt vmcnt(%0)" ::"n"(n) : "memory")
#define WAITL0() __builtin_amdgcn_s_waitcnt(0xC07F)
#define CODE_PF 69632
#define PF0 2
#define STAUX 16
#define SPIN_MAX (1u << 18)

__device__ __forceinline__ rsrc_t mk_rsrc(const void* p, unsigned bytes) {
  return __builtin_amdgcn_make_buffer_rsrc((void*)p, 0, bytes, 0x00020000);
}
__device__ __forceinline__ u32x4 mk_srd(const void* p, unsigned bytes) {
  const unsigned long long a = (unsigned long long)p;
  return u32x4{(unsigned)a, (unsigned)(a >> 32) & 0xffffu, bytes, 0x00020000u};
}
__device__ __forceinline__ unsigned lds_addr(lptr p) { return (unsigned)(size_t)p; }
__device__ __forceinline__ void dma16(const u32x4& srd, unsigned dst, int voff, int soff) {
  asm volatile("s_mov_b32 m0, %0\n\ts_nop 0\n\tbuffer_load_dwordx4 %1, %2, %3 offen lds" ::"s"(dst), "v"(voff), "s"(srd), "s"(soff) : "memory");
}
__device__ __forceinline__ void dma16c(const u32x4& srd, unsigned dst, int voff, int soff) {
  asm volatile("s_mov_b32 m0, %0\n\ts_nop 0\n\tbuffer_load_dwordx4 %1, %2, %3 offen sc1 lds" ::"s"(dst), "v"(voff), "s"(srd), "s"(soff) : "memory");
}
__device__ __forceinline__ void pf4c(const char* p, unsigned dst) {
  asm volatile("s_mov_b32 m0, %0\n\ts_nop 0\n\tglobal_load_lds_dword %1, off sc1" ::"s"(dst), "v"(p) : "memory");
}
__device__ __forceinline__ void pf4(const char* p, unsigned dst) {
  asm volatile("s_mov_b32 m0, %0\n\ts_nop 0\n\tglobal_load_lds_dword %1, off" ::"s"(dst), "v"(p) : "memory");
}
__device__ __forceinline__ bf16x8 ldsr128(lptr p) { return *(const LAS bf16x8*)p; }
__device__ __forceinline__ bf16x8 ldstr(lptr p0, lptr p1) {
  typedef short v4i16_t __attribute__((ext_vector_type(4)));
  v4i16_t lo = __builtin_amdgcn_ds_read_tr16_b64_v4i16((LAS v4i16_t*)p0);
  v4i16_t hi = __builtin_amdgcn_ds_read_tr16_b64_v4i16((LAS v4i16_t*)p1);
  s16x8 v = __builtin_shufflevector(lo, hi, 0, 1, 2, 3, 4, 5, 6, 7);
  return __builtin_bit_cast(bf16x8, v);
}
__device__ __forceinline__ f32x4 mfma(bf16x8 a, bf16x8 b, f32x4 c) { return __builtin_amdgcn_mfma_f32_16x16x32_bf16(a, b, c, 0, 0, 0); }
__device__ __forceinline__ void mfma_ip(f32x4& c, const bf16x8& a, const bf16x8& b) {
  asm volatile("v_mfma_f32_16x16x32_bf16 %0, %1, %2, %0" : "+v"(c) : "v"(a), "v"(b));
}
#define FILL() asm volatile("s_nop 0")
#define PAD_MFMA_OUT() asm volatile("s_nop 7\n\ts_nop 3")
#define PAD_MFMA_IN() asm volatile("s_nop 3")
__device__ __forceinline__ unsigned pk_bf16(float lo, float hi) {
  typedef __bf16 bf16x2 __attribute__((ext_vector_type(2)));
  f32x2 v = {lo, hi};
  bf16x2 b = __builtin_convertvector(v, bf16x2);
  return __builtin_bit_cast(unsigned, b);
}
__device__ __forceinline__ float bf_lo(unsigned v) { return __uint_as_float(v << 16); }
__device__ __forceinline__ float bf_hi(unsigned v) { return __uint_as_float(v & 0xffff0000u); }
__device__ __forceinline__ float rbf(float f) { return bf_lo(pk_bf16(f, 0.f)); }
__device__ __forceinline__ float silu_f(float x) {
  float e = __builtin_amdgcn_exp2f(x * -1.4426950409f);
  return x * __builtin_amdgcn_rcpf(1.0f + e);
}
template <int N>
__device__ __forceinline__ void wave_sums(float (&v)[N]) {
#define DPP_STEP(ctrl, rmask) _Pragma("unroll") for (int i = 0; i < N; ++i) \
    v[i] += __builtin_bit_cast(float, __builtin_amdgcn_update_dpp(0, __builtin_bit_cast(int, v[i]), ctrl, rmask, 0xf, true));
  DPP_STEP(0x111, 0xf)
  DPP_STEP(0x112, 0xf)
  DPP_STEP(0x114, 0xf)
  DPP_STEP(0x118, 0xf)
  DPP_STEP(0x142, 0xa)
  DPP_STEP(0x143, 0xc)
#undef DPP_STEP
#pragma unroll
  for (int i = 0; i < N; ++i) v[i] = __builtin_bit_cast(float, __builtin_amdgcn_readlane(__builtin_bit_cast(int, v[i]), 63));
}
__device__ __forceinline__ unsigned ld_flag(const void* p) { return __hip_atomic_load((const GAS unsigned*)p, __ATOMIC_RELAXED, __HIP_MEMORY_SCOPE_AGENT); }
__device__ __forceinline__ void st_flag(void* p, unsigned v) { __hip_atomic_store((GAS unsigned*)p, v, __ATOMIC_RELAXED, __HIP_MEMORY_SCOPE_AGENT); }
__device__ __forceinline__ unsigned get_tag(lptr lds);
__device__ __forceinline__ void poll_all(const char* p, bool active, unsigned tag) {
  for (unsigned sp = 0; sp < SPIN_MAX; ++sp) {
    const unsigned v = active ? ld_flag(p) : tag;
    if (__all(v == tag)) break;
    __builtin_amdgcn_s_sleep(2);
  }
}
__device__ __forceinline__ void lds_spin(lptr p) {
  for (unsigned sp = 0; sp < SPIN_MAX * 4; ++sp) {
    if (*(volatile LAS unsigned*)p != 0u) break;
    __builtin_amdgcn_s_sleep(2);
  }
}

constexpr int K1_STAGE = 53248, K1_NST = 7, K1_WO = 16384;
constexpr int L_MISC = 3 * K1_STAGE;
constexpr int L_STATS = L_MISC;
constexpr int L_CS = L_MISC + 1024;
constexpr int L_WORDS = L_CS + 18 * 64;
constexpr int L_JOBS = L_WORDS + 64;
constexpr int L_DUMMY = L_WORDS + 128;
constexpr int L_TOTAL = L_DUMMY + 512;
#define YREP 1
constexpr int P_STAGE = 30720, P_NY = 16 * YREP, P_ND = 7, P_NST = P_NY + P_ND;
static_assert(L_TOTAL <= 163840 && 5 * P_STAGE <= L_MISC, "LDS");

__device__ __forceinline__ unsigned get_tag(lptr lds) { return *(volatile LAS unsigned*)(lds + L_WORDS + 16); }

struct Args {
  const u16 *x, *Cr, *Ci, *Dr, *Di, *Wg, *Wu, *Wd;
  char* ws;
  u16* out;
};

__device__ __forceinline__ void launder(Args& a, int& xcd, int& idx) {
  asm volatile("" : "+s"(a.x), "+s"(a.Cr), "+s"(a.Ci), "+s"(a.Dr), "+s"(a.Di));
  asm volatile("" : "+s"(a.Wg), "+s"(a.Wu), "+s"(a.Wd), "+s"(a.ws), "+s"(a.out));
  asm volatile("" : "+s"(xcd), "+s"(idx));
}

struct StartJobs { unsigned pay0, pay1; float ds0, ds1; };
template <int MODE>
__device__ __forceinline__ StartJobs start_jobs(lptr lds, const Args& a, int xcd, int idx, int mt, int grp, int h, int lane) {
  const int nq = xcd >> 1;

  const int srow = mt * 128 + 8 * grp + 2 * h;
  u32x4 v[4] = {{0, 0, 0, 0}, {0, 0, 0, 0}, {0, 0, 0, 0}, {0, 0, 0, 0}};
  if (lane < 56) {
    v[0] = *(const u32x4*)(a.x + (size_t)srow * D + lane * 8);
    v[1] = *(const u32x4*)(a.x + (size_t)(srow + 1) * D + lane * 8);
    const u16* dp = (nq >= 2 ? a.Di : a.Dr) + (size_t)(224 * (nq & 1) + 7 * idx + h) * D + lane * 8;
    v[2] = *(const u32x4*)dp;
    v[3] = *(const u32x4*)(dp + (h < 3 ? 4 * D : 0));
  }

  float f[4][8], sm[4];
#pragma unroll
  for (int r = 0; r < 4; ++r) {
#pragma unroll
    for (int i = 0; i < 4; ++i) { f[r][2 * i] = bf_lo(v[r][i]); f[r][2 * i + 1] = bf_hi(v[r][i]); }
    sm[r] = ((f[r][0] + f[r][1]) + (f[r][2] + f[r][3])) + ((f[r][4] + f[r][5]) + (f[r][6] + f[r][7]));
  }
  wave_sums<4>(sm);
  float q[2], mean[2];
#pragma unroll
  for (int r = 0; r < 2; ++r) {
    mean[r] = sm[r] * (1.0f / D);
    float t0 = 0.f, t1 = 0.f;
#pragma unroll
    for (int i = 0; i < 4; ++i) {
      const float d0 = f[r][2 * i] - mean[r], d1 = f[r][2 * i + 1] - mean[r];
      t0 = __builtin_fmaf(d0, d0, t0); t1 = __builtin_fmaf(d1, d1, t1);
    }
    q[r] = lane < 56 ? t0 + t1 : 0.f;
  }
  wave_sums<2>(q);
  StartJobs j;
  j.pay0 = pk_bf16(mean[0], rbf(__builtin_sqrtf(rbf(rbf(q[0] * (1.0f / D)) + 1e-5f))));
  j.pay1 = pk_bf16(mean[1], rbf(__builtin_sqrtf(rbf(rbf(q[1] * (1.0f / D)) + 1e-5f))));
  j.ds0 = sm[2]; j.ds1 = sm[3];
  return j;
}
__device__ __forceinline__ void stash_jobs(lptr lds, const StartJobs& j, int h, int lane) {
  if (lane == 0) *(LAS u32x4*)(lds + L_JOBS + h * 16) = u32x4{j.pay0, j.pay1, __float_as_uint(j.ds0), __float_as_uint(j.ds1)};
}
__device__ __forceinline__ void publish_jobs(lptr lds, char* ws, unsigned tag1, int xcd, int idx, int mt, int grp, int h, int lane) {
  const int nq = xcd >> 1;
  if (lane < 2 || lane == 2 || (lane == 3 && h < 3)) {
    const unsigned v = *(LAS unsigned*)(lds + L_JOBS + h * 16 + lane * 4);
    const size_t off = lane < 2 ? WS_ST + (size_t)(mt * 128 + 8 * grp + 2 * h + lane) * 8 : WS_CD + (size_t)(224 * nq + 7 * idx + h + 4 * (lane - 2)) * 8;
    __hip_atomic_store((GAS unsigned long long*)(ws + off), ((unsigned long long)tag1 << 32) | v, __ATOMIC_RELAXED, __HIP_MEMORY_SCOPE_AGENT);
  }
}

__device__ __forceinline__ void h_batch(const f32x4 (&g)[2][2], const f32x4 (&u)[2][2], const f32x4 (&cg)[2], const f32x4 (&cu)[2], const f32x2 (&st)[2],
                                        rsrc_t rh, int off0) {
  f32x2 gg[8], uu[8], z[8];
#pragma unroll
  for (int i = 0; i < 8; ++i) {
    const int t = i >> 2, j = (i >> 1) & 1, hh = i & 1;
    gg[i] = (f32x2{g[j][t][2 * hh], g[j][t][2 * hh + 1]} - f32x2{cg[j][2 * hh], cg[j][2 * hh + 1]} * st[t].x) * st[t].y;
  }
#pragma unroll
  for (int i = 0; i < 8; ++i) z[i] = gg[i] * ((gg[i] * gg[i]) * -0.1029432397f + -2.302208198f);
#pragma unroll
  for (int i = 0; i < 8; ++i) { z[i].x = __builtin_amdgcn_exp2f(z[i].x); z[i].y = __builtin_amdgcn_exp2f(z[i].y); }
#pragma unroll
  for (int i = 0; i < 8; ++i) {
    const int t = i >> 2, j = (i >> 1) & 1, hh = i & 1;
    uu[i] = ((f32x2{u[j][t][2 * hh], u[j][t][2 * hh + 1]} - f32x2{cu[j][2 * hh], cu[j][2 * hh + 1]} * st[t].x) * st[t].y) * gg[i];
    z[i] = z[i] + 1.0f;
  }
#pragma unroll
  for (int i = 0; i < 8; ++i) { z[i].x = __builtin_amdgcn_rcpf(z[i].x); z[i].y = __builtin_amdgcn_rcpf(z[i].y); }
#pragma unroll
  for (int i = 0; i < 8; ++i) uu[i] = uu[i] * z[i];
#pragma unroll
  for (int tl = 0; tl < 2; ++tl) {
    const u32x4 o = {pk_bf16(uu[4 * tl].x, uu[4 * tl].y), pk_bf16(uu[4 * tl + 1].x, uu[4 * tl + 1].y),
                     pk_bf16(uu[4 * tl + 2].x, uu[4 * tl + 2].y), pk_bf16(uu[4 * tl + 3].x, uu[4 * tl + 3].y)};
    __builtin_amdgcn_raw_buffer_store_b128(o, rh, off0 + tl * 56 * 1024, 0, STAUX);
  }
}
constexpr int L_HD = 61440;

template <int MODE, int NG, int NA>
__device__ __forceinline__ void p1_consumer(lptr lds, Args a, int xcd, int idx, int mt, int grp, int f0, int a0, int wm, int wn, int lane) {
  StartJobs jobs = start_jobs<MODE>(lds, a, xcd, idx, mt, grp, 2 * wn + wm, lane);
  stash_jobs(lds, jobs, 2 * wn + wm, lane);
  launder(a, xcd, idx);
  asm volatile("" : "+s"(mt), "+s"(grp), "+s"(f0), "+s"(a0));
  char* ws = a.ws;
  const char* cp = (const char*)((xcd >> 2) ? a.Ci : a.Cr) + (size_t)(1024 * (xcd & 1) + 32 * idx + 8 * (2 * wn + wm) + (lane >> 5)) * 4096 + 2048 * ((xcd >> 1) & 1) + (lane & 31) * 64;
  unsigned nonce = 0;
  if (MODE == 0 && wm == 0 && wn == 0) asm volatile("global_load_dword %0, %1, off sc1" : "+v"(nonce) : "v"(ws + WS_NONCE) : "memory");
  constexpr int NBK = 2 * NG, NCQ = NBK / 4, WSZ = NBK * 8 * 256, KSO = 4 * NCQ * 1024;
  constexpr int GO = K1_WO, UO = K1_WO + WSZ, DO = K1_WO + 2 * WSZ;
  const int i16 = lane & 15, g = lane >> 4, q = i16 >> 2, p = i16 & 3;
  const int rowx = 64 * wm + 8 * (i16 >> 2) + (i16 & 3);
  const int vx0 = rowx * 128 + ((g) ^ (i16 & 7)) * 16;
  const int vx1 = rowx * 128 + ((g + 4) ^ (i16 & 7)) * 16;
  const int fw = ((q >> 1) & 1) | ((g & 1) << 1);
  int vw[NG];
#pragma unroll
  for (int j = 0; j < NG; ++j) { const int nb = NG * wn + j; vw[j] = g * NCQ * 1024 + (nb >> 2) * 1024 + q * 128 + (((nb & 3) ^ fw) * 32) + p * 8; }
  const int vd0 = DO + (16 * NA * wn + i16) * 128 + ((g) ^ (i16 & 7)) * 16;
  const int vd1 = DO + (16 * NA * wn + i16) * 128 + ((g + 4) ^ (i16 & 7)) * 16;

  f32x4 G[NG][4], U[NG][4], AB[NA][4];
#pragma unroll
  for (int j = 0; j < NG; ++j)
#pragma unroll
    for (int t = 0; t < 4; ++t) { G[j][t] = f32x4{0, 0, 0, 0}; U[j][t] = f32x4{0, 0, 0, 0}; }
#pragma unroll
  for (int j = 0; j < NA; ++j)
#pragma unroll
    for (int t = 0; t < 4; ++t) AB[j][t] = f32x4{0, 0, 0, 0};

  bf16x8 X0[4], X1[4], WG[NG], WU[NG], WD[NA];
  auto ldx = [&](bf16x8 (&X)[4], lptr st, int ks) {
#pragma unroll
    for (int t = 0; t < 4; ++t) X[t] = ldsr128(st + (ks ? vx1 : vx0) + (32 * (t >> 1) + 4 * (t & 1)) * 128);
  };
  auto ldg = [&](int j, lptr st, int ks) { lptr b = st + GO + ks * KSO + vw[j]; WG[j] = ldstr(b, b + 512); };
  auto ldu = [&](int j, lptr st, int ks) { lptr b = st + UO + ks * KSO + vw[j]; WU[j] = ldstr(b, b + 512); };
  auto ldd = [&](int j, lptr st, int ks) { WD[j] = ldsr128(st + (ks ? vd1 : vd0) + j * 16 * 128); };
  auto grp4 = [&](f32x4 (&C)[4], const bf16x8& W, bf16x8 (&X)[4], bool wfirst, auto mid) {
    if (wfirst) { mfma_ip(C[0], W, X[0]); mfma_ip(C[1], W, X[1]); } else { mfma_ip(C[0], X[0], W); mfma_ip(C[1], X[1], W); }
    SBAR(); mid(); SBAR();
    if (wfirst) { mfma_ip(C[2], W, X[2]); mfma_ip(C[3], W, X[3]); } else { mfma_ip(C[2], X[2], W); mfma_ip(C[3], X[3], W); }
    SBAR();
  };
  auto fill = [&]() { FILL(); };
  auto ldx1 = [&](bf16x8 (&X)[4], lptr st, int ks, int t) { X[t] = ldsr128(st + (ks ? vx1 : vx0) + (32 * (t >> 1) + 4 * (t & 1)) * 128); };
  auto blockA = [&](lptr st) {
#pragma unroll
    for (int j = 0; j < NG; ++j) {
      grp4(G[j], WG[j], X0, true, [&]() { if (2 * j < 4) ldx1(X1, st, 1, 2 * j); else FILL(); }); ldg(j, st, 1); SBAR();
      grp4(U[j], WU[j], X0, true, [&]() { if (2 * j + 1 < 4) ldx1(X1, st, 1, 2 * j + 1); else FILL(); }); ldu(j, st, 1); SBAR();
    }
#pragma unroll
    for (int j = 0; j < NA; ++j) { grp4(AB[j], WD[j], X0, false, fill); ldd(j, st, 1); SBAR(); }
  };
  auto blockB_head = [&]() { grp4(G[0], WG[0], X1, true, fill); FILL(); grp4(U[0], WU[0], X1, true, fill); };
  auto blockB_rest = [&](lptr sn, bool reload) {
    int k = 0;
    auto mid = [&]() {
      if (!reload) FILL();
      else if (k == 0) ldg(0, sn, 0);
      else if (k == 1) ldu(0, sn, 0);
      else if (k < 6) ldx1(X0, sn, 0, k - 2);
      else FILL();
      ++k;
    };
#pragma unroll
    for (int j = 1; j < NG; ++j) {
      grp4(G[j], WG[j], X1, true, mid); if (reload) { ldg(j, sn, 0); SBAR(); } else FILL();
      grp4(U[j], WU[j], X1, true, mid); if (reload) { ldu(j, sn, 0); SBAR(); } else FILL();
    }
#pragma unroll
    for (int j = 0; j < NA; ++j) { grp4(AB[j], WD[j], X1, false, mid); if (reload) { ldd(j, sn, 0); SBAR(); } else FILL(); }
  };

#pragma unroll
  for (int j = 0; j < NG; ++j)
#pragma unroll
    for (int t = 0; t < 4; ++t) { asm volatile("" : "+v"(G[j][t])); asm volatile("" : "+v"(U[j][t])); }
#pragma unroll
  for (int j = 0; j < NA; ++j)
#pragma unroll
    for (int t = 0; t < 4; ++t) asm volatile("" : "+v"(AB[j][t]));
  PAD_MFMA_IN();
  STAMP(1);
  WAITL0(); BAR();
  STAMP(2);
  ldx(X0, lds, 0);
#pragma unroll
  for (int j = 0; j < NG; ++j) { ldg(j, lds, 0); ldu(j, lds, 0); }
#pragma unroll
  for (int j = 0; j < NA; ++j) ldd(j, lds, 0);
  SBAR();
  blockA(lds);
  blockB_head();
  if (wm == 0 && wn == 0) {
    WAITV(0);
    asm volatile("" : "+v"(nonce));
    if (lane == 0) *(volatile LAS unsigned*)(lds + L_WORDS + 16) = (MODE == 0) ? nonce * 0x9E3779B1u + 0x7F4A7C15u : 0x51u;
  }
  WAITL0(); STAMP(6); BAR();
  STAMP(7);
  int slot = 1;
#pragma unroll 1
  for (int s = 0; s < K1_NST - 1; ++s) {
    lptr sn = lds + slot * K1_STAGE;
    slot = (slot == 2) ? 0 : slot + 1;
    blockB_rest(sn, true);
    blockA(sn);
    if (s >= PF0 && s < PF0 + 4) { pf4(cp, lds_addr(lds) + L_DUMMY); cp += 2 * 4096; }
    blockB_head();
    WAITL0();
    STAMP(20 + s);
    BAR();
    STAMP(8 + s);
  }
#pragma unroll
  for (int j = 0; j < NA; ++j) { grp4(AB[j], WD[j], X1, false, fill); FILL(); }
  PAD_MFMA_OUT();
  STAMP(3);
  const lptr cs = lds + L_CS;
  const lptr stats = lds + L_STATS;
  {
    const rsrc_t rab = mk_rsrc(ws + WS_AB, 2 * 28 * 64 * 1024);
#pragma unroll
    for (int pi = 0; pi < 2; ++pi) {
      const lptr sp = stats + (64 * wm + 32 * pi + 8 * g) * 8;
      const f32x4 s0 = *(LAS f32x4*)(sp), s1 = *(LAS f32x4*)(sp + 16), s2 = *(LAS f32x4*)(sp + 32), s3 = *(LAS f32x4*)(sp + 48);
#pragma unroll
      for (int j = 0; j < NA; ++j) {
        const float c = *(LAS float*)(cs + (2 * NBK + NA * wn + j) * 64 + i16 * 4);
        const f32x4 a_ = AB[j][2 * pi], b = AB[j][2 * pi + 1];
        const int ks = mt * 4 + wm * 2 + pi;
        u32x4 o;
        o[0] = pk_bf16((a_[0] - s0[0] * c) * s0[1], (a_[1] - s0[2] * c) * s0[3]);
        o[1] = pk_bf16((a_[2] - s1[0] * c) * s1[1], (a_[3] - s1[2] * c) * s1[3]);
        o[2] = pk_bf16((b[0] - s2[0] * c) * s2[1], (b[1] - s2[2] * c) * s2[3]);
        o[3] = pk_bf16((b[2] - s3[0] * c) * s3[1], (b[3] - s3[2] * c) * s3[3]);
        __builtin_amdgcn_raw_buffer_store_b128(o, rab, lane * 16 + ((a0 + NA * wn + j) * 64 + ks) * 1024, 0, STAUX);
      }
    }
  }
  SBAR();
#pragma unroll
  for (int j = 1; j < NG; ++j) { grp4(G[j], WG[j], X1, true, fill); FILL(); grp4(U[j], WU[j], X1, true, fill); FILL(); }
  PAD_MFMA_OUT();
  {
    lptr hd = lds + L_HD + (2 * wn + wm) * (NG * 4096) + lane * 16;
#pragma unroll
    for (int j = 0; j < NG; ++j)
#pragma unroll
      for (int tl = 0; tl < 2; ++tl) {
        *(LAS f32x4*)(hd + ((j * 2 + 0) * 2 + tl) * 1024) = G[j][2 + tl];
        *(LAS f32x4*)(hd + ((j * 2 + 1) * 2 + tl) * 1024) = U[j][2 + tl];
      }
  }
  WAITV(0);
  if (lane == 0) {
    const unsigned n = __hip_atomic_fetch_add((LAS unsigned*)(lds + L_WORDS), 1u, __ATOMIC_RELAXED, __HIP_MEMORY_SCOPE_WORKGROUP);
    if (n == 3u) st_flag(ws + WS_FAB + (mt * 16 + grp) * 64, get_tag(lds));
  }
  STAMP(5);
  WAITL0(); BAR();
  STAMP(14);
  const rsrc_t rh = mk_rsrc(ws + WS_H, 128 * 56 * 1024);
  const int tt0 = mt * 8 + wm * 4;
  f32x2 st[2];
#pragma unroll
  for (int t = 0; t < 2; ++t) st[t] = *(LAS f32x2*)(stats + (64 * wm + 8 * (i16 >> 2) + 4 * t + (i16 & 3)) * 8);
#pragma unroll
  for (int pp = 0; pp < NG / 2; ++pp) {
    const int ks = ((f0 >> 4) + NG * wn + 2 * pp) >> 1;
    f32x4 cg[2], cu[2], gq[2][2], uq[2][2];
#pragma unroll
    for (int j = 0; j < 2; ++j) {
      cg[j] = *(LAS f32x4*)(cs + (NG * wn + 2 * pp + j) * 64 + g * 16);
      cu[j] = *(LAS f32x4*)(cs + (NBK + NG * wn + 2 * pp + j) * 64 + g * 16);
      gq[j][0] = G[2 * pp + j][0]; gq[j][1] = G[2 * pp + j][1]; uq[j][0] = U[2 * pp + j][0]; uq[j][1] = U[2 * pp + j][1];
    }
    h_batch(gq, uq, cg, cu, st, rh, lane * 16 + (tt0 * 56 + ks) * 1024);
  }
  STAMP(4);
}

template <int NG>
__device__ __forceinline__ void p1_helper_epilogue(lptr lds, char* ws, int mt, int f0, int h, int lane) {
  constexpr int NBK = 2 * NG;
  const int wm = h & 1, wn = h >> 1, i16 = lane & 15, g = lane >> 4;
  const lptr cs = lds + L_CS, stats = lds + L_STATS;
  const rsrc_t rh = mk_rsrc(ws + WS_H, 128 * 56 * 1024);
  const int tt0 = mt * 8 + wm * 4 + 2;
  lptr hd = lds + L_HD + h * (NG * 4096) + lane * 16;
  f32x2 st[2];
#pragma unroll
  for (int t = 0; t < 2; ++t) st[t] = *(LAS f32x2*)(stats + (64 * wm + 32 + 8 * (i16 >> 2) + 4 * t + (i16 & 3)) * 8);
#pragma unroll
  for (int pp = 0; pp < NG / 2; ++pp) {
    const int ks = ((f0 >> 4) + NG * wn + 2 * pp) >> 1;
    f32x4 cg[2], cu[2], gq[2][2], uq[2][2];
#pragma unroll
    for (int j = 0; j < 2; ++j) {
      cg[j] = *(LAS f32x4*)(cs + (NG * wn + 2 * pp + j) * 64 + g * 16);
      cu[j] = *(LAS f32x4*)(cs + (NBK + NG * wn + 2 * pp + j) * 64 + g * 16);
#pragma unroll
      for (int tl = 0; tl < 2; ++tl) {
        gq[j][tl] = *(LAS f32x4*)(hd + (((2 * pp + j) * 2 + 0) * 2 + tl) * 1024);
        uq[j][tl] = *(LAS f32x4*)(hd + (((2 * pp + j) * 2 + 1) * 2 + tl) * 1024);
      }
    }
    h_batch(gq, uq, cg, cu, st, rh, lane * 16 + (tt0 * 56 + ks) * 1024);
  }
}

template <int MODE, bool TB>
__device__ __forceinline__ void p1_helper(lptr lds, Args a, int xcd, int idx, int mt, int grp, int f0, int a0, int h, int lane) {
  constexpr int NBK = TB ? 4 : 8, WSZ = NBK * 8 * 256;
  constexpr int NWP = TB ? 4 : 8, NDP = TB ? 4 : 1, PER = 4 + NWP + NDP;
  const int mh = xcd & 1, nq = xcd >> 1;
  const int up = h >> 1;
  const u32x4 rx = mk_srd(a.x, S * D * 2);
  const u32x4 rw = mk_srd(up ? a.Wu : a.Wg, D * F * 2);
  const u32x4 rd = mk_srd(a0 >= 28 ? a.Di : a.Dr, D * D * 2);
  const int e0 = (a0 >= 28 ? a0 - 28 : a0) * 16;
  const int r8 = lane >> 3, c8 = lane & 7;
  int vx[2];
  vx[0] = r8 * 896 + (c8 ^ (r8 & 3)) * 16;
  vx[1] = r8 * 896 + (c8 ^ ((r8 & 3) | 4)) * 16;
  const int vd = r8 * 896 + (c8 ^ r8) * 16;
  int vw[2];
  {
    const int f0_ = (r8 >> 1) & 1;
    vw[0] = r8 * (F * 2) + f0 * 2 + ((((c8 >> 1) ^ f0_) * 2) + (c8 & 1)) * 16;
    vw[1] = r8 * (F * 2) + f0 * 2 + ((((c8 >> 1) ^ (f0_ | 2)) * 2) + (c8 & 1)) * 16;
  }
  const unsigned l0 = lds_addr(lds);
  const unsigned dummy = l0 + L_DUMMY + (h & 1) * 256;
  const int sxb = (mt * 128 + 32 * h) * 896, swb = 32 * (h & 1) * (F * 2), sdb = (e0 + 8 * NDP * h) * 896;
  const unsigned lxb = l0 + 4 * h * 1024, lwb = l0 + K1_WO + up * WSZ + NWP * (h & 1) * 1024, ldb = l0 + K1_WO + 2 * WSZ + NDP * h * 1024;
  auto piece = [&](int s, int slot, int i) {
    const unsigned so = slot * K1_STAGE;
    if (i < 4) dma16(rx, lxb + so + i * 1024, vx[i & 1], sxb + s * 128 + i * 8 * 896);
    else if (i < 4 + NWP) {
      const int j = i - 4, kb = TB ? j : j >> 1, cq = TB ? 0 : j & 1;
      dma16(rw, lwb + so + j * 1024, vw[kb & 1], swb + s * 64 * (F * 2) + kb * 8 * (F * 2) + cq * 128);
    } else dma16(rd, ldb + so + (i - 4 - NWP) * 1024, vd, sdb + s * 128 + (i - 4 - NWP) * 8 * 896);
  };
  auto issue_a = [&](int s, int slot) {
#pragma unroll
    for (int i = 0; i < 8; ++i) piece(s, slot, i);
  };
  auto issue_b = [&](int s, int slot) {
#pragma unroll
    for (int i = 8; i < PER; ++i) piece(s, slot, i);
  };
  const int ln2 = lane >> 1, hb = (lane & 1) * 64;
  const char* pfx = (const char*)a.x + (size_t)(1024 * mh + 32 * idx + ln2) * 896 + hb;
  STAMP(1);
  issue_a(0, 0); issue_b(0, 0);
  STAMP(5);
  launder(a, xcd, idx);
  char* ws = a.ws;
  const bool wjob = idx < 28;
  const int wj_mat = idx >= 14, wj_cb = (idx - 14 * wj_mat) >> 1, wj_kh = idx & 1;
  u32x4 wx[7];
  {
    const u32x4 rj = mk_srd(wj_mat ? a.Wu : a.Wg, D * F * 2);
    const int vj = r8 * (F * 2) + c8 * 16 + (448 * nq + 64 * (wjob ? wj_cb : 0)) * 2;
    const int sj = (224 * (wjob ? wj_kh : 0) + 56 * h) * (F * 2);
    asm volatile("buffer_load_dwordx4 %0, %7, %8, %9 offen\n\tbuffer_load_dwordx4 %1, %7, %8, %10 offen\n\tbuffer_load_dwordx4 %2, %7, %8, %11 offen\n\t"
                 "buffer_load_dwordx4 %3, %7, %8, %12 offen\n\tbuffer_load_dwordx4 %4, %7, %8, %13 offen\n\tbuffer_load_dwordx4 %5, %7, %8, %14 offen\n\t"
                 "buffer_load_dwordx4 %6, %7, %8, %15 offen"
                 : "=&v"(wx[0]), "=&v"(wx[1]), "=&v"(wx[2]), "=&v"(wx[3]), "=&v"(wx[4]), "=&v"(wx[5]), "=&v"(wx[6])
                 : "v"(vj), "s"(rj), "s"(sj), "s"(sj + 8 * F * 2), "s"(sj + 16 * F * 2), "s"(sj + 24 * F * 2), "s"(sj + 32 * F * 2), "s"(sj + 40 * F * 2), "s"(sj + 48 * F * 2)
                 : "memory");
  }
  pf4(pfx + (1 + h) * 128, dummy);
  if (h == 3 && lane < 4) ((LAS unsigned*)(lds + L_WORDS))[lane] = 0u;
  WAITL0(); WAITV(8);
  STAMP(7); BAR();
  STAMP(2);
#pragma unroll
  for (int i = 0; i < PER; ++i) piece(1, 1, i);
  issue_a(2, 2);
  if (h < 2) pf4(pfx + (5 + h) * 128, dummy);
  WAITL0();
  if (h < 2) { WAITV(9); } else { WAITV(8); }
  asm volatile("" : "+v"(wx[0]), "+v"(wx[1]), "+v"(wx[2]), "+v"(wx[3]), "+v"(wx[4]), "+v"(wx[5]), "+v"(wx[6]));
  BAR();
  const unsigned tag1 = get_tag(lds);
  publish_jobs(lds, ws, tag1, xcd, idx, mt, grp, h, lane);
  float cw[8];
#pragma unroll
  for (int k = 0; k < 8; ++k) cw[k] = 0.f;
  constexpr bool TBc = TB;
  unsigned long long g0 = 0, g1 = 0, g2 = 0, g3 = 0;
  const bool wdjob = blockIdx.x < 196;
  const int wd_kp = blockIdx.x % 28, wd_cg = blockIdx.x / 28;
#pragma unroll
  for (int s = 1; s < K1_NST; ++s) {
    const int s1_ = (s + 1) % 3, s2_ = (s + 2) % 3;
    if (s == 1) {
#pragma unroll
      for (int i = 0; i < PER; ++i) {
        if (i < PER - 8) piece(2, s1_, 8 + i); else piece(3, s2_, i - (PER - 8));
        SBAR();
        if (i < 7) {
#pragma unroll
          for (int k = 0; k < 4; ++k) { cw[2 * k] += bf_lo(wx[i][k]); cw[2 * k + 1] += bf_hi(wx[i][k]); }
        }
        SBAR();
      }
    } else {
      if (s + 1 < K1_NST) issue_b(s + 1, s1_);

      if (s + 2 < K1_NST) issue_a(s + 2, s2_);
    }
    if (s == 1) {
      pf4((const char*)a.Wd + (size_t)(64 * wd_kp + 16 * h + ((lane >> 1) & 15)) * 896 + (wdjob ? wd_cg : 0) * 128 + (lane & 1) * 64, dummy);
#pragma unroll
      for (int k = 0; k < 8; ++k) {
        cw[k] += __builtin_bit_cast(float, __builtin_amdgcn_update_dpp(0, __builtin_bit_cast(int, cw[k]), 0x128, 0xf, 0xf, true));
        const auto p16 = __builtin_amdgcn_permlane16_swap(__float_as_uint(cw[k]), __float_as_uint(cw[k]), false, false);
        cw[k] = __uint_as_float(p16[0]) + __uint_as_float(p16[1]);
        const auto p32 = __builtin_amdgcn_permlane32_swap(__float_as_uint(cw[k]), __float_as_uint(cw[k]), false, false);
        cw[k] = __uint_as_float(p32[0]) + __uint_as_float(p32[1]);
      }
      if (lane < 8) {
        *(LAS f32x4*)(lds + L_CS + h * 256 + lane * 32) = f32x4{cw[0], cw[1], cw[2], cw[3]};
        *(LAS f32x4*)(lds + L_CS + h * 256 + lane * 32 + 16) = f32x4{cw[4], cw[5], cw[6], cw[7]};
      }
    }
    if (s == 2 && h == 0 && wjob) {
      const float t = (*(LAS float*)(lds + L_CS + lane * 4) + *(LAS float*)(lds + L_CS + 256 + lane * 4)) +
                      (*(LAS float*)(lds + L_CS + 512 + lane * 4) + *(LAS float*)(lds + L_CS + 768 + lane * 4));
      __hip_atomic_store((GAS unsigned long long*)(ws + WS_CW + (size_t)((wj_mat * 2 + wj_kh) * F + 448 * nq + 64 * wj_cb + lane) * 8),
                         ((unsigned long long)tag1 << 32) | __float_as_uint(t), __ATOMIC_RELAXED, __HIP_MEMORY_SCOPE_AGENT);
    }
    if (s == 4 && h == 0 && lane == 0) st_flag(ws + WS_ARRIVE + blockIdx.x * 64, tag1);
    if (s == 5) {
      if (wdjob) {
        const u32x4 rwd = mk_srd(a.Wd, F * D * 2);
        const int rs = (lane & 15) >> 1, half = lane & 1, b = lane >> 4;
        const int vq = rs * 896 + (64 * wd_cg + 16 * b + 8 * half) * 2;
#pragma unroll
        for (int i = 0; i < 2; ++i) dma16(rwd, l0 + s2_ * K1_STAGE + (2 * h + i) * 1024, vq, (64 * wd_kp + 8 * (2 * h + i)) * 896);
      }
    }
    const int gc = 64 * h + lane;
    constexpr int NWC = TBc ? 128 : 256, CPM = NWC / 2;
    const bool isw = gc < NWC;
    const char* p_st = ws + WS_ST + (size_t)(mt * 128 + 32 * h + (lane & 31)) * 8;
    const char* p_c0 = ws + (isw ? WS_CW + (size_t)((gc / CPM) * 2 * F + f0 + gc % CPM) * 8 : WS_CD + (size_t)(a0 * 16 + (gc - NWC)) * 8);
    const char* p_c1 = isw ? p_c0 + (size_t)F * 8 : p_c0;
    const char* p_d = ws + WS_CD + (size_t)(a0 * 16 + (lane & 31)) * 8;
    if (s == 5)
      asm volatile("global_load_dwordx2 %0, %4, off sc1\n\tglobal_load_dwordx2 %1, %5, off sc1\n\tglobal_load_dwordx2 %2, %6, off sc1\n\tglobal_load_dwordx2 %3, %7, off sc1"
                   : "=&v"(g0), "=&v"(g1), "=&v"(g2), "=&v"(g3) : "v"(p_st), "v"(p_c0), "v"(p_c1), "v"(p_d) : "memory");
    if (s == 6) {
      WAITV(0);
      asm volatile("" : "+v"(g0), "+v"(g1), "+v"(g2), "+v"(g3));
      auto ld_gran = [&](const char* p) { return __hip_atomic_load((const GAS unsigned long long*)p, __ATOMIC_RELAXED, __HIP_MEMORY_SCOPE_AGENT); };
      for (unsigned sp = 0; sp < SPIN_MAX; ++sp) {
        if (__all((unsigned)(g0 >> 32) == tag1 && (unsigned)(g1 >> 32) == tag1 && (unsigned)(g2 >> 32) == tag1 && (unsigned)(g3 >> 32) == tag1)) break;
        __builtin_amdgcn_s_sleep(2);
        g0 = ld_gran(p_st); g1 = ld_gran(p_c0); g2 = ld_gran(p_c1); g3 = ld_gran(p_d);
      }
      if (lane < 32) *(LAS f32x2*)(lds + L_STATS + (32 * h + lane) * 8) = f32x2{bf_lo((unsigned)g0), 1.0f / bf_hi((unsigned)g0)};
      *(LAS float*)(lds + L_CS + gc * 4) = isw ? __uint_as_float((unsigned)g1) + __uint_as_float((unsigned)g2) : __uint_as_float((unsigned)g1);
      if (!TBc && h == 0 && lane < 32) *(LAS float*)(lds + L_CS + (256 + lane) * 4) = __uint_as_float((unsigned)g3);
    }
    WAITL0();
    if (s == 1) { WAITV(9); } else if (s + 2 < K1_NST) { WAITV(8); } else if (s == 5) { if (wdjob) { WAITV(6); } else { WAITV(4); } }
    STAMP(7 + s);
    BAR();
  }
  __builtin_amdgcn_s_setprio(0);
  if (h == 3 && wdjob) {
    const int i16 = lane & 15, g = lane >> 4, q = i16 >> 2, p = i16 & 3;
    const rsrc_t rt = mk_rsrc(ws + WS_WDT, 28 * 56 * 1024);
    lptr b0 = lds + 1 * K1_STAGE + (g >> 1) * 1024 + ((g & 1) * 4 + q) * 32 + p * 8;
#pragma unroll
    for (int kk = 0; kk < 2; ++kk)
#pragma unroll
      for (int u = 0; u < 4; ++u) {
        const bf16x8 f = ldstr(b0 + kk * 4096 + u * 256, b0 + kk * 4096 + u * 256 + 2048);
        __builtin_amdgcn_raw_buffer_store_b128(__builtin_bit_cast(u32x4, f), rt, lane * 16 + ((4 * wd_cg + u) * 56 + 2 * wd_kp + kk) * 1024, 0, STAUX);
      }
    WAITL0();
  }
  BAR();
  p1_helper_epilogue<TB ? 2 : 4>(lds, ws, mt, f0, h, lane);
}

template <int MODE, int NB, int JO>
__device__ __forceinline__ void p2_loader(lptr lds, const Args& a, int xcd, int idx, int r, int m, int n, int lw, int lane) {
  char* ws = a.ws;
  constexpr int PER = 4 + NB;
  const u32x4 rc = mk_srd((r >> 1) ? a.Ci : a.Cr, S * S * 2);
  const u32x4 rb = mk_srd(ws + WS_AB, 2 * 28 * 64 * 1024);
  const u32x4 rh = mk_srd(ws + WS_H, 128 * 56 * 1024);
  const u32x4 rw = mk_srd(ws + WS_WDT, 28 * 56 * 1024);
  const int r8 = lane >> 3, c8 = lane & 7;
  int va[2];
  va[0] = r8 * 4096 + (c8 ^ (r8 & 3)) * 16;
  va[1] = r8 * 4096 + (c8 ^ ((r8 & 3) | 4)) * 16;
  const int vl = lane * 16;
  const int k0 = 1024 * (r & 1), p = r >> 1;
  const int j0 = (lw < 2) ? 4 * lw : 8 + 3 * (lw - 2);
  const unsigned l0 = lds_addr(lds);
  const unsigned dummy = l0 + L_DUMMY + (lw & 1) * 256;
  const int sab = (128 * m + 32 * lw) * 4096 + k0 * 2;
  const int shb = ((8 * m + 2 * lw) * 56 + 14 * r) * 1024;
  const unsigned lab = l0 + 4 * lw * 1024, lbb = l0 + 16384 + j0 * 1024;
  const int sbb = ((p * 28 + 7 * n + (j0 >> 1)) * 64 + (k0 >> 5)) * 1024, swb = ((7 * n + (j0 >> 1)) * 56 + 14 * r) * 1024;
  auto issueAy = [&](int s, int slot) {
    const unsigned so = slot * P_STAGE; const int b = sab + 128 * (s & 15);
#pragma unroll
    for (int i = 0; i < 4; ++i) dma16(rc, lab + so + i * 1024, va[i & 1], b + i * 8 * 4096);
  };
  auto issueAd = [&](int sd, int slot) {
    const unsigned so = slot * P_STAGE; const int b = shb + 2048 * sd;
#pragma unroll
    for (int i = 0; i < 4; ++i) dma16c(rh, lab + so + i * 1024, vl, b + (i >> 1) * 56 * 1024 + (i & 1) * 1024);
  };
  auto issueBy = [&](int s, int slot) {
    const unsigned so = slot * P_STAGE; const int b = sbb + 2048 * (s & 15);
#pragma unroll
    for (int j = 0; j < NB; ++j) dma16c(rb, lbb + so + j * 1024, vl, b + ((j + JO) >> 1) * 65536 + ((j + JO) & 1) * 1024);
  };
  auto issueBd = [&](int sd, int slot) {
    const unsigned so = slot * P_STAGE; const int b = swb + 2048 * sd;
#pragma unroll
    for (int j = 0; j < NB; ++j) dma16c(rw, lbb + so + j * 1024, vl, b + ((j + JO) >> 1) * 57344 + ((j + JO) & 1) * 1024);
  };
  __builtin_amdgcn_s_setprio(0);
  const unsigned tag = get_tag(lds);
  if (MODE != 2 && lw == 3 && blockIdx.x < 196) {
    WAITV(0);
    if (lane == 0) st_flag(ws + WS_FWD + blockIdx.x * 64, tag);
  }
  issueAy(0, 0); issueAy(1, 1);
  if (lw == 3) {
    const int mtp = 8 * (r & 1) + (lane & 7), j = (lane >> 3) & 3;
    const bool act = lane < 32 && ((n & 1) == 0 || j == 3);
    poll_all(ws + WS_FAB + (mtp * 16 + (2 * p + (n >> 1)) * 4 + j) * 64, act, tag);
    if (lane == 0) *(volatile LAS unsigned*)(lds + L_WORDS + 8) = 1u;
  } else lds_spin(lds + L_WORDS + 8);
  STAMP(16);
  issueBy(0, 0);
  {
    const int l = (lane >> 1) < 28 ? (lane >> 1) : 27;
    const int sl = l >= 14, li = 14 * idx + l - 14 * sl;
    const char* bp = ws + WS_AB + ((size_t)(p * 28 + (li >> 4)) * 64 + 32 * (r & 1) + 2 * (4 * lw + sl) + ((li >> 3) & 1)) * 1024 + (li & 7) * 128 + (lane & 1) * 64;
    pf4c(bp, dummy); pf4c(bp + 4 * 1024, dummy);
  }
  issueBy(1, 1);
  __builtin_amdgcn_s_setprio(3);
  WAITV(NB + 2); BAR();
  if (MODE == 0 && lane == 0) {
    const unsigned cnt = __hip_atomic_fetch_add((LAS unsigned*)(lds + L_WORDS + 4), 1u, __ATOMIC_RELAXED, __HIP_MEMORY_SCOPE_WORKGROUP);
    if (cnt == 7u) st_flag(ws + WS_FH + (((xcd & 1) * 8 + (idx & 7)) * 16 + (xcd >> 1) * 4 + (idx >> 3)) * 64, tag);
  }
  issueAy(2, 2); issueBy(2, 2);
  WAITV(PER); BAR();
  issueAy(3, 3); issueBy(3, 3); issueAy(4, 4); issueBy(4, 4);
  WAITV(2 * PER); BAR();
  int slot = 0;
  auto ystep = [&](int s) { issueAy(s + 3, slot); issueBy(s + 3, slot); slot = (slot == 4) ? 0 : slot + 1; WAITV(2 * PER); BAR(); };
  auto dstep = [&](int sd) { issueAd(sd, slot); issueBd(sd, slot); slot = (slot == 4) ? 0 : slot + 1; WAITV(2 * PER); BAR(); };
#pragma unroll 1
  for (int s = 2; s < P_NY - 9; ++s) ystep(s);
  const int cg0 = (112 * n) >> 6, cg1 = (112 * n + 111) >> 6, jj = lane - 4;
  const bool fact = lane < 4 || (jj < 21 && cg0 + jj / 7 <= cg1);
  const char* fp = lane < 4 ? ws + WS_FH + (m * 16 + 4 * r + lane) * 64 : ws + WS_FWD + ((fact ? cg0 + jj / 7 : cg0) * 28 + 7 * r + (fact ? jj % 7 : 0)) * 64;
  unsigned fval = 0;
  if (lw == 3) asm volatile("global_load_dword %0, %1, off sc1" : "+v"(fval) : "v"(fp) : "memory");
#pragma unroll 1
  for (int s = P_NY - 9; s < P_NY - 6; ++s) ystep(s);
  if (lw == 3) {
    asm volatile("" : "+v"(fval));
    if (!__all(!fact || fval == tag)) poll_all(fp, fact, tag);
    if (lane == 0) *(volatile LAS unsigned*)(lds + L_WORDS + 12) = 1u;
  }
#pragma unroll 1
  for (int s = P_NY - 6; s < P_NY - 3; ++s) ystep(s);
  if (lw != 3) lds_spin(lds + L_WORDS + 12);
#pragma unroll 1
  for (int sd = 0; sd < P_ND; ++sd) dstep(sd);
  WAITV(PER); BAR();
  WAITV(0); BAR();
}

template <int MODE>
__global__ __launch_bounds__(512, 2) void fused_kernel(Args a) {
  extern __shared__ __attribute__((aligned(1024))) char smem[];
  lptr lds = (lptr)smem;
  char* ws = a.ws;
  const int tid = threadIdx.x, lane = tid & 63;
  const int wid = __builtin_amdgcn_readfirstlane(tid >> 6);
  int xcd = blockIdx.x & 7, idx = blockIdx.x >> 3;
  STAMP(0); STAMPR(30);
  if (wid == 3) {
    unsigned long long pc;
    asm volatile("s_getpc_b64 %0" : "=s"(pc));
    const unsigned ln = 18 * idx + ((lane >> 1) < 18 ? (lane >> 1) : 17);
    pf4((const char*)(pc & ~127ull) + (ln * 128 < CODE_PF ? ln * 128 : 0) + (lane & 1) * 64, lds_addr(lds) + L_DUMMY);
  }
  if (wid >= 4) __builtin_amdgcn_s_setprio(3);

  if (MODE != 2) {
    const int nq = xcd >> 1;
    const int mt = (xcd & 1) * 8 + (idx & 7), j = idx >> 3;
    const bool tb = j == 3;
    const int grp = nq * 4 + j;
    const int f0 = 448 * nq + 128 * j, a0 = 14 * nq + 2 * j;
    if (wid < 4) {
      if (tb) p1_consumer<MODE, 2, 4>(lds, a, xcd, idx, mt, grp, f0, a0, wid & 1, wid >> 1, lane);
      else p1_consumer<MODE, 4, 1>(lds, a, xcd, idx, mt, grp, f0, a0, wid & 1, wid >> 1, lane);
    } else {
      const int h = wid - 4;
      if (tb) p1_helper<MODE, true>(lds, a, xcd, idx, mt, grp, f0, a0, h, lane);
      else p1_helper<MODE, false>(lds, a, xcd, idx, mt, grp, f0, a0, h, lane);
    }
  }
  if (MODE == 1) return;
  if (MODE == 2) { if (tid < 4) ((LAS unsigned*)(lds + L_WORDS))[tid] = 0u; if (tid == 4) ((LAS unsigned*)(lds + L_WORDS))[4] = 0x51u; __syncthreads(); }

  launder(a, xcd, idx);
  ws = a.ws;
  const int p1_mt = (xcd & 1) * 8 + (idx & 7), p1_grp = (xcd >> 1) * 4 + (idx >> 3);
  const int r = xcd >> 1, m = 8 * (xcd & 1) + (idx & 7), n = idx >> 3;
  const int tile = m * 4 + n;
  const int i16 = lane & 15, g = lane >> 4;
  f32x4 accY[7][2], acc[7][2];
  if (wid == 7) {
    p2_loader<MODE, 3, 1>(lds, a, xcd, idx, r, m, n, 3, lane);
  } else if (wid == 6) {
    p2_loader<MODE, 3, 0>(lds, a, xcd, idx, r, m, n, 2, lane);
  } else if (wid >= 4) {
    p2_loader<MODE, 4, 0>(lds, a, xcd, idx, r, m, n, wid - 4, lane);
  } else {
    const int w = wid;
    const int rowt = 32 * w + 8 * (i16 >> 2) + (i16 & 3);
    const int vt0 = rowt * 128 + ((g) ^ (i16 & 7)) * 16, vt1 = rowt * 128 + ((g + 4) ^ (i16 & 7)) * 16;
    const int ve = 16384 + lane * 16;
    const int vh = (4 * w) * 1024 + lane * 16;
    bf16x8 T0[4], T1[4], E[14];
    auto ldTy = [&](bf16x8 (&T)[4], lptr st) {
      T[0] = ldsr128(st + vt0); T[1] = ldsr128(st + vt0 + 512); T[2] = ldsr128(st + vt1); T[3] = ldsr128(st + vt1 + 512);
    };
    auto ldTd = [&](bf16x8 (&T)[4], lptr st) {
      T[0] = ldsr128(st + vh); T[1] = ldsr128(st + vh + 2048); T[2] = ldsr128(st + vh + 1024); T[3] = ldsr128(st + vh + 3072);
    };
    auto ldE = [&](int j, lptr st) { E[j] = ldsr128(st + ve + ((j % 7) * 2 + j / 7) * 1024); };
    auto mm = [&](int j, bf16x8 (&T)[4]) {
      mfma_ip(acc[j % 7][0], E[j], T[(j / 7) * 2]);
      mfma_ip(acc[j % 7][1], E[j], T[(j / 7) * 2 + 1]);
    };
#define GHEAD 3
    constexpr int GH = GHEAD;
    auto ldT1 = [&](bf16x8 (&T)[4], lptr st, int i, int NT) {
      if (NT == 0) T[i] = ldsr128(st + (i >= 2 ? vt1 : vt0) + (i & 1) * 512);
      else T[i] = ldsr128(st + vh + (i & 1) * 2048 + (i >> 1) * 1024);
    };
    auto rest = [&](bf16x8 (&Tc)[4], bf16x8 (&Tn)[4], lptr sn, int NT) {
#pragma unroll
      for (int j = GH; j < 14; ++j) {
        const int k = j - GH;
        mfma_ip(acc[j % 7][0], E[j], Tc[(j / 7) * 2]); SBAR();
        if (NT != 2 && k < 4 + GH) { if (k < 4) ldT1(Tn, sn, k, NT); else ldE(k - 4, sn); SBAR(); }
        mfma_ip(acc[j % 7][1], E[j], Tc[(j / 7) * 2 + 1]); SBAR();
        if (NT != 2) ldE(j, sn); else FILL();
        SBAR();
      }
    };
    auto head = [&](bf16x8 (&Tc)[4]) {
#pragma unroll
      for (int j = 0; j < GH; ++j) { mm(j, Tc); FILL(); }
      SBAR();
    };
#pragma unroll
    for (int u = 0; u < 7; ++u) { acc[u][0] = f32x4{0, 0, 0, 0}; acc[u][1] = f32x4{0, 0, 0, 0}; }
#pragma unroll
    for (int u = 0; u < 7; ++u) { asm volatile("" : "+v"(acc[u][0])); asm volatile("" : "+v"(acc[u][1])); }
    PAD_MFMA_IN();
    if (w != r) {
      const rsrc_t rp = mk_rsrc(ws + WS_PD + (size_t)((tile * 4 + r) * 4 + w) * 7168, 7168);
      const u32x4 sent = {0xffffffffu, 0xffffffffu, 0xffffffffu, 0xffffffffu};
#pragma unroll
      for (int u = 0; u < 7; ++u) {
        __builtin_amdgcn_raw_buffer_store_b128(sent, rp, u * 1024 + lane * 16, 0, 16);
      }
    }
    STAMP(16); STAMPR(28);
    BAR();
    STAMP(17); STAMPR(27);
    ldTy(T0, lds);
#pragma unroll
    for (int j = 0; j < 14; ++j) ldE(j, lds);
    SBAR();
    head(T0);
    WAITL0(); BAR();
    int slot = 1;
#pragma unroll 1
    for (int s = 0; s < P_NY - 2; s += 2) {
      lptr sn = lds + slot * P_STAGE; slot = (slot == 4) ? 0 : slot + 1;
      rest(T0, T1, sn, 0); head(T1);
      WAITL0(); BAR();
      sn = lds + slot * P_STAGE; slot = (slot == 4) ? 0 : slot + 1;
      rest(T1, T0, sn, 0); head(T0);
      WAITL0(); BAR();
      if (YREP > 1 && s == 14) { STAMP(23); }
      if (MODE == 0 && s == 0) {
        WAITV(0);
        if (lane == 0) {
          const unsigned cnt = __hip_atomic_fetch_add((LAS unsigned*)(lds + L_WORDS + 4), 1u, __ATOMIC_RELAXED, __HIP_MEMORY_SCOPE_WORKGROUP);
          if (cnt == 7u) st_flag(ws + WS_FH + (p1_mt * 16 + p1_grp) * 64, get_tag(lds));
        }
      }
    }
    {
      lptr sn = lds + slot * P_STAGE; slot = (slot == 4) ? 0 : slot + 1;
      rest(T0, T1, sn, 0); head(T1);
      WAITL0(); BAR();
      sn = lds + slot * P_STAGE; slot = (slot == 4) ? 0 : slot + 1;
      rest(T1, T0, sn, 1);
    }
    PAD_MFMA_OUT();
    STAMP(18);
    {
      const float sg = (r >> 1) ? -1.0f : 1.0f;
#pragma unroll
      for (int u = 0; u < 7; ++u) { accY[u][0] = acc[u][0] * sg; accY[u][1] = acc[u][1] * sg; acc[u][0] = f32x4{0, 0, 0, 0}; acc[u][1] = f32x4{0, 0, 0, 0}; }
      if (w != r) {
        const rsrc_t rp = mk_rsrc(ws + WS_PY + (size_t)((tile * 4 + r) * 4 + w) * 7168, 7168);
#pragma unroll
        for (int u = 0; u < 7; ++u) {
          const u32x4 o = {pk_bf16(accY[u][0][0], accY[u][0][1]), pk_bf16(accY[u][0][2], accY[u][0][3]),
                           pk_bf16(accY[u][1][0], accY[u][1][1]), pk_bf16(accY[u][1][2], accY[u][1][3])};
          __builtin_amdgcn_raw_buffer_store_b128(o, rp, u * 1024 + lane * 16, 0, 16);
        }
      }
    }
#pragma unroll
    for (int u = 0; u < 7; ++u) { asm volatile("" : "+v"(acc[u][0])); asm volatile("" : "+v"(acc[u][1])); }
    PAD_MFMA_IN();
    head(T0);
    WAITL0(); BAR();
#pragma unroll 1
    for (int s = 0; s < P_ND - 1; s += 2) {
      lptr sn = lds + slot * P_STAGE; slot = (slot == 4) ? 0 : slot + 1;
      rest(T0, T1, sn, 1); head(T1);
      WAITL0(); BAR();
      if (s == 0 && w != r) {
        WAITV(0);
        const unsigned tag = get_tag(lds);
        if (lane == 0) st_flag(ws + WS_FY + ((tile * 4 + r) * 4 + w) * 64, tag);
      }
      sn = lds + slot * P_STAGE; slot = (slot == 4) ? 0 : slot + 1;
      rest(T1, T0, sn, 1); head(T0);
      WAITL0(); if (s + 2 < P_ND - 1) BAR();
    }
    rest(T0, T1, lds, 2);
    PAD_MFMA_OUT();
    STAMP(19); STAMPR(29);
    if (w != r) {
      const rsrc_t rp = mk_rsrc(ws + WS_PD + (size_t)((tile * 4 + r) * 4 + w) * 7168, 7168);
#pragma unroll
      for (int u = 0; u < 7; ++u) {
        u32x4 o = {pk_bf16(acc[u][0][0], acc[u][0][1]), pk_bf16(acc[u][0][2], acc[u][0][3]),
                   pk_bf16(acc[u][1][0], acc[u][1][1]), pk_bf16(acc[u][1][2], acc[u][1][3])};
#pragma unroll
        for (int k = 0; k < 4; ++k) o[k] = o[k] < 0xfffffffeu ? o[k] : 0xfffffffeu;
        __builtin_amdgcn_raw_buffer_store_b128(o, rp, u * 1024 + lane * 16, 0, 16);
      }
    } else {
#pragma unroll
      for (int uu = 0; uu < 7; ++uu) {
        *(LAS f32x4*)(lds + (uu * 4 + 0) * 1024 + lane * 16) = accY[uu][0];
        *(LAS f32x4*)(lds + (uu * 4 + 1) * 1024 + lane * 16) = accY[uu][1];
        *(LAS f32x4*)(lds + (uu * 4 + 2) * 1024 + lane * 16) = acc[uu][0];
        *(LAS f32x4*)(lds + (uu * 4 + 3) * 1024 + lane * 16) = acc[uu][1];
      }
      WAITL0();
    }
    BAR();
  }
  if (wid >= 4) {
    __builtin_amdgcn_s_setprio(0);
    const int hw = wid - 4;
    const int nu = hw < 3 ? 2 : 1;
    const unsigned tag = get_tag(lds);
    const int tok0 = 128 * m + 32 * r + 8 * (i16 >> 2) + (i16 & 3);
    const rsrc_t ry = mk_rsrc(ws + WS_PY, 64 * 16 * 7168), rd = mk_rsrc(ws + WS_PD, 64 * 16 * 7168);
    u32x2 x0[2], x1[2];
    u32x4 fy[2][3], fd[2][3];
#pragma unroll
    for (int k = 0; k < 2; ++k) {
      const int u = 2 * hw + (k < nu ? k : 0);
      const int ecol = 112 * n + 16 * u + 4 * g;
      x0[k] = *(const u32x2*)(a.x + (size_t)tok0 * D + ecol); x1[k] = *(const u32x2*)(a.x + (size_t)(tok0 + 4) * D + ecol);
    }
    {
      const int rr = (r + 1 + (lane % 3)) & 3;
      poll_all(ws + WS_FY + ((tile * 4 + rr) * 4 + r) * 64, lane < 3, tag);
    }
#pragma unroll
    for (int k = 0; k < 2; ++k)
#pragma unroll
      for (int i = 0; i < 3; ++i)
        fy[k][i] = __builtin_amdgcn_raw_buffer_load_b128(ry, ((tile * 4 + ((r + 1 + i) & 3)) * 4 + r) * 7168 + (2 * hw + (k < nu ? k : 0)) * 1024 + lane * 16, 0, 16);
    STAMP(20);
    if (MODE == 0 && blockIdx.x == 0 && hw == 3) {
#pragma unroll 1
      for (int i = 0; i < 4; ++i) poll_all(ws + WS_ARRIVE + (64 * i + lane) * 64, true, tag);
      if (lane == 0) st_flag(ws + WS_NONCE, tag);
    }
    for (unsigned sp = 0; sp < (SPIN_MAX >> 3); ++sp) {
      const unsigned v = lane < 3 ? __builtin_amdgcn_raw_buffer_load_b32(rd, ((tile * 4 + ((r + 1 + lane) & 3)) * 4 + r) * 7168 + 6 * 1024 + 63 * 16 + 12, 0, 16) : 0u;
      if (__all(v != 0xffffffffu)) break;
      __builtin_amdgcn_s_sleep(1);
    }
    for (unsigned sp = 0; sp < (SPIN_MAX >> 3); ++sp) {
      bool ok = true;
#pragma unroll
      for (int k = 0; k < 2; ++k)
#pragma unroll
        for (int i = 0; i < 3; ++i) {
          fd[k][i] = __builtin_amdgcn_raw_buffer_load_b128(rd, ((tile * 4 + ((r + 1 + i) & 3)) * 4 + r) * 7168 + (2 * hw + (k < nu ? k : 0)) * 1024 + lane * 16, 0, 16);
        }
#pragma unroll
      for (int k = 0; k < 2; ++k)
#pragma unroll
        for (int i = 0; i < 3; ++i)
          ok = ok && fd[k][i][0] != 0xffffffffu && fd[k][i][1] != 0xffffffffu && fd[k][i][2] != 0xffffffffu && fd[k][i][3] != 0xffffffffu;
      if (__all(ok)) break;
      __builtin_amdgcn_s_sleep(2);
    }
    STAMP(21); STAMPR(29);
    BAR();
#pragma unroll
    for (int k = 0; k < 2; ++k) {
      if (k < nu) {
        const int u = 2 * hw + k;
        const int ecol = 112 * n + 16 * u + 4 * g;
        f32x4 y0 = *(LAS f32x4*)(lds + (u * 4 + 0) * 1024 + lane * 16), y1 = *(LAS f32x4*)(lds + (u * 4 + 1) * 1024 + lane * 16);
        f32x4 d0 = *(LAS f32x4*)(lds + (u * 4 + 2) * 1024 + lane * 16), d1 = *(LAS f32x4*)(lds + (u * 4 + 3) * 1024 + lane * 16);
#pragma unroll
        for (int i = 0; i < 3; ++i) {
          y0 += f32x4{bf_lo(fy[k][i][0]), bf_hi(fy[k][i][0]), bf_lo(fy[k][i][1]), bf_hi(fy[k][i][1])};
          y1 += f32x4{bf_lo(fy[k][i][2]), bf_hi(fy[k][i][2]), bf_lo(fy[k][i][3]), bf_hi(fy[k][i][3])};
          d0 += f32x4{bf_lo(fd[k][i][0]), bf_hi(fd[k][i][0]), bf_lo(fd[k][i][1]), bf_hi(fd[k][i][1])};
          d1 += f32x4{bf_lo(fd[k][i][2]), bf_hi(fd[k][i][2]), bf_lo(fd[k][i][3]), bf_hi(fd[k][i][3])};
        }
        const u32x2 o0 = {pk_bf16(bf_lo(x0[k][0]) + y0[0] * silu_f(d0[0]), bf_hi(x0[k][0]) + y0[1] * silu_f(d0[1])),
                          pk_bf16(bf_lo(x0[k][1]) + y0[2] * silu_f(d0[2]), bf_hi(x0[k][1]) + y0[3] * silu_f(d0[3]))};
        const u32x2 o1 = {pk_bf16(bf_lo(x1[k][0]) + y1[0] * silu_f(d1[0]), bf_hi(x1[k][0]) + y1[1] * silu_f(d1[1])),
                          pk_bf16(bf_lo(x1[k][1]) + y1[2] * silu_f(d1[2]), bf_hi(x1[k][1]) + y1[3] * silu_f(d1[3]))};
        *(u32x2*)(a.out + (size_t)tok0 * D + ecol) = o0;
        *(u32x2*)(a.out + (size_t)(tok0 + 4) * D + ecol) = o1;
      }
    }
  }
}

#define SPLIT 0
extern "C" void kernel_launch(void* const* d_in, const int* in_sizes, int n_in, void* d_out, int out_size, void* d_ws, size_t ws_size,
                              hipStream_t stream) {
  Args a;
  a.x = (const u16*)d_in[0]; a.Cr = (const u16*)d_in[1]; a.Ci = (const u16*)d_in[2]; a.Dr = (const u16*)d_in[3]; a.Di = (const u16*)d_in[4];
  a.Wg = (const u16*)d_in[6]; a.Wu = (const u16*)d_in[7]; a.Wd = (const u16*)d_in[8];
  a.ws = (char*)d_ws; a.out = (u16*)d_out;
  (void)hipFuncSetAttribute((const void*)fused_kernel<0>, hipFuncAttributeMaxDynamicSharedMemorySize, L_TOTAL);
  fused_kernel<0><<<256, 512, L_TOTAL, stream>>>(a);
}
```

```cpp
#define PROF 1
#define PRINTF_DIAG 1
#include <hip/hip_runtime.h>
#include <hip/hip_bf16.h>
#include <stdint.h>
#include <stdio.h>

typedef unsigned short u16;
typedef __bf16 bf16x8 __attribute__((ext_vector_type(8)));
typedef short s16x8 __attribute__((ext_vector_type(8)));
typedef float f32x4 __attribute__((ext_vector_type(4)));
typedef float f32x2 __attribute__((ext_vector_type(2)));
typedef unsigned u32x4 __attribute__((ext_vector_type(4)));
typedef unsigned u32x2 __attribute__((ext_vector_type(2)));
#define LAS __attribute__((address_space(3)))
#define GAS __attribute__((address_space(1)))
typedef LAS char* lptr;
typedef __amdgpu_buffer_rsrc_t rsrc_t;

constexpr int S = 2048, D = 448, F = 1792;

constexpr size_t WS_NONCE  = 0;
constexpr size_t WS_ARRIVE = 4096;
constexpr size_t WS_FAB    = 32768;
constexpr size_t WS_FH     = 49152;
constexpr size_t WS_FWD    = 65536;
constexpr size_t WS_FY     = 81920;
constexpr size_t WS_FD     = 147456;
constexpr size_t WS_ST     = 212992;
constexpr size_t WS_CW     = 229376;
constexpr size_t WS_CD     = 286720;
constexpr size_t WS_AB     = 327680;
constexpr size_t WS_H      = WS_AB + (size_t)2 * 28 * 64 * 1024;
constexpr size_t WS_WDT    = WS_H + (size_t)128 * 56 * 1024;
constexpr size_t WS_PY     = WS_WDT + (size_t)28 * 56 * 1024;
constexpr size_t WS_PD     = WS_PY + (size_t)64 * 16 * 7 * 1024;

constexpr size_t WS_PROF = 64u << 20;
#define STAMP(i) do { unsigned t_ = (unsigned)__builtin_amdgcn_s_memtime(); if ((threadIdx.x & 63) == 0) \
   *(volatile LAS unsigned*)(lds + L_PROF + ((threadIdx.x >> 6) * 32 + (i)) * 4) = t_; } while (0)
#define STAMPR(i) do { if ((threadIdx.x & 63) == 0) { unsigned long long t_ = __builtin_amdgcn_s_memrealtime(); \
   ((unsigned long long*)(ws + WS_PROF))[((size_t)blockIdx.x * 8 + (threadIdx.x >> 6)) * 32 + (i)] = t_; } } while (0)
#define SBAR() __builtin_amdgcn_sched_barrier(0)
#define BAR() __builtin_amdgcn_s_barrier()
#define WAITV(n) asm volatile("s_waitcnt vmcnt(%0)" ::"n"(n) : "memory")
#define WAITL0() __builtin_amdgcn_s_waitcnt(0xC07F)
#define CODE_PF 65536
#define PF0 2
#define STAUX 16
#define SPIN_MAX (1u << 18)

__device__ __forceinline__ rsrc_t mk_rsrc(const void* p, unsigned bytes) {
  return __builtin_amdgcn_make_buffer_rsrc((void*)p, 0, bytes, 0x00020000);
}
__device__ __forceinline__ u32x4 mk_srd(const void* p, unsigned bytes) {
  const unsigned long long a = (unsigned long long)p;
  return u32x4{(unsigned)a, (unsigned)(a >> 32) & 0xffffu, bytes, 0x00020000u};
}
__device__ __forceinline__ unsigned lds_addr(lptr p) { return (unsigned)(size_t)p; }
__device__ __forceinline__ void dma16(const u32x4& srd, unsigned dst, int voff, int soff) {
  asm volatile("s_mov_b32 m0, %0\n\ts_nop 0\n\tbuffer_load_dwordx4 %1, %2, %3 offen lds" ::"s"(dst), "v"(voff), "s"(srd), "s"(soff) : "memory");
}
__device__ __forceinline__ void dma16c(const u32x4& srd, unsigned dst, int voff, int soff) {
  asm volatile("s_mov_b32 m0, %0\n\ts_nop 0\n\tbuffer_load_dwordx4 %1, %2, %3 offen sc1 lds" ::"s"(dst), "v"(voff), "s"(srd), "s"(soff) : "memory");
}
__device__ __forceinline__ void pf4c(const char* p, unsigned dst) {
  asm volatile("s_mov_b32 m0, %0\n\ts_nop 0\n\tglobal_load_lds_dword %1, off sc1" ::"s"(dst), "v"(p) : "memory");
}
__device__ __forceinline__ void pf4(const char* p, unsigned dst) {
  asm volatile("s_mov_b32 m0, %0\n\ts_nop 0\n\tglobal_load_lds_dword %1, off" ::"s"(dst), "v"(p) : "memory");
}
__device__ __forceinline__ bf16x8 ldsr128(lptr p) { return *(const LAS bf16x8*)p; }
__device__ __forceinline__ bf16x8 ldstr(lptr p0, lptr p1) {
  typedef short v4i16_t __attribute__((ext_vector_type(4)));
  v4i16_t lo = __builtin_amdgcn_ds_read_tr16_b64_v4i16((LAS v4i16_t*)p0);
  v4i16_t hi = __builtin_amdgcn_ds_read_tr16_b64_v4i16((LAS v4i16_t*)p1);
  s16x8 v = __builtin_shufflevector(lo, hi, 0, 1, 2, 3, 4, 5, 6, 7);
  return __builtin_bit_cast(bf16x8, v);
}
__device__ __forceinline__ f32x4 mfma(bf16x8 a, bf16x8 b, f32x4 c) { return __builtin_amdgcn_mfma_f32_16x16x32_bf16(a, b, c, 0, 0, 0); }
__device__ __forceinline__ void mfma_ip(f32x4& c, const bf16x8& a, const bf16x8& b) {
  asm volatile("v_mfma_f32_16x16x32_bf16 %0, %1, %2, %0" : "+v"(c) : "v"(a), "v"(b));
}
#define FILL() asm volatile("s_nop 0")
#define PAD_MFMA_OUT() asm volatile("s_nop 7\n\ts_nop 3")
#define PAD_MFMA_IN() asm volatile("s_nop 3")
__device__ __forceinline__ unsigned pk_bf16(float lo, float hi) {
  typedef __bf16 bf16x2 __attribute__((ext_vector_type(2)));
  f32x2 v = {lo, hi};
  bf16x2 b = __builtin_convertvector(v, bf16x2);
  return __builtin_bit_cast(unsigned, b);
}
__device__ __forceinline__ float bf_lo(unsigned v) { return __uint_as_float(v << 16); }
__device__ __forceinline__ float bf_hi(unsigned v) { return __uint_as_float(v & 0xffff0000u); }
__device__ __forceinline__ float rbf(float f) { return bf_lo(pk_bf16(f, 0.f)); }
__device__ __forceinline__ float silu_f(float x) {
  float e = __builtin_amdgcn_exp2f(x * -1.4426950409f);
  return x * __builtin_amdgcn_rcpf(1.0f + e);
}
template <int N>
__device__ __forceinline__ void wave_sums(float (&v)[N]) {
#define DPP_STEP(ctrl, rmask) _Pragma("unroll") for (int i = 0; i < N; ++i) \
    v[i] += __builtin_bit_cast(float, __builtin_amdgcn_update_dpp(0, __builtin_bit_cast(int, v[i]), ctrl, rmask, 0xf, true));
  DPP_STEP(0x111, 0xf)
  DPP_STEP(0x112, 0xf)
  DPP_STEP(0x114, 0xf)
  DPP_STEP(0x118, 0xf)
  DPP_STEP(0x142, 0xa)
  DPP_STEP(0x143, 0xc)
#undef DPP_STEP
#pragma unroll
  for (int i = 0; i < N; ++i) v[i] = __builtin_bit_cast(float, __builtin_amdgcn_readlane(__builtin_bit_cast(int, v[i]), 63));
}
__device__ __forceinline__ unsigned ld_flag(const void* p) { return __hip_atomic_load((const GAS unsigned*)p, __ATOMIC_RELAXED, __HIP_MEMORY_SCOPE_AGENT); }
__device__ __forceinline__ void st_flag(void* p, unsigned v) { __hip_atomic_store((GAS unsigned*)p, v, __ATOMIC_RELAXED, __HIP_MEMORY_SCOPE_AGENT); }
__device__ __forceinline__ unsigned get_tag(lptr lds);
__device__ __forceinline__ void poll_all(const char* p, bool active, unsigned tag) {
  for (unsigned sp = 0; sp < SPIN_MAX; ++sp) {
    const unsigned v = active ? ld_flag(p) : tag;
    if (__all(v == tag)) break;
    __builtin_amdgcn_s_sleep(2);
  }
}
__device__ __forceinline__ void lds_spin(lptr p) {
  for (unsigned sp = 0; sp < SPIN_MAX * 4; ++sp) {
    if (*(volatile LAS unsigned*)p != 0u) break;
    __builtin_amdgcn_s_sleep(2);
  }
}

constexpr int K1_STAGE = 53248, K1_NST = 7, K1_WO = 16384;
constexpr int L_MISC = 3 * K1_STAGE;
constexpr int L_STATS = L_MISC;
constexpr int L_CS = L_MISC + 1024;
constexpr int L_WORDS = L_CS + 18 * 64;
constexpr int L_JOBS = L_WORDS + 64;
constexpr int L_DUMMY = L_WORDS + 128;
constexpr int L_PROF = L_DUMMY + 512;
constexpr int L_TOTAL = L_PROF + 1024;
#define YREP 1
constexpr int P_STAGE = 30720, P_NY = 16 * YREP, P_ND = 7, P_NST = P_NY + P_ND;
static_assert(L_TOTAL <= 163840 && 5 * P_STAGE <= L_MISC, "LDS");

__device__ __forceinline__ unsigned get_tag(lptr lds) { return *(volatile LAS unsigned*)(lds + L_WORDS + 16); }

struct Args {
  const u16 *x, *Cr, *Ci, *Dr, *Di, *Wg, *Wu, *Wd;
  char* ws;
  u16* out;
};

__device__ __forceinline__ void launder(Args& a, int& xcd, int& idx) {
  asm volatile("" : "+s"(a.x), "+s"(a.Cr), "+s"(a.Ci), "+s"(a.Dr), "+s"(a.Di));
  asm volatile("" : "+s"(a.Wg), "+s"(a.Wu), "+s"(a.Wd), "+s"(a.ws), "+s"(a.out));
  asm volatile("" : "+s"(xcd), "+s"(idx));
}

struct StartJobs { unsigned pay0, pay1; float ds0, ds1; };
template <int MODE>
__device__ __forceinline__ StartJobs start_jobs(lptr lds, const Args& a, int xcd, int idx, int mt, int grp, int h, int lane) {
  const int nq = xcd >> 1;

  const int srow = mt * 128 + 8 * grp + 2 * h;
  u32x4 v[4] = {{0, 0, 0, 0}, {0, 0, 0, 0}, {0, 0, 0, 0}, {0, 0, 0, 0}};
  if (lane < 56) {
    v[0] = *(const u32x4*)(a.x + (size_t)srow * D + lane * 8);
    v[1] = *(const u32x4*)(a.x + (size_t)(srow + 1) * D + lane * 8);
    const u16* dp = (nq >= 2 ? a.Di : a.Dr) + (size_t)(224 * (nq & 1) + 7 * idx + h) * D + lane * 8;
    v[2] = *(const u32x4*)dp;
    v[3] = *(const u32x4*)(dp + (h < 3 ? 4 * D : 0));
  }

  float f[4][8], sm[4];
#pragma unroll
  for (int r = 0; r < 4; ++r) {
#pragma unroll
    for (int i = 0; i < 4; ++i) { f[r][2 * i] = bf_lo(v[r][i]); f[r][2 * i + 1] = bf_hi(v[r][i]); }
    sm[r] = ((f[r][0] + f[r][1]) + (f[r][2] + f[r][3])) + ((f[r][4] + f[r][5]) + (f[r][6] + f[r][7]));
  }
  wave_sums<4>(sm);
  float q[2], mean[2];
#pragma unroll
  for (int r = 0; r < 2; ++r) {
    mean[r] = sm[r] * (1.0f / D);
    float t0 = 0.f, t1 = 0.f;
#pragma unroll
    for (int i = 0; i < 4; ++i) {
      const float d0 = f[r][2 * i] - mean[r], d1 = f[r][2 * i + 1] - mean[r];
      t0 = __builtin_fmaf(d0, d0, t0); t1 = __builtin_fmaf(d1, d1, t1);
    }
    q[r] = lane < 56 ? t0 + t1 : 0.f;
  }
  wave_sums<2>(q);
  StartJobs j;
  j.pay0 = pk_bf16(mean[0], rbf(__builtin_sqrtf(rbf(rbf(q[0] * (1.0f / D)) + 1e-5f))));
  j.pay1 = pk_bf16(mean[1], rbf(__builtin_sqrtf(rbf(rbf(q[1] * (1.0f / D)) + 1e-5f))));
  j.ds0 = sm[2]; j.ds1 = sm[3];
  return j;
}
__device__ __forceinline__ void stash_jobs(lptr lds, const StartJobs& j, int h, int lane) {
  if (lane == 0) *(LAS u32x4*)(lds + L_JOBS + h * 16) = u32x4{j.pay0, j.pay1, __float_as_uint(j.ds0), __float_as_uint(j.ds1)};
}
__device__ __forceinline__ void publish_jobs(lptr lds, char* ws, unsigned tag1, int xcd, int idx, int mt, int grp, int h, int lane) {
  const int nq = xcd >> 1;
  if (lane < 2 || lane == 2 || (lane == 3 && h < 3)) {
    const unsigned v = *(LAS unsigned*)(lds + L_JOBS + h * 16 + lane * 4);
    const size_t off = lane < 2 ? WS_ST + (size_t)(mt * 128 + 8 * grp + 2 * h + lane) * 8 : WS_CD + (size_t)(224 * nq + 7 * idx + h + 4 * (lane - 2)) * 8;
    __hip_atomic_store((GAS unsigned long long*)(ws + off), ((unsigned long long)tag1 << 32) | v, __ATOMIC_RELAXED, __HIP_MEMORY_SCOPE_AGENT);
  }
}

__device__ __forceinline__ void h_batch(const f32x4 (&g)[2][2], const f32x4 (&u)[2][2], const f32x4 (&cg)[2], const f32x4 (&cu)[2], const f32x2 (&st)[2],
                                        rsrc_t rh, int off0) {
  f32x2 gg[8], uu[8], z[8];
#pragma unroll
  for (int i = 0; i < 8; ++i) {
    const int t = i >> 2, j = (i >> 1) & 1, hh = i & 1;
    gg[i] = (f32x2{g[j][t][2 * hh], g[j][t][2 * hh + 1]} - f32x2{cg[j][2 * hh], cg[j][2 * hh + 1]} * st[t].x) * st[t].y;
  }
#pragma unroll
  for (int i = 0; i < 8; ++i) z[i] = gg[i] * ((gg[i] * gg[i]) * -0.1029432397f + -2.302208198f);
#pragma unroll
  for (int i = 0; i < 8; ++i) { z[i].x = __builtin_amdgcn_exp2f(z[i].x); z[i].y = __builtin_amdgcn_exp2f(z[i].y); }
#pragma unroll
  for (int i = 0; i < 8; ++i) {
    const int t = i >> 2, j = (i >> 1) & 1, hh = i & 1;
    uu[i] = ((f32x2{u[j][t][2 * hh], u[j][t][2 * hh + 1]} - f32x2{cu[j][2 * hh], cu[j][2 * hh + 1]} * st[t].x) * st[t].y) * gg[i];
    z[i] = z[i] + 1.0f;
  }
#pragma unroll
  for (int i = 0; i < 8; ++i) { z[i].x = __builtin_amdgcn_rcpf(z[i].x); z[i].y = __builtin_amdgcn_rcpf(z[i].y); }
#pragma unroll
  for (int i = 0; i < 8; ++i) uu[i] = uu[i] * z[i];
#pragma unroll
  for (int tl = 0; tl < 2; ++tl) {
    const u32x4 o = {pk_bf16(uu[4 * tl].x, uu[4 * tl].y), pk_bf16(uu[4 * tl + 1].x, uu[4 * tl + 1].y),
                     pk_bf16(uu[4 * tl + 2].x, uu[4 * tl + 2].y), pk_bf16(uu[4 * tl + 3].x, uu[4 * tl + 3].y)};
    __builtin_amdgcn_raw_buffer_store_b128(o, rh, off0 + tl * 56 * 1024, 0, STAUX);
  }
}
constexpr int L_HD = 61440;

template <int MODE, int NG, int NA>
__device__ __forceinline__ void p1_consumer(lptr lds, Args a, int xcd, int idx, int mt, int grp, int f0, int a0, int wm, int wn, int lane) {
  StartJobs jobs = start_jobs<MODE>(lds, a, xcd, idx, mt, grp, 2 * wn + wm, lane);
  stash_jobs(lds, jobs, 2 * wn + wm, lane);
  launder(a, xcd, idx);
  asm volatile("" : "+s"(mt), "+s"(grp), "+s"(f0), "+s"(a0));
  char* ws = a.ws;
  const char* cp = (const char*)((xcd >> 2) ? a.Ci : a.Cr) + (size_t)(1024 * (xcd & 1) + 32 * idx + 8 * (2 * wn + wm) + (lane >> 5)) * 4096 + 2048 * ((xcd >> 1) & 1) + (lane & 31) * 64;
  unsigned nonce = 0;
  if (MODE == 0 && wm == 0 && wn == 0) asm volatile("global_load_dword %0, %1, off sc1" : "+v"(nonce) : "v"(ws + WS_NONCE) : "memory");
  constexpr int NBK = 2 * NG, NCQ = NBK / 4, WSZ = NBK * 8 * 256, KSO = 4 * NCQ * 1024;
  constexpr int GO = K1_WO, UO = K1_WO + WSZ, DO = K1_WO + 2 * WSZ;
  const int i16 = lane & 15, g = lane >> 4, q = i16 >> 2, p = i16 & 3;
  const int rowx = 64 * wm + 8 * (i16 >> 2) + (i16 & 3);
  const int vx0 = rowx * 128 + ((g) ^ (i16 & 7)) * 16;
  const int vx1 = rowx * 128 + ((g + 4) ^ (i16 & 7)) * 16;
  const int fw = ((q >> 1) & 1) | ((g & 1) << 1);
  int vw[NG];
#pragma unroll
  for (int j = 0; j < NG; ++j) { const int nb = NG * wn + j; vw[j] = g * NCQ * 1024 + (nb >> 2) * 1024 + q * 128 + (((nb & 3) ^ fw) * 32) + p * 8; }
  const int vd0 = DO + (16 * NA * wn + i16) * 128 + ((g) ^ (i16 & 7)) * 16;
  const int vd1 = DO + (16 * NA * wn + i16) * 128 + ((g + 4) ^ (i16 & 7)) * 16;

  f32x4 G[NG][4], U[NG][4], AB[NA][4];
#pragma unroll
  for (int j = 0; j < NG; ++j)
#pragma unroll
    for (int t = 0; t < 4; ++t) { G[j][t] = f32x4{0, 0, 0, 0}; U[j][t] = f32x4{0, 0, 0, 0}; }
#pragma unroll
  for (int j = 0; j < NA; ++j)
#pragma unroll
    for (int t = 0; t < 4; ++t) AB[j][t] = f32x4{0, 0, 0, 0};

  bf16x8 X0[4], X1[4], WG[NG], WU[NG], WD[NA];
  auto ldx = [&](bf16x8 (&X)[4], lptr st, int ks) {
#pragma unroll
    for (int t = 0; t < 4; ++t) X[t] = ldsr128(st + (ks ? vx1 : vx0) + (32 * (t >> 1) + 4 * (t & 1)) * 128);
  };
  auto ldg = [&](int j, lptr st, int ks) { lptr b = st + GO + ks * KSO + vw[j]; WG[j] = ldstr(b, b + 512); };
  auto ldu = [&](int j, lptr st, int ks) { lptr b = st + UO + ks * KSO + vw[j]; WU[j] = ldstr(b, b + 512); };
  auto ldd = [&](int j, lptr st, int ks) { WD[j] = ldsr128(st + (ks ? vd1 : vd0) + j * 16 * 128); };
  auto grp4 = [&](f32x4 (&C)[4], const bf16x8& W, bf16x8 (&X)[4], bool wfirst, auto mid) {
    if (wfirst) { mfma_ip(C[0], W, X[0]); mfma_ip(C[1], W, X[1]); } else { mfma_ip(C[0], X[0], W); mfma_ip(C[1], X[1], W); }
    SBAR(); mid(); SBAR();
    if (wfirst) { mfma_ip(C[2], W, X[2]); mfma_ip(C[3], W, X[3]); } else { mfma_ip(C[2], X[2], W); mfma_ip(C[3], X[3], W); }
    SBAR();
  };
  auto fill = [&]() { FILL(); };
  auto ldx1 = [&](bf16x8 (&X)[4], lptr st, int ks, int t) { X[t] = ldsr128(st + (ks ? vx1 : vx0) + (32 * (t >> 1) + 4 * (t & 1)) * 128); };
  auto blockA = [&](lptr st) {
#pragma unroll
    for (int j = 0; j < NG; ++j) {
      grp4(G[j], WG[j], X0, true, [&]() { if (2 * j < 4) ldx1(X1, st, 1, 2 * j); else FILL(); }); ldg(j, st, 1); SBAR();
      grp4(U[j], WU[j], X0, true, [&]() { if (2 * j + 1 < 4) ldx1(X1, st, 1, 2 * j + 1); else FILL(); }); ldu(j, st, 1); SBAR();
    }
#pragma unroll
    for (int j = 0; j < NA; ++j) { grp4(AB[j], WD[j], X0, false, fill); ldd(j, st, 1); SBAR(); }
  };
  auto blockB_head = [&]() { grp4(G[0], WG[0], X1, true, fill); FILL(); grp4(U[0], WU[0], X1, true, fill); };
  auto blockB_rest = [&](lptr sn, bool reload) {
    int k = 0;
    auto mid = [&]() {
      if (!reload) FILL();
      else if (k == 0) ldg(0, sn, 0);
      else if (k == 1) ldu(0, sn, 0);
      else if (k < 6) ldx1(X0, sn, 0, k - 2);
      else FILL();
      ++k;
    };
#pragma unroll
    for (int j = 1; j < NG; ++j) {
      grp4(G[j], WG[j], X1, true, mid); if (reload) { ldg(j, sn, 0); SBAR(); } else FILL();
      grp4(U[j], WU[j], X1, true, mid); if (reload) { ldu(j, sn, 0); SBAR(); } else FILL();
    }
#pragma unroll
    for (int j = 0; j < NA; ++j) { grp4(AB[j], WD[j], X1, false, mid); if (reload) { ldd(j, sn, 0); SBAR(); } else FILL(); }
  };

#pragma unroll
  for (int j = 0; j < NG; ++j)
#pragma unroll
    for (int t = 0; t < 4; ++t) { asm volatile("" : "+v"(G[j][t])); asm volatile("" : "+v"(U[j][t])); }
#pragma unroll
  for (int j = 0; j < NA; ++j)
#pragma unroll
    for (int t = 0; t < 4; ++t) asm volatile("" : "+v"(AB[j][t]));
  PAD_MFMA_IN();
  STAMP(1);
  WAITL0(); BAR();
  STAMP(2);
  ldx(X0, lds, 0);
#pragma unroll
  for (int j = 0; j < NG; ++j) { ldg(j, lds, 0); ldu(j, lds, 0); }
#pragma unroll
  for (int j = 0; j < NA; ++j) ldd(j, lds, 0);
  SBAR();
  blockA(lds);
  blockB_head();
  if (wm == 0 && wn == 0) {
    WAITV(0);
    asm volatile("" : "+v"(nonce));
    if (lane == 0) *(volatile LAS unsigned*)(lds + L_WORDS + 16) = (MODE == 0) ? nonce * 0x9E3779B1u + 0x7F4A7C15u : 0x51u;
  }
  WAITL0(); STAMP(6); BAR();
  STAMP(7);
  int slot = 1;
#pragma unroll 1
  for (int s = 0; s < K1_NST - 1; ++s) {
    lptr sn = lds + slot * K1_STAGE;
    slot = (slot == 2) ? 0 : slot + 1;
    blockB_rest(sn, true);
    blockA(sn);
    if (s >= PF0 && s < PF0 + 4) { pf4(cp, lds_addr(lds) + L_DUMMY); cp += 2 * 4096; }
    blockB_head();
    WAITL0();
    STAMP(20 + s);
    BAR();
    STAMP(8 + s);
  }
#pragma unroll
  for (int j = 0; j < NA; ++j) { grp4(AB[j], WD[j], X1, false, fill); FILL(); }
  PAD_MFMA_OUT();
  STAMP(3);
  const lptr cs = lds + L_CS;
  const lptr stats = lds + L_STATS;
  {
    const rsrc_t rab = mk_rsrc(ws + WS_AB, 2 * 28 * 64 * 1024);
#pragma unroll
    for (int pi = 0; pi < 2; ++pi) {
      const lptr sp = stats + (64 * wm + 32 * pi + 8 * g) * 8;
      const f32x4 s0 = *(LAS f32x4*)(sp), s1 = *(LAS f32x4*)(sp + 16), s2 = *(LAS f32x4*)(sp + 32), s3 = *(LAS f32x4*)(sp + 48);
#pragma unroll
      for (int j = 0; j < NA; ++j) {
        const float c = *(LAS float*)(cs + (2 * NBK + NA * wn + j) * 64 + i16 * 4);
        const f32x4 a_ = AB[j][2 * pi], b = AB[j][2 * pi + 1];
        const int ks = mt * 4 + wm * 2 + pi;
        u32x4 o;
        o[0] = pk_bf16((a_[0] - s0[0] * c) * s0[1], (a_[1] - s0[2] * c) * s0[3]);
        o[1] = pk_bf16((a_[2] - s1[0] * c) * s1[1], (a_[3] - s1[2] * c) * s1[3]);
        o[2] = pk_bf16((b[0] - s2[0] * c) * s2[1], (b[1] - s2[2] * c) * s2[3]);
        o[3] = pk_bf16((b[2] - s3[0] * c) * s3[1], (b[3] - s3[2] * c) * s3[3]);
        __builtin_amdgcn_raw_buffer_store_b128(o, rab, lane * 16 + ((a0 + NA * wn + j) * 64 + ks) * 1024, 0, STAUX);
      }
    }
  }
  SBAR();
#pragma unroll
  for (int j = 1; j < NG; ++j) { grp4(G[j], WG[j], X1, true, fill); FILL(); grp4(U[j], WU[j], X1, true, fill); FILL(); }
  PAD_MFMA_OUT();
  {
    lptr hd = lds + L_HD + (2 * wn + wm) * (NG * 4096) + lane * 16;
#pragma unroll
    for (int j = 0; j < NG; ++j)
#pragma unroll
      for (int tl = 0; tl < 2; ++tl) {
        *(LAS f32x4*)(hd + ((j * 2 + 0) * 2 + tl) * 1024) = G[j][2 + tl];
        *(LAS f32x4*)(hd + ((j * 2 + 1) * 2 + tl) * 1024) = U[j][2 + tl];
      }
  }
  WAITV(0);
  if (lane == 0) {
    const unsigned n = __hip_atomic_fetch_add((LAS unsigned*)(lds + L_WORDS), 1u, __ATOMIC_RELAXED, __HIP_MEMORY_SCOPE_WORKGROUP);
    if (n == 3u) st_flag(ws + WS_FAB + (mt * 16 + grp) * 64, get_tag(lds));
  }
  STAMP(5);
  WAITL0(); BAR();
  STAMP(14);
  const rsrc_t rh = mk_rsrc(ws + WS_H, 128 * 56 * 1024);
  const int tt0 = mt * 8 + wm * 4;
  f32x2 st[2];
#pragma unroll
  for (int t = 0; t < 2; ++t) st[t] = *(LAS f32x2*)(stats + (64 * wm + 8 * (i16 >> 2) + 4 * t + (i16 & 3)) * 8);
#pragma unroll
  for (int pp = 0; pp < NG / 2; ++pp) {
    const int ks = ((f0 >> 4) + NG * wn + 2 * pp) >> 1;
    f32x4 cg[2], cu[2], gq[2][2], uq[2][2];
#pragma unroll
    for (int j = 0; j < 2; ++j) {
      cg[j] = *(LAS f32x4*)(cs + (NG * wn + 2 * pp + j) * 64 + g * 16);
      cu[j] = *(LAS f32x4*)(cs + (NBK + NG * wn + 2 * pp + j) * 64 + g * 16);
      gq[j][0] = G[2 * pp + j][0]; gq[j][1] = G[2 * pp + j][1]; uq[j][0] = U[2 * pp + j][0]; uq[j][1] = U[2 * pp + j][1];
    }
    h_batch(gq, uq, cg, cu, st, rh, lane * 16 + (tt0 * 56 + ks) * 1024);
  }
  STAMP(4);
}

template <int NG>
__device__ __forceinline__ void p1_helper_epilogue(lptr lds, char* ws, int mt, int f0, int h, int lane) {
  constexpr int NBK = 2 * NG;
  const int wm = h & 1, wn = h >> 1, i16 = lane & 15, g = lane >> 4;
  const lptr cs = lds + L_CS, stats = lds + L_STATS;
  const rsrc_t rh = mk_rsrc(ws + WS_H, 128 * 56 * 1024);
  const int tt0 = mt * 8 + wm * 4 + 2;
  lptr hd = lds + L_HD + h * (NG * 4096) + lane * 16;
  f32x2 st[2];
#pragma unroll
  for (int t = 0; t < 2; ++t) st[t] = *(LAS f32x2*)(stats + (64 * wm + 32 + 8 * (i16 >> 2) + 4 * t + (i16 & 3)) * 8);
#pragma unroll
  for (int pp = 0; pp < NG / 2; ++pp) {
    const int ks = ((f0 >> 4) + NG * wn + 2 * pp) >> 1;
    f32x4 cg[2], cu[2], gq[2][2], uq[2][2];
#pragma unroll
    for (int j = 0; j < 2; ++j) {
      cg[j] = *(LAS f32x4*)(cs + (NG * wn + 2 * pp + j) * 64 + g * 16);
      cu[j] = *(LAS f32x4*)(cs + (NBK + NG * wn + 2 * pp + j) * 64 + g * 16);
#pragma unroll
      for (int tl = 0; tl < 2; ++tl) {
        gq[j][tl] = *(LAS f32x4*)(hd + (((2 * pp + j) * 2 + 0) * 2 + tl) * 1024);
        uq[j][tl] = *(LAS f32x4*)(hd + (((2 * pp + j) * 2 + 1) * 2 + tl) * 1024);
      }
    }
    h_batch(gq, uq, cg, cu, st, rh, lane * 16 + (tt0 * 56 + ks) * 1024);
  }
}

template <int MODE, bool TB>
__device__ __forceinline__ void p1_helper(lptr lds, Args a, int xcd, int idx, int mt, int grp, int f0, int a0, int h, int lane) {
  constexpr int NBK = TB ? 4 : 8, WSZ = NBK * 8 * 256;
  constexpr int NWP = TB ? 4 : 8, NDP = TB ? 4 : 1, PER = 4 + NWP + NDP;
  const int mh = xcd & 1, nq = xcd >> 1;
  const int up = h >> 1;
  const u32x4 rx = mk_srd(a.x, S * D * 2);
  const u32x4 rw = mk_srd(up ? a.Wu : a.Wg, D * F * 2);
  const u32x4 rd = mk_srd(a0 >= 28 ? a.Di : a.Dr, D * D * 2);
  const int e0 = (a0 >= 28 ? a0 - 28 : a0) * 16;
  const int r8 = lane >> 3, c8 = lane & 7;
  int vx[2];
  vx[0] = r8 * 896 + (c8 ^ (r8 & 3)) * 16;
  vx[1] = r8 * 896 + (c8 ^ ((r8 & 3) | 4)) * 16;
  const int vd = r8 * 896 + (c8 ^ r8) * 16;
  int vw[2];
  {
    const int f0_ = (r8 >> 1) & 1;
    vw[0] = r8 * (F * 2) + f0 * 2 + ((((c8 >> 1) ^ f0_) * 2) + (c8 & 1)) * 16;
    vw[1] = r8 * (F * 2) + f0 * 2 + ((((c8 >> 1) ^ (f0_ | 2)) * 2) + (c8 & 1)) * 16;
  }
  const unsigned l0 = lds_addr(lds);
  const unsigned dummy = l0 + L_DUMMY + (h & 1) * 256;
  const int sxb = (mt * 128 + 32 * h) * 896, swb = 32 * (h & 1) * (F * 2), sdb = (e0 + 8 * NDP * h) * 896;
  const unsigned lxb = l0 + 4 * h * 1024, lwb = l0 + K1_WO + up * WSZ + NWP * (h & 1) * 1024, ldb = l0 + K1_WO + 2 * WSZ + NDP * h * 1024;
  auto piece = [&](int s, int slot, int i) {
    const unsigned so = slot * K1_STAGE;
    if (i < 4) dma16(rx, lxb + so + i * 1024, vx[i & 1], sxb + s * 128 + i * 8 * 896);
    else if (i < 4 + NWP) {
      const int j = i - 4, kb = TB ? j : j >> 1, cq = TB ? 0 : j & 1;
      dma16(rw, lwb + so + j * 1024, vw[kb & 1], swb + s * 64 * (F * 2) + kb * 8 * (F * 2) + cq * 128);
    } else dma16(rd, ldb + so + (i - 4 - NWP) * 1024, vd, sdb + s * 128 + (i - 4 - NWP) * 8 * 896);
  };
  auto issue_a = [&](int s, int slot) {
#pragma unroll
    for (int i = 0; i < 8; ++i) piece(s, slot, i);
  };
  auto issue_b = [&](int s, int slot) {
#pragma unroll
    for (int i = 8; i < PER; ++i) piece(s, slot, i);
  };
  const int ln2 = lane >> 1, hb = (lane & 1) * 64;
  const char* pfx = (const char*)a.x + (size_t)(1024 * mh + 32 * idx + ln2) * 896 + hb;
  STAMP(1);
  issue_a(0, 0); issue_b(0, 0);
  STAMP(5);
  launder(a, xcd, idx);
  char* ws = a.ws;
  const bool wjob = idx < 28;
  const int wj_mat = idx >= 14, wj_cb = (idx - 14 * wj_mat) >> 1, wj_kh = idx & 1;
  u32x4 wx[7];
  {
    const u32x4 rj = mk_srd(wj_mat ? a.Wu : a.Wg, D * F * 2);
    const int vj = r8 * (F * 2) + c8 * 16 + (448 * nq + 64 * (wjob ? wj_cb : 0)) * 2;
    const int sj = (224 * (wjob ? wj_kh : 0) + 56 * h) * (F * 2);
    asm volatile("buffer_load_dwordx4 %0, %7, %8, %9 offen\n\tbuffer_load_dwordx4 %1, %7, %8, %10 offen\n\tbuffer_load_dwordx4 %2, %7, %8, %11 offen\n\t"
                 "buffer_load_dwordx4 %3, %7, %8, %12 offen\n\tbuffer_load_dwordx4 %4, %7, %8, %13 offen\n\tbuffer_load_dwordx4 %5, %7, %8, %14 offen\n\t"
                 "buffer_load_dwordx4 %6, %7, %8, %15 offen"
                 : "=&v"(wx[0]), "=&v"(wx[1]), "=&v"(wx[2]), "=&v"(wx[3]), "=&v"(wx[4]), "=&v"(wx[5]), "=&v"(wx[6])
                 : "v"(vj), "s"(rj), "s"(sj), "s"(sj + 8 * F * 2), "s"(sj + 16 * F * 2), "s"(sj + 24 * F * 2), "s"(sj + 32 * F * 2), "s"(sj + 40 * F * 2), "s"(sj + 48 * F * 2)
                 : "memory");
  }
  pf4(pfx + (1 + h) * 128, dummy);
  if (h == 3 && lane < 4) ((LAS unsigned*)(lds + L_WORDS))[lane] = 0u;
  WAITL0(); WAITV(8);
  STAMP(7); BAR();
  STAMP(2);
#pragma unroll
  for (int i = 0; i < PER; ++i) piece(1, 1, i);
  issue_a(2, 2);
  if (h < 2) pf4(pfx + (5 + h) * 128, dummy);
  WAITL0();
  if (h < 2) { WAITV(9); } else { WAITV(8); }
  asm volatile("" : "+v"(wx[0]), "+v"(wx[1]), "+v"(wx[2]), "+v"(wx[3]), "+v"(wx[4]), "+v"(wx[5]), "+v"(wx[6]));
  BAR();
  const unsigned tag1 = get_tag(lds);
  publish_jobs(lds, ws, tag1, xcd, idx, mt, grp, h, lane);
  float cw[8];
#pragma unroll
  for (int k = 0; k < 8; ++k) cw[k] = 0.f;
  constexpr bool TBc = TB;
  unsigned long long g0 = 0, g1 = 0, g2 = 0, g3 = 0;
  const bool wdjob = blockIdx.x < 196;
  const int wd_kp = blockIdx.x % 28, wd_cg = blockIdx.x / 28;
#pragma unroll
  for (int s = 1; s < K1_NST; ++s) {
    const int s1_ = (s + 1) % 3, s2_ = (s + 2) % 3;
    if (s == 1) {
#pragma unroll
      for (int i = 0; i < PER; ++i) {
        if (i < PER - 8) piece(2, s1_, 8 + i); else piece(3, s2_, i - (PER - 8));
        SBAR();
        if (i < 7) {
#pragma unroll
          for (int k = 0; k < 4; ++k) { cw[2 * k] += bf_lo(wx[i][k]); cw[2 * k + 1] += bf_hi(wx[i][k]); }
        }
        SBAR();
      }
    } else {
      if (s + 1 < K1_NST) issue_b(s + 1, s1_);

      if (s + 2 < K1_NST) issue_a(s + 2, s2_);
    }
    if (s == 1) {
      pf4((const char*)a.Wd + (size_t)(64 * wd_kp + 16 * h + ((lane >> 1) & 15)) * 896 + (wdjob ? wd_cg : 0) * 128 + (lane & 1) * 64, dummy);
#pragma unroll
      for (int k = 0; k < 8; ++k) {
        cw[k] += __builtin_bit_cast(float, __builtin_amdgcn_update_dpp(0, __builtin_bit_cast(int, cw[k]), 0x128, 0xf, 0xf, true));
        const auto p16 = __builtin_amdgcn_permlane16_swap(__float_as_uint(cw[k]), __float_as_uint(cw[k]), false, false);
        cw[k] = __uint_as_float(p16[0]) + __uint_as_float(p16[1]);
        const auto p32 = __builtin_amdgcn_permlane32_swap(__float_as_uint(cw[k]), __float_as_uint(cw[k]), false, false);
        cw[k] = __uint_as_float(p32[0]) + __uint_as_float(p32[1]);
      }
      if (lane < 8) {
        *(LAS f32x4*)(lds + L_CS + h * 256 + lane * 32) = f32x4{cw[0], cw[1], cw[2], cw[3]};
        *(LAS f32x4*)(lds + L_CS + h * 256 + lane * 32 + 16) = f32x4{cw[4], cw[5], cw[6], cw[7]};
      }
    }
    if (s == 2 && h == 0 && wjob) {
      const float t = (*(LAS float*)(lds + L_CS + lane * 4) + *(LAS float*)(lds + L_CS + 256 + lane * 4)) +
                      (*(LAS float*)(lds + L_CS + 512 + lane * 4) + *(LAS float*)(lds + L_CS + 768 + lane * 4));
      __hip_atomic_store((GAS unsigned long long*)(ws + WS_CW + (size_t)((wj_mat * 2 + wj_kh) * F + 448 * nq + 64 * wj_cb + lane) * 8),
                         ((unsigned long long)tag1 << 32) | __float_as_uint(t), __ATOMIC_RELAXED, __HIP_MEMORY_SCOPE_AGENT);
    }
    if (s == 4 && h == 0 && lane == 0) st_flag(ws + WS_ARRIVE + blockIdx.x * 64, tag1);
    if (s == 5) {
      if (wdjob) {
        const u32x4 rwd = mk_srd(a.Wd, F * D * 2);
        const int rs = (lane & 15) >> 1, half = lane & 1, b = lane >> 4;
        const int vq = rs * 896 + (64 * wd_cg + 16 * b + 8 * half) * 2;
#pragma unroll
        for (int i = 0; i < 2; ++i) dma16(rwd, l0 + s2_ * K1_STAGE + (2 * h + i) * 1024, vq, (64 * wd_kp + 8 * (2 * h + i)) * 896);
      }
    }
    const int gc = 64 * h + lane;
    constexpr int NWC = TBc ? 128 : 256, CPM = NWC / 2;
    const bool isw = gc < NWC;
    const char* p_st = ws + WS_ST + (size_t)(mt * 128 + 32 * h + (lane & 31)) * 8;
    const char* p_c0 = ws + (isw ? WS_CW + (size_t)((gc / CPM) * 2 * F + f0 + gc % CPM) * 8 : WS_CD + (size_t)(a0 * 16 + (gc - NWC)) * 8);
    const char* p_c1 = isw ? p_c0 + (size_t)F * 8 : p_c0;
    const char* p_d = ws + WS_CD + (size_t)(a0 * 16 + (lane & 31)) * 8;
    if (s == 5)
      asm volatile("global_load_dwordx2 %0, %4, off sc1\n\tglobal_load_dwordx2 %1, %5, off sc1\n\tglobal_load_dwordx2 %2, %6, off sc1\n\tglobal_load_dwordx2 %3, %7, off sc1"
                   : "=&v"(g0), "=&v"(g1), "=&v"(g2), "=&v"(g3) : "v"(p_st), "v"(p_c0), "v"(p_c1), "v"(p_d) : "memory");
    if (s == 6) {
      WAITV(0);
      asm volatile("" : "+v"(g0), "+v"(g1), "+v"(g2), "+v"(g3));
      auto ld_gran = [&](const char* p) { return __hip_atomic_load((const GAS unsigned long long*)p, __ATOMIC_RELAXED, __HIP_MEMORY_SCOPE_AGENT); };
      for (unsigned sp = 0; sp < SPIN_MAX; ++sp) {
        if (__all((unsigned)(g0 >> 32) == tag1 && (unsigned)(g1 >> 32) == tag1 && (unsigned)(g2 >> 32) == tag1 && (unsigned)(g3 >> 32) == tag1)) break;
        __builtin_amdgcn_s_sleep(2);
        g0 = ld_gran(p_st); g1 = ld_gran(p_c0); g2 = ld_gran(p_c1); g3 = ld_gran(p_d);
      }
      if (lane < 32) *(LAS f32x2*)(lds + L_STATS + (32 * h + lane) * 8) = f32x2{bf_lo((unsigned)g0), 1.0f / bf_hi((unsigned)g0)};
      *(LAS float*)(lds + L_CS + gc * 4) = isw ? __uint_as_float((unsigned)g1) + __uint_as_float((unsigned)g2) : __uint_as_float((unsigned)g1);
      if (!TBc && h == 0 && lane < 32) *(LAS float*)(lds + L_CS + (256 + lane) * 4) = __uint_as_float((unsigned)g3);
    }
    WAITL0();
    if (s == 1) { WAITV(9); } else if (s + 2 < K1_NST) { WAITV(8); } else if (s == 5) { if (wdjob) { WAITV(6); } else { WAITV(4); } }
    STAMP(7 + s);
    BAR();
  }
  __builtin_amdgcn_s_setprio(0);
  if (h == 3 && wdjob) {
    const int i16 = lane & 15, g = lane >> 4, q = i16 >> 2, p = i16 & 3;
    const rsrc_t rt = mk_rsrc(ws + WS_WDT, 28 * 56 * 1024);
    lptr b0 = lds + 1 * K1_STAGE + (g >> 1) * 1024 + ((g & 1) * 4 + q) * 32 + p * 8;
#pragma unroll
    for (int kk = 0; kk < 2; ++kk)
#pragma unroll
      for (int u = 0; u < 4; ++u) {
        const bf16x8 f = ldstr(b0 + kk * 4096 + u * 256, b0 + kk * 4096 + u * 256 + 2048);
        __builtin_amdgcn_raw_buffer_store_b128(__builtin_bit_cast(u32x4, f), rt, lane * 16 + ((4 * wd_cg + u) * 56 + 2 * wd_kp + kk) * 1024, 0, STAUX);
      }
    WAITL0();
  }
  BAR();
  p1_helper_epilogue<TB ? 2 : 4>(lds, ws, mt, f0, h, lane);
}

template <int MODE, int NB, int JO>
__device__ __forceinline__ void p2_loader(lptr lds, const Args& a, int xcd, int idx, int r, int m, int n, int lw, int lane) {
  char* ws = a.ws;
  constexpr int PER = 4 + NB;
  const u32x4 rc = mk_srd((r >> 1) ? a.Ci : a.Cr, S * S * 2);
  const u32x4 rb = mk_srd(ws + WS_AB, 2 * 28 * 64 * 1024);
  const u32x4 rh = mk_srd(ws + WS_H, 128 * 56 * 1024);
  const u32x4 rw = mk_srd(ws + WS_WDT, 28 * 56 * 1024);
  const int r8 = lane >> 3, c8 = lane & 7;
  int va[2];
  va[0] = r8 * 4096 + (c8 ^ (r8 & 3)) * 16;
  va[1] = r8 * 4096 + (c8 ^ ((r8 & 3) | 4)) * 16;
  const int vl = lane * 16;
  const int k0 = 1024 * (r & 1), p = r >> 1;
  const int j0 = (lw < 2) ? 4 * lw : 8 + 3 * (lw - 2);
  const unsigned l0 = lds_addr(lds);
  const unsigned dummy = l0 + L_DUMMY + (lw & 1) * 256;
  const int sab = (128 * m + 32 * lw) * 4096 + k0 * 2;
  const int shb = ((8 * m + 2 * lw) * 56 + 14 * r) * 1024;
  const unsigned lab = l0 + 4 * lw * 1024, lbb = l0 + 16384 + j0 * 1024;
  const int sbb = ((p * 28 + 7 * n + (j0 >> 1)) * 64 + (k0 >> 5)) * 1024, swb = ((7 * n + (j0 >> 1)) * 56 + 14 * r) * 1024;
  auto issueAy = [&](int s, int slot) {
    const unsigned so = slot * P_STAGE; const int b = sab + 128 * (s & 15);
#pragma unroll
    for (int i = 0; i < 4; ++i) dma16(rc, lab + so + i * 1024, va[i & 1], b + i * 8 * 4096);
  };
  auto issueAd = [&](int sd, int slot) {
    const unsigned so = slot * P_STAGE; const int b = shb + 2048 * sd;
#pragma unroll
    for (int i = 0; i < 4; ++i) dma16c(rh, lab + so + i * 1024, vl, b + (i >> 1) * 56 * 1024 + (i & 1) * 1024);
  };
  auto issueBy = [&](int s, int slot) {
    const unsigned so = slot * P_STAGE; const int b = sbb + 2048 * (s & 15);
#pragma unroll
    for (int j = 0; j < NB; ++j) dma16c(rb, lbb + so + j * 1024, vl, b + ((j + JO) >> 1) * 65536 + ((j + JO) & 1) * 1024);
  };
  auto issueBd = [&](int sd, int slot) {
    const unsigned so = slot * P_STAGE; const int b = swb + 2048 * sd;
#pragma unroll
    for (int j = 0; j < NB; ++j) dma16c(rw, lbb + so + j * 1024, vl, b + ((j + JO) >> 1) * 57344 + ((j + JO) & 1) * 1024);
  };
  __builtin_amdgcn_s_setprio(0);
  const unsigned tag = get_tag(lds);
  if (MODE != 2 && lw == 3 && blockIdx.x < 196) {
    WAITV(0);
    if (lane == 0) st_flag(ws + WS_FWD + blockIdx.x * 64, tag);
  }
  issueAy(0, 0); issueAy(1, 1);
  if (lw == 3) {
    const int mtp = 8 * (r & 1) + (lane & 7), j = (lane >> 3) & 3;
    const bool act = lane < 32 && ((n & 1) == 0 || j == 3);
    poll_all(ws + WS_FAB + (mtp * 16 + (2 * p + (n >> 1)) * 4 + j) * 64, act, tag);
    if (lane == 0) *(volatile LAS unsigned*)(lds + L_WORDS + 8) = 1u;
  } else lds_spin(lds + L_WORDS + 8);
  STAMP(16);
  issueBy(0, 0);
  {
    const int l = (lane >> 1) < 28 ? (lane >> 1) : 27;
    const int sl = l >= 14, li = 14 * idx + l - 14 * sl;
    const char* bp = ws + WS_AB + ((size_t)(p * 28 + (li >> 4)) * 64 + 32 * (r & 1) + 2 * (4 * lw + sl) + ((li >> 3) & 1)) * 1024 + (li & 7) * 128 + (lane & 1) * 64;
    pf4c(bp, dummy); pf4c(bp + 4 * 1024, dummy);
  }
  issueBy(1, 1);
  __builtin_amdgcn_s_setprio(3);
  WAITV(NB + 2); BAR();
  if (MODE == 0 && lane == 0) {
    const unsigned cnt = __hip_atomic_fetch_add((LAS unsigned*)(lds + L_WORDS + 4), 1u, __ATOMIC_RELAXED, __HIP_MEMORY_SCOPE_WORKGROUP);
    if (cnt == 7u) st_flag(ws + WS_FH + (((xcd & 1) * 8 + (idx & 7)) * 16 + (xcd >> 1) * 4 + (idx >> 3)) * 64, tag);
  }
  issueAy(2, 2); issueBy(2, 2);
  WAITV(PER); BAR();
  issueAy(3, 3); issueBy(3, 3); issueAy(4, 4); issueBy(4, 4);
  WAITV(2 * PER); BAR();
  int slot = 0;
  auto ystep = [&](int s) { issueAy(s + 3, slot); issueBy(s + 3, slot); slot = (slot == 4) ? 0 : slot + 1; WAITV(2 * PER); BAR(); };
  auto dstep = [&](int sd) { issueAd(sd, slot); issueBd(sd, slot); slot = (slot == 4) ? 0 : slot + 1; WAITV(2 * PER); BAR(); };
#pragma unroll 1
  for (int s = 2; s < P_NY - 9; ++s) ystep(s);
  const int cg0 = (112 * n) >> 6, cg1 = (112 * n + 111) >> 6, jj = lane - 4;
  const bool fact = lane < 4 || (jj < 21 && cg0 + jj / 7 <= cg1);
  const char* fp = lane < 4 ? ws + WS_FH + (m * 16 + 4 * r + lane) * 64 : ws + WS_FWD + ((fact ? cg0 + jj / 7 : cg0) * 28 + 7 * r + (fact ? jj % 7 : 0)) * 64;
  unsigned fval = 0;
  if (lw == 3) asm volatile("global_load_dword %0, %1, off sc1" : "+v"(fval) : "v"(fp) : "memory");
#pragma unroll 1
  for (int s = P_NY - 9; s < P_NY - 6; ++s) ystep(s);
  if (lw == 3) {
    asm volatile("" : "+v"(fval));
    if (!__all(!fact || fval == tag)) poll_all(fp, fact, tag);
    if (lane == 0) *(volatile LAS unsigned*)(lds + L_WORDS + 12) = 1u;
  }
#pragma unroll 1
  for (int s = P_NY - 6; s < P_NY - 3; ++s) ystep(s);
  if (lw != 3) lds_spin(lds + L_WORDS + 12);
#pragma unroll 1
  for (int sd = 0; sd < P_ND; ++sd) dstep(sd);
  WAITV(PER); BAR();
  WAITV(0); BAR();
}

template <int MODE>
__global__ __launch_bounds__(512, 2) void fused_kernel(Args a) {
  extern __shared__ __attribute__((aligned(1024))) char smem[];
  lptr lds = (lptr)smem;
  char* ws = a.ws;
  const int tid = threadIdx.x, lane = tid & 63;
  const int wid = __builtin_amdgcn_readfirstlane(tid >> 6);
  int xcd = blockIdx.x & 7, idx = blockIdx.x >> 3;
  if (tid < 256) *(volatile LAS unsigned*)(lds + L_PROF + tid * 4) = 0u;
  __syncthreads();
  STAMP(0); STAMPR(30);
  if (wid == 3) {
    unsigned long long pc;
    asm volatile("s_getpc_b64 %0" : "=s"(pc));
    const unsigned ln = 18 * idx + ((lane >> 1) < 18 ? (lane >> 1) : 17);
    pf4((const char*)(pc & ~127ull) + (ln * 128 < CODE_PF ? ln * 128 : 0) + (lane & 1) * 64, lds_addr(lds) + L_DUMMY);
  }
  if (wid >= 4) __builtin_amdgcn_s_setprio(3);

  if (MODE != 2) {
    const int nq = xcd >> 1;
    const int mt = (xcd & 1) * 8 + (idx & 7), j = idx >> 3;
    const bool tb = j == 3;
    const int grp = nq * 4 + j;
    const int f0 = 448 * nq + 128 * j, a0 = 14 * nq + 2 * j;
    if (wid < 4) {
      if (tb) p1_consumer<MODE, 2, 4>(lds, a, xcd, idx, mt, grp, f0, a0, wid & 1, wid >> 1, lane);
      else p1_consumer<MODE, 4, 1>(lds, a, xcd, idx, mt, grp, f0, a0, wid & 1, wid >> 1, lane);
    } else {
      const int h = wid - 4;
      if (tb) p1_helper<MODE, true>(lds, a, xcd, idx, mt, grp, f0, a0, h, lane);
      else p1_helper<MODE, false>(lds, a, xcd, idx, mt, grp, f0, a0, h, lane);
    }
  }
  if (MODE == 1) { WAITV(0); STAMPR(29); __syncthreads();
    if (tid < 128) { const int w_ = tid / 16, i_ = tid % 16; ((unsigned long long*)(ws + WS_PROF + (1 << 20)))[((size_t)blockIdx.x * 8 + w_) * 32 + i_] = *(volatile LAS unsigned*)(lds + L_PROF + (w_ * 32 + i_) * 4); } }
  if (MODE == 1) return;
  if (MODE == 2) { if (tid < 4) ((LAS unsigned*)(lds + L_WORDS))[tid] = 0u; if (tid == 4) ((LAS unsigned*)(lds + L_WORDS))[4] = 0x51u; __syncthreads(); }

  launder(a, xcd, idx);
  ws = a.ws;
  const int p1_mt = (xcd & 1) * 8 + (idx & 7), p1_grp = (xcd >> 1) * 4 + (idx >> 3);
  const int r = xcd >> 1, m = 8 * (xcd & 1) + (idx & 7), n = idx >> 3;
  const int tile = m * 4 + n;
  const int i16 = lane & 15, g = lane >> 4;
  f32x4 accY[7][2], acc[7][2];
  if (wid == 7) {
    p2_loader<MODE, 3, 1>(lds, a, xcd, idx, r, m, n, 3, lane);
  } else if (wid == 6) {
    p2_loader<MODE, 3, 0>(lds, a, xcd, idx, r, m, n, 2, lane);
  } else if (wid >= 4) {
    p2_loader<MODE, 4, 0>(lds, a, xcd, idx, r, m, n, wid - 4, lane);
  } else {
    const int w = wid;
    const int rowt = 32 * w + 8 * (i16 >> 2) + (i16 & 3);
    const int vt0 = rowt * 128 + ((g) ^ (i16 & 7)) * 16, vt1 = rowt * 128 + ((g + 4) ^ (i16 & 7)) * 16;
    const int ve = 16384 + lane * 16;
    const int vh = (4 * w) * 1024 + lane * 16;
    bf16x8 T0[4], T1[4], E[14];
    auto ldTy = [&](bf16x8 (&T)[4], lptr st) {
      T[0] = ldsr128(st + vt0); T[1] = ldsr128(st + vt0 + 512); T[2] = ldsr128(st + vt1); T[3] = ldsr128(st + vt1 + 512);
    };
    auto ldTd = [&](bf16x8 (&T)[4], lptr st) {
      T[0] = ldsr128(st + vh); T[1] = ldsr128(st + vh + 2048); T[2] = ldsr128(st + vh + 1024); T[3] = ldsr128(st + vh + 3072);
    };
    auto ldE = [&](int j, lptr st) { E[j] = ldsr128(st + ve + ((j % 7) * 2 + j / 7) * 1024); };
    auto mm = [&](int j, bf16x8 (&T)[4]) {
      mfma_ip(acc[j % 7][0], E[j], T[(j / 7) * 2]);
      mfma_ip(acc[j % 7][1], E[j], T[(j / 7) * 2 + 1]);
    };
#define GHEAD 3
    constexpr int GH = GHEAD;
    auto ldT1 = [&](bf16x8 (&T)[4], lptr st, int i, int NT) {
      if (NT == 0) T[i] = ldsr128(st + (i >= 2 ? vt1 : vt0) + (i & 1) * 512);
      else T[i] = ldsr128(st + vh + (i & 1) * 2048 + (i >> 1) * 1024);
    };
    auto rest = [&](bf16x8 (&Tc)[4], bf16x8 (&Tn)[4], lptr sn, int NT) {
#pragma unroll
      for (int j = GH; j < 14; ++j) {
        const int k = j - GH;
        mfma_ip(acc[j % 7][0], E[j], Tc[(j / 7) * 2]); SBAR();
        if (NT != 2 && k < 4 + GH) { if (k < 4) ldT1(Tn, sn, k, NT); else ldE(k - 4, sn); SBAR(); }
        mfma_ip(acc[j % 7][1], E[j], Tc[(j / 7) * 2 + 1]); SBAR();
        if (NT != 2) ldE(j, sn); else FILL();
        SBAR();
      }
    };
    auto head = [&](bf16x8 (&Tc)[4]) {
#pragma unroll
      for (int j = 0; j < GH; ++j) { mm(j, Tc); FILL(); }
      SBAR();
    };
#pragma unroll
    for (int u = 0; u < 7; ++u) { acc[u][0] = f32x4{0, 0, 0, 0}; acc[u][1] = f32x4{0, 0, 0, 0}; }
#pragma unroll
    for (int u = 0; u < 7; ++u) { asm volatile("" : "+v"(acc[u][0])); asm volatile("" : "+v"(acc[u][1])); }
    PAD_MFMA_IN();
    if (w != r) {
      const rsrc_t rp = mk_rsrc(ws + WS_PD + (size_t)((tile * 4 + r) * 4 + w) * 7168, 7168);
      const u32x4 sent = {0xffffffffu, 0xffffffffu, 0xffffffffu, 0xffffffffu};
#pragma unroll
      for (int u = 0; u < 7; ++u) {
        __builtin_amdgcn_raw_buffer_store_b128(sent, rp, u * 1024 + lane * 16, 0, 16);
      }
    }
    STAMP(16); STAMPR(28);
    BAR();
    STAMP(17); STAMPR(27);
    ldTy(T0, lds);
#pragma unroll
    for (int j = 0; j < 14; ++j) ldE(j, lds);
    SBAR();
    head(T0);
    WAITL0(); BAR();
    int slot = 1;
#pragma unroll 1
    for (int s = 0; s < P_NY - 2; s += 2) {
      lptr sn = lds + slot * P_STAGE; slot = (slot == 4) ? 0 : slot + 1;
      rest(T0, T1, sn, 0); head(T1);
      WAITL0(); BAR();
      sn = lds + slot * P_STAGE; slot = (slot == 4) ? 0 : slot + 1;
      rest(T1, T0, sn, 0); head(T0);
      WAITL0(); BAR();
      if (YREP > 1 && s == 14) { STAMP(23); }
      if (MODE == 0 && s == 0) {
        WAITV(0);
        if (lane == 0) {
          const unsigned cnt = __hip_atomic_fetch_add((LAS unsigned*)(lds + L_WORDS + 4), 1u, __ATOMIC_RELAXED, __HIP_MEMORY_SCOPE_WORKGROUP);
          if (cnt == 7u) st_flag(ws + WS_FH + (p1_mt * 16 + p1_grp) * 64, get_tag(lds));
        }
      }
    }
    {
      lptr sn = lds + slot * P_STAGE; slot = (slot == 4) ? 0 : slot + 1;
      rest(T0, T1, sn, 0); head(T1);
      WAITL0(); BAR();
      sn = lds + slot * P_STAGE; slot = (slot == 4) ? 0 : slot + 1;
      rest(T1, T0, sn, 1);
    }
    PAD_MFMA_OUT();
    STAMP(18);
    {
      const float sg = (r >> 1) ? -1.0f : 1.0f;
#pragma unroll
      for (int u = 0; u < 7; ++u) { accY[u][0] = acc[u][0] * sg; accY[u][1] = acc[u][1] * sg; acc[u][0] = f32x4{0, 0, 0, 0}; acc[u][1] = f32x4{0, 0, 0, 0}; }
      if (w != r) {
        const rsrc_t rp = mk_rsrc(ws + WS_PY + (size_t)((tile * 4 + r) * 4 + w) * 7168, 7168);
#pragma unroll
        for (int u = 0; u < 7; ++u) {
          const u32x4 o = {pk_bf16(accY[u][0][0], accY[u][0][1]), pk_bf16(accY[u][0][2], accY[u][0][3]),
                           pk_bf16(accY[u][1][0], accY[u][1][1]), pk_bf16(accY[u][1][2], accY[u][1][3])};
          __builtin_amdgcn_raw_buffer_store_b128(o, rp, u * 1024 + lane * 16, 0, 16);
        }
      }
    }
#pragma unroll
    for (int u = 0; u < 7; ++u) { asm volatile("" : "+v"(acc[u][0])); asm volatile("" : "+v"(acc[u][1])); }
    PAD_MFMA_IN();
    head(T0);
    WAITL0(); BAR();
#pragma unroll 1
    for (int s = 0; s < P_ND - 1; s += 2) {
      lptr sn = lds + slot * P_STAGE; slot = (slot == 4) ? 0 : slot + 1;
      rest(T0, T1, sn, 1); head(T1);
      WAITL0(); BAR();
      if (s == 0 && w != r) {
        WAITV(0);
        const unsigned tag = get_tag(lds);
        if (lane == 0) st_flag(ws + WS_FY + ((tile * 4 + r) * 4 + w) * 64, tag);
      }
      sn = lds + slot * P_STAGE; slot = (slot == 4) ? 0 : slot + 1;
      rest(T1, T0, sn, 1); head(T0);
      WAITL0(); if (s + 2 < P_ND - 1) BAR();
    }
    rest(T0, T1, lds, 2);
    PAD_MFMA_OUT();
    STAMP(19); STAMPR(29);
    if (w != r) {
      const rsrc_t rp = mk_rsrc(ws + WS_PD + (size_t)((tile * 4 + r) * 4 + w) * 7168, 7168);
#pragma unroll
      for (int u = 0; u < 7; ++u) {
        u32x4 o = {pk_bf16(acc[u][0][0], acc[u][0][1]), pk_bf16(acc[u][0][2], acc[u][0][3]),
                   pk_bf16(acc[u][1][0], acc[u][1][1]), pk_bf16(acc[u][1][2], acc[u][1][3])};
#pragma unroll
        for (int k = 0; k < 4; ++k) o[k] = o[k] < 0xfffffffeu ? o[k] : 0xfffffffeu;
        __builtin_amdgcn_raw_buffer_store_b128(o, rp, u * 1024 + lane * 16, 0, 16);
      }
    } else {
#pragma unroll
      for (int uu = 0; uu < 7; ++uu) {
        *(LAS f32x4*)(lds + (uu * 4 + 0) * 1024 + lane * 16) = accY[uu][0];
        *(LAS f32x4*)(lds + (uu * 4 + 1) * 1024 + lane * 16) = accY[uu][1];
        *(LAS f32x4*)(lds + (uu * 4 + 2) * 1024 + lane * 16) = acc[uu][0];
        *(LAS f32x4*)(lds + (uu * 4 + 3) * 1024 + lane * 16) = acc[uu][1];
      }
      WAITL0();
    }
    BAR();
  }
  if (wid >= 4) {
    __builtin_amdgcn_s_setprio(0);
    const int hw = wid - 4;
    const int nu = hw < 3 ? 2 : 1;
    const unsigned tag = get_tag(lds);
    const int tok0 = 128 * m + 32 * r + 8 * (i16 >> 2) + (i16 & 3);
    const rsrc_t ry = mk_rsrc(ws + WS_PY, 64 * 16 * 7168), rd = mk_rsrc(ws + WS_PD, 64 * 16 * 7168);
    u32x2 x0[2], x1[2];
    u32x4 fy[2][3], fd[2][3];
#pragma unroll
    for (int k = 0; k < 2; ++k) {
      const int u = 2 * hw + (k < nu ? k : 0);
      const int ecol = 112 * n + 16 * u + 4 * g;
      x0[k] = *(const u32x2*)(a.x + (size_t)tok0 * D + ecol); x1[k] = *(const u32x2*)(a.x + (size_t)(tok0 + 4) * D + ecol);
    }
    {
      const int rr = (r + 1 + (lane % 3)) & 3;
      poll_all(ws + WS_FY + ((tile * 4 + rr) * 4 + r) * 64, lane < 3, tag);
    }
#pragma unroll
    for (int k = 0; k < 2; ++k)
#pragma unroll
      for (int i = 0; i < 3; ++i)
        fy[k][i] = __builtin_amdgcn_raw_buffer_load_b128(ry, ((tile * 4 + ((r + 1 + i) & 3)) * 4 + r) * 7168 + (2 * hw + (k < nu ? k : 0)) * 1024 + lane * 16, 0, 16);
    STAMP(20);
    if (MODE == 0 && blockIdx.x == 0 && hw == 3) {
#pragma unroll 1
      for (int i = 0; i < 4; ++i) poll_all(ws + WS_ARRIVE + (64 * i + lane) * 64, true, tag);
      if (lane == 0) st_flag(ws + WS_NONCE, tag);
    }
    for (unsigned sp = 0; sp < (SPIN_MAX >> 3); ++sp) {
      const unsigned v = lane < 3 ? __builtin_amdgcn_raw_buffer_load_b32(rd, ((tile * 4 + ((r + 1 + lane) & 3)) * 4 + r) * 7168 + 6 * 1024 + 63 * 16 + 12, 0, 16) : 0u;
      if (__all(v != 0xffffffffu)) break;
      __builtin_amdgcn_s_sleep(1);
    }
    for (unsigned sp = 0; sp < (SPIN_MAX >> 3); ++sp) {
      bool ok = true;
#pragma unroll
      for (int k = 0; k < 2; ++k)
#pragma unroll
        for (int i = 0; i < 3; ++i) {
          fd[k][i] = __builtin_amdgcn_raw_buffer_load_b128(rd, ((tile * 4 + ((r + 1 + i) & 3)) * 4 + r) * 7168 + (2 * hw + (k < nu ? k : 0)) * 1024 + lane * 16, 0, 16);
        }
#pragma unroll
      for (int k = 0; k < 2; ++k)
#pragma unroll
        for (int i = 0; i < 3; ++i)
          ok = ok && fd[k][i][0] != 0xffffffffu && fd[k][i][1] != 0xffffffffu && fd[k][i][2] != 0xffffffffu && fd[k][i][3] != 0xffffffffu;
      if (__all(ok)) break;
      __builtin_amdgcn_s_sleep(2);
    }
    STAMP(21); STAMPR(29);
    BAR();
#pragma unroll
    for (int k = 0; k < 2; ++k) {
      if (k < nu) {
        const int u = 2 * hw + k;
        const int ecol = 112 * n + 16 * u + 4 * g;
        f32x4 y0 = *(LAS f32x4*)(lds + (u * 4 + 0) * 1024 + lane * 16), y1 = *(LAS f32x4*)(lds + (u * 4 + 1) * 1024 + lane * 16);
        f32x4 d0 = *(LAS f32x4*)(lds + (u * 4 + 2) * 1024 + lane * 16), d1 = *(LAS f32x4*)(lds + (u * 4 + 3) * 1024 + lane * 16);
#pragma unroll
        for (int i = 0; i < 3; ++i) {
          y0 += f32x4{bf_lo(fy[k][i][0]), bf_hi(fy[k][i][0]), bf_lo(fy[k][i][1]), bf_hi(fy[k][i][1])};
          y1 += f32x4{bf_lo(fy[k][i][2]), bf_hi(fy[k][i][2]), bf_lo(fy[k][i][3]), bf_hi(fy[k][i][3])};
          d0 += f32x4{bf_lo(fd[k][i][0]), bf_hi(fd[k][i][0]), bf_lo(fd[k][i][1]), bf_hi(fd[k][i][1])};
          d1 += f32x4{bf_lo(fd[k][i][2]), bf_hi(fd[k][i][2]), bf_lo(fd[k][i][3]), bf_hi(fd[k][i][3])};
        }
        const u32x2 o0 = {pk_bf16(bf_lo(x0[k][0]) + y0[0] * silu_f(d0[0]), bf_hi(x0[k][0]) + y0[1] * silu_f(d0[1])),
                          pk_bf16(bf_lo(x0[k][1]) + y0[2] * silu_f(d0[2]), bf_hi(x0[k][1]) + y0[3] * silu_f(d0[3]))};
        const u32x2 o1 = {pk_bf16(bf_lo(x1[k][0]) + y1[0] * silu_f(d1[0]), bf_hi(x1[k][0]) + y1[1] * silu_f(d1[1])),
                          pk_bf16(bf_lo(x1[k][1]) + y1[2] * silu_f(d1[2]), bf_hi(x1[k][1]) + y1[3] * silu_f(d1[3]))};
        *(u32x2*)(a.out + (size_t)tok0 * D + ecol) = o0;
        *(u32x2*)(a.out + (size_t)(tok0 + 4) * D + ecol) = o1;
      }
    }
  }
  WAITV(0);
  STAMP(26); STAMPR(31);
  __syncthreads();
  if (tid == 0 && (blockIdx.x == 0 || blockIdx.x == 101 || blockIdx.x == 255)) {
    volatile LAS unsigned* pp = (volatile LAS unsigned*)(lds + L_PROF);
    unsigned long long r1 = __builtin_amdgcn_s_memrealtime();
    unsigned long long r0 = ((unsigned long long*)(ws + WS_PROF))[((size_t)blockIdx.x * 8) * 32 + 30];
    printf("blk %d: real %d (x10ns) clk %d | w0: j%d B0 %d B1 %d loopend %d abflag %d B8 %d epi %d p2rdy %d B'0 %d yend %d dend %d | w4: first %d st0 %d go %d [20]%d [21]%d\n", (int)blockIdx.x, (int)(r1 - r0), (int)(pp[26] - pp[0]),
           (int)(pp[1]-pp[0]), (int)(pp[2]-pp[0]), (int)(pp[7]-pp[0]), (int)(pp[3]-pp[0]), (int)(pp[5]-pp[0]), (int)(pp[14]-pp[0]), (int)(pp[4]-pp[0]), (int)(pp[16]-pp[0]), (int)(pp[17]-pp[0]), (int)(pp[18]-pp[0]), (int)(pp[19]-pp[0]),
           (int)(pp[128+1]-pp[128]), (int)(pp[128+7]-pp[128]), (int)(pp[128+16]-pp[128]), (int)(pp[128+20]-pp[128]), (int)(pp[128+21]-pp[128]));
  }
  if (tid < 216) { const int w_ = tid / 27, i_ = tid % 27; ((unsigned long long*)(ws + WS_PROF))[((size_t)blockIdx.x * 8 + w_) * 32 + i_] = *(volatile LAS unsigned*)(lds + L_PROF + (w_ * 32 + i_) * 4); }
}

#define SPLIT 0
extern "C" void kernel_launch(void* const* d_in, const int* in_sizes, int n_in, void* d_out, int out_size, void* d_ws, size_t ws_size,
                              hipStream_t stream) {
  Args a;
  a.x = (const u16*)d_in[0]; a.Cr = (const u16*)d_in[1]; a.Ci = (const u16*)d_in[2]; a.Dr = (const u16*)d_in[3]; a.Di = (const u16*)d_in[4];
  a.Wg = (const u16*)d_in[6]; a.Wu = (const u16*)d_in[7]; a.Wd = (const u16*)d_in[8];
  a.ws = (char*)d_ws; a.out = (u16*)d_out;
  (void)hipFuncSetAttribute((const void*)fused_kernel<0>, hipFuncAttributeMaxDynamicSharedMemorySize, L_TOTAL);
  fused_kernel<0><<<256, 512, L_TOTAL, stream>>>(a);
}
```

```cpp
#include <hip/hip_runtime.h>
#include <hip/hip_bf16.h>
#include <stdint.h>

typedef unsigned short u16;
typedef __bf16 bf16x8 __attribute__((ext_vector_type(8)));
typedef short s16x8 __attribute__((ext_vector_type(8)));
typedef float f32x4 __attribute__((ext_vector_type(4)));
typedef float f32x2 __attribute__((ext_vector_type(2)));
typedef unsigned u32x4 __attribute__((ext_vector_type(4)));
typedef unsigned u32x2 __attribute__((ext_vector_type(2)));
#define LAS __attribute__((address_space(3)))
#define GAS __attribute__((address_space(1)))
typedef LAS char* lptr;
typedef __amdgpu_buffer_rsrc_t rsrc_t;

constexpr int S = 2048, D = 448, F = 1792;

constexpr size_t WS_NONCE  = 0;
constexpr size_t WS_ARRIVE = 4096;
constexpr size_t WS_FAB    = 32768;
constexpr size_t WS_FH     = 49152;
constexpr size_t WS_FWD    = 65536;
constexpr size_t WS_FY     = 81920;
constexpr size_t WS_FD     = 147456;
constexpr size_t WS_ST     = 212992;
constexpr size_t WS_CW     = 229376;
constexpr size_t WS_CD     = 286720;
constexpr size_t WS_AB     = 327680;
constexpr size_t WS_H      = WS_AB + (size_t)2 * 28 * 64 * 1024;
constexpr size_t WS_WDT    = WS_H + (size_t)128 * 56 * 1024;
constexpr size_t WS_PY     = WS_WDT + (size_t)28 * 56 * 1024;
constexpr size_t WS_PD     = WS_PY + (size_t)64 * 16 * 7 * 1024;

#define STAMP(i)
#define STAMPR(i)
#define SBAR() __builtin_amdgcn_sched_barrier(0)
#define BAR() __builtin_amdgcn_s_barrier()
#define WAITV(n) asm volatile("s_waitcnt vmcnt(%0)" ::"n"(n) : "memory")
#define WAITL0() __builtin_amdgcn_s_waitcnt(0xC07F)
#define CODE_PF 49152
#define PF0 2
#define STAUX 16
#define SPIN_MAX (1u << 18)

__device__ __forceinline__ rsrc_t mk_rsrc(const void* p, unsigned bytes) {
  return __builtin_amdgcn_make_buffer_rsrc((void*)p, 0, bytes, 0x00020000);
}
__device__ __forceinline__ u32x4 mk_srd(const void* p, unsigned bytes) {
  const unsigned long long a = (unsigned long long)p;
  return u32x4{(unsigned)a, (unsigned)(a >> 32) & 0xffffu, bytes, 0x00020000u};
}
__device__ __forceinline__ unsigned lds_addr(lptr p) { return (unsigned)(size_t)p; }
__device__ __forceinline__ void dma16(const u32x4& srd, unsigned dst, int voff, int soff) {
  asm volatile("s_mov_b32 m0, %0\n\ts_nop 0\n\tbuffer_load_dwordx4 %1, %2, %3 offen lds" ::"s"(dst), "v"(voff), "s"(srd), "s"(soff) : "memory");
}
__device__ __forceinline__ void dma16c(const u32x4& srd, unsigned dst, int voff, int soff) {
  asm volatile("s_mov_b32 m0, %0\n\ts_nop 0\n\tbuffer_load_dwordx4 %1, %2, %3 offen sc1 lds" ::"s"(dst), "v"(voff), "s"(srd), "s"(soff) : "memory");
}
__device__ __forceinline__ void pf4c(const char* p, unsigned dst) {
  asm volatile("s_mov_b32 m0, %0\n\ts_nop 0\n\tglobal_load_lds_dword %1, off sc1" ::"s"(dst), "v"(p) : "memory");
}
__device__ __forceinline__ void pf4(const char* p, unsigned dst) {
  asm volatile("s_mov_b32 m0, %0\n\ts_nop 0\n\tglobal_load_lds_dword %1, off" ::"s"(dst), "v"(p) : "memory");
}
__device__ __forceinline__ bf16x8 ldsr128(lptr p) { return *(const LAS bf16x8*)p; }
__device__ __forceinline__ bf16x8 ldstr(lptr p0, lptr p1) {
  typedef short v4i16_t __attribute__((ext_vector_type(4)));
  v4i16_t lo = __builtin_amdgcn_ds_read_tr16_b64_v4i16((LAS v4i16_t*)p0);
  v4i16_t hi = __builtin_amdgcn_ds_read_tr16_b64_v4i16((LAS v4i16_t*)p1);
  s16x8 v = __builtin_shufflevector(lo, hi, 0, 1, 2, 3, 4, 5, 6, 7);
  return __builtin_bit_cast(bf16x8, v);
}
__device__ __forceinline__ f32x4 mfma(bf16x8 a, bf16x8 b, f32x4 c) { return __builtin_amdgcn_mfma_f32_16x16x32_bf16(a, b, c, 0, 0, 0); }
__device__ __forceinline__ void mfma_ip(f32x4& c, const bf16x8& a, const bf16x8& b) {
  asm volatile("v_mfma_f32_16x16x32_bf16 %0, %1, %2, %0" : "+v"(c) : "v"(a), "v"(b));
}
#define FILL() asm volatile("s_nop 0")
#define PAD_MFMA_OUT() asm volatile("s_nop 7\n\ts_nop 3")
#define PAD_MFMA_IN() asm volatile("s_nop 3")
__device__ __forceinline__ unsigned pk_bf16(float lo, float hi) {
  typedef __bf16 bf16x2 __attribute__((ext_vector_type(2)));
  f32x2 v = {lo, hi};
  bf16x2 b = __builtin_convertvector(v, bf16x2);
  return __builtin_bit_cast(unsigned, b);
}
__device__ __forceinline__ float bf_lo(unsigned v) { return __uint_as_float(v << 16); }
__device__ __forceinline__ float bf_hi(unsigned v) { return __uint_as_float(v & 0xffff0000u); }
__device__ __forceinline__ float rbf(float f) { return bf_lo(pk_bf16(f, 0.f)); }
__device__ __forceinline__ float silu_f(float x) {
  float e = __builtin_amdgcn_exp2f(x * -1.4426950409f);
  return x * __builtin_amdgcn_rcpf(1.0f + e);
}
template <int N>
__device__ __forceinline__ void wave_sums(float (&v)[N]) {
#define DPP_STEP(ctrl, rmask) _Pragma("unroll") for (int i = 0; i < N; ++i) \
    v[i] += __builtin_bit_cast(float, __builtin_amdgcn_update_dpp(0, __builtin_bit_cast(int, v[i]), ctrl, rmask, 0xf, true));
  DPP_STEP(0x111, 0xf)
  DPP_STEP(0x112, 0xf)
  DPP_STEP(0x114, 0xf)
  DPP_STEP(0x118, 0xf)
  DPP_STEP(0x142, 0xa)
  DPP_STEP(0x143, 0xc)
#undef DPP_STEP
#pragma unroll
  for (int i = 0; i < N; ++i) v[i] = __builtin_bit_cast(float, __builtin_amdgcn_readlane(__builtin_bit_cast(int, v[i]), 63));
}
__device__ __forceinline__ unsigned ld_flag(const void* p) { return __hip_atomic_load((const GAS unsigned*)p, __ATOMIC_RELAXED, __HIP_MEMORY_SCOPE_AGENT); }
__device__ __forceinline__ void st_flag(void* p, unsigned v) { __hip_atomic_store((GAS unsigned*)p, v, __ATOMIC_RELAXED, __HIP_MEMORY_SCOPE_AGENT); }
__device__ __forceinline__ unsigned get_tag(lptr lds);
__device__ __forceinline__ void poll_all(const char* p, bool active, unsigned tag) {
  for (unsigned sp = 0; sp < SPIN_MAX; ++sp) {
    const unsigned v = active ? ld_flag(p) : tag;
    if (__all(v == tag)) break;
    __builtin_amdgcn_s_sleep(2);
  }
}
__device__ __forceinline__ void lds_spin(lptr p) {
  for (unsigned sp = 0; sp < SPIN_MAX * 4; ++sp) {
    if (*(volatile LAS unsigned*)p != 0u) break;
    __builtin_amdgcn_s_sleep(2);
  }
}

constexpr int K1_STAGE = 53248, K1_NST = 7, K1_WO = 16384;
constexpr int L_MISC = 3 * K1_STAGE;
constexpr int L_STATS = L_MISC;
constexpr int L_CS = L_MISC + 1024;
constexpr int L_WORDS = L_CS + 18 * 64;
constexpr int L_JOBS = L_WORDS + 64;
constexpr int L_DUMMY = L_WORDS + 128;
constexpr int L_TOTAL = L_DUMMY + 512;
#define YREP 1
constexpr int P_STAGE = 30720, P_NY = 16 * YREP, P_ND = 7, P_NST = P_NY + P_ND;
static_assert(L_TOTAL <= 163840 && 5 * P_STAGE <= L_MISC, "LDS");

__device__ __forceinline__ unsigned get_tag(lptr lds) { return *(volatile LAS unsigned*)(lds + L_WORDS + 16); }

struct Args {
  const u16 *x, *Cr, *Ci, *Dr, *Di, *Wg, *Wu, *Wd;
  char* ws;
  u16* out;
};

__device__ __forceinline__ void launder(Args& a, int& xcd, int& idx) {
  asm volatile("" : "+s"(a.x), "+s"(a.Cr), "+s"(a.Ci), "+s"(a.Dr), "+s"(a.Di));
  asm volatile("" : "+s"(a.Wg), "+s"(a.Wu), "+s"(a.Wd), "+s"(a.ws), "+s"(a.out));
  asm volatile("" : "+s"(xcd), "+s"(idx));
}

struct StartJobs { unsigned pay0, pay1; float ds0, ds1; };
template <int MODE>
__device__ __forceinline__ StartJobs start_jobs(lptr lds, const Args& a, int xcd, int idx, int mt, int grp, int h, int lane) {
  const int nq = xcd >> 1;

  const int srow = mt * 128 + 8 * grp + 2 * h;
  u32x4 v[4] = {{0, 0, 0, 0}, {0, 0, 0, 0}, {0, 0, 0, 0}, {0, 0, 0, 0}};
  if (lane < 56) {
    v[0] = *(const u32x4*)(a.x + (size_t)srow * D + lane * 8);
    v[1] = *(const u32x4*)(a.x + (size_t)(srow + 1) * D + lane * 8);
    const u16* dp = (nq >= 2 ? a.Di : a.Dr) + (size_t)(224 * (nq & 1) + 7 * idx + h) * D + lane * 8;
    v[2] = *(const u32x4*)dp;
    v[3] = *(const u32x4*)(dp + (h < 3 ? 4 * D : 0));
  }

  float f[4][8], sm[4];
#pragma unroll
  for (int r = 0; r < 4; ++r) {
#pragma unroll
    for (int i = 0; i < 4; ++i) { f[r][2 * i] = bf_lo(v[r][i]); f[r][2 * i + 1] = bf_hi(v[r][i]); }
    sm[r] = ((f[r][0] + f[r][1]) + (f[r][2] + f[r][3])) + ((f[r][4] + f[r][5]) + (f[r][6] + f[r][7]));
  }
  wave_sums<4>(sm);
  float q[2], mean[2];
#pragma unroll
  for (int r = 0; r < 2; ++r) {
    mean[r] = sm[r] * (1.0f / D);
    float t0 = 0.f, t1 = 0.f;
#pragma unroll
    for (int i = 0; i < 4; ++i) {
      const float d0 = f[r][2 * i] - mean[r], d1 = f[r][2 * i + 1] - mean[r];
      t0 = __builtin_fmaf(d0, d0, t0); t1 = __builtin_fmaf(d1, d1, t1);
    }
    q[r] = lane < 56 ? t0 + t1 : 0.f;
  }
  wave_sums<2>(q);
  StartJobs j;
  j.pay0 = pk_bf16(mean[0], rbf(__builtin_sqrtf(rbf(rbf(q[0] * (1.0f / D)) + 1e-5f))));
  j.pay1 = pk_bf16(mean[1], rbf(__builtin_sqrtf(rbf(rbf(q[1] * (1.0f / D)) + 1e-5f))));
  j.ds0 = sm[2]; j.ds1 = sm[3];
  return j;
}
__device__ __forceinline__ void stash_jobs(lptr lds, const StartJobs& j, int h, int lane) {
  if (lane == 0) *(LAS u32x4*)(lds + L_JOBS + h * 16) = u32x4{j.pay0, j.pay1, __float_as_uint(j.ds0), __float_as_uint(j.ds1)};
}
__device__ __forceinline__ void publish_jobs(lptr lds, char* ws, unsigned tag1, int xcd, int idx, int mt, int grp, int h, int lane) {
  const int nq = xcd >> 1;
  if (lane < 2 || lane == 2 || (lane == 3 && h < 3)) {
    const unsigned v = *(LAS unsigned*)(lds + L_JOBS + h * 16 + lane * 4);
    const size_t off = lane < 2 ? WS_ST + (size_t)(mt * 128 + 8 * grp + 2 * h + lane) * 8 : WS_CD + (size_t)(224 * nq + 7 * idx + h + 4 * (lane - 2)) * 8;
    __hip_atomic_store((GAS unsigned long long*)(ws + off), ((unsigned long long)tag1 << 32) | v, __ATOMIC_RELAXED, __HIP_MEMORY_SCOPE_AGENT);
  }
}

__device__ __forceinline__ void h_batch(const f32x4 (&g)[2][2], const f32x4 (&u)[2][2], const f32x4 (&cg)[2], const f32x4 (&cu)[2], const f32x2 (&st)[2],
                                        rsrc_t rh, int off0) {
  f32x2 gg[8], uu[8], z[8];
#pragma unroll
  for (int i = 0; i < 8; ++i) {
    const int t = i >> 2, j = (i >> 1) & 1, hh = i & 1;
    gg[i] = (f32x2{g[j][t][2 * hh], g[j][t][2 * hh + 1]} - f32x2{cg[j][2 * hh], cg[j][2 * hh + 1]} * st[t].x) * st[t].y;
  }
#pragma unroll
  for (int i = 0; i < 8; ++i) z[i] = gg[i] * ((gg[i] * gg[i]) * -0.1029432397f + -2.302208198f);
#pragma unroll
  for (int i = 0; i < 8; ++i) { z[i].x = __builtin_amdgcn_exp2f(z[i].x); z[i].y = __builtin_amdgcn_exp2f(z[i].y); }
#pragma unroll
  for (int i = 0; i < 8; ++i) {
    const int t = i >> 2, j = (i >> 1) & 1, hh = i & 1;
    uu[i] = ((f32x2{u[j][t][2 * hh], u[j][t][2 * hh + 1]} - f32x2{cu[j][2 * hh], cu[j][2 * hh + 1]} * st[t].x) * st[t].y) * gg[i];
    z[i] = z[i] + 1.0f;
  }
#pragma unroll
  for (int i = 0; i < 8; ++i) { z[i].x = __builtin_amdgcn_rcpf(z[i].x); z[i].y = __builtin_amdgcn_rcpf(z[i].y); }
#pragma unroll
  for (int i = 0; i < 8; ++i) uu[i] = uu[i] * z[i];
#pragma unroll
  for (int tl = 0; tl < 2; ++tl) {
    const u32x4 o = {pk_bf16(uu[4 * tl].x, uu[4 * tl].y), pk_bf16(uu[4 * tl + 1].x, uu[4 * tl + 1].y),
                     pk_bf16(uu[4 * tl + 2].x, uu[4 * tl + 2].y), pk_bf16(uu[4 * tl + 3].x, uu[4 * tl + 3].y)};
    __builtin_amdgcn_raw_buffer_store_b128(o, rh, off0 + tl * 56 * 1024, 0, STAUX);
  }
}
constexpr int L_HD = 61440;

template <int MODE, int NG, int NA>
__device__ __forceinline__ void p1_consumer(lptr lds, Args a, int xcd, int idx, int mt, int grp, int f0, int a0, int wm, int wn, int lane) {
  StartJobs jobs = start_jobs<MODE>(lds, a, xcd, idx, mt, grp, 2 * wn + wm, lane);
  stash_jobs(lds, jobs, 2 * wn + wm, lane);
  launder(a, xcd, idx);
  asm volatile("" : "+s"(mt), "+s"(grp), "+s"(f0), "+s"(a0));
  char* ws = a.ws;
  const char* cp = (const char*)((xcd >> 2) ? a.Ci : a.Cr) + (size_t)(1024 * (xcd & 1) + 32 * idx + 8 * (2 * wn + wm) + (lane >> 5)) * 4096 + 2048 * ((xcd >> 1) & 1) + (lane & 31) * 64;
  unsigned nonce = 0;
  if (MODE == 0 && wm == 0 && wn == 0) asm volatile("global_load_dword %0, %1, off sc1" : "+v"(nonce) : "v"(ws + WS_NONCE) : "memory");
  constexpr int NBK = 2 * NG, NCQ = NBK / 4, WSZ = NBK * 8 * 256, KSO = 4 * NCQ * 1024;
  constexpr int GO = K1_WO, UO = K1_WO + WSZ, DO = K1_WO + 2 * WSZ;
  const int i16 = lane & 15, g = lane >> 4, q = i16 >> 2, p = i16 & 3;
  const int rowx = 64 * wm + 8 * (i16 >> 2) + (i16 & 3);
  const int vx0 = rowx * 128 + ((g) ^ (i16 & 7)) * 16;
  const int vx1 = rowx * 128 + ((g + 4) ^ (i16 & 7)) * 16;
  const int fw = ((q >> 1) & 1) | ((g & 1) << 1);
  int vw[NG];
#pragma unroll
  for (int j = 0; j < NG; ++j) { const int nb = NG * wn + j; vw[j] = g * NCQ * 1024 + (nb >> 2) * 1024 + q * 128 + (((nb & 3) ^ fw) * 32) + p * 8; }
  const int vd0 = DO + (16 * NA * wn + i16) * 128 + ((g) ^ (i16 & 7)) * 16;
  const int vd1 = DO + (16 * NA * wn + i16) * 128 + ((g + 4) ^ (i16 & 7)) * 16;

  f32x4 G[NG][4], U[NG][4], AB[NA][4];
#pragma unroll
  for (int j = 0; j < NG; ++j)
#pragma unroll
    for (int t = 0; t < 4; ++t) { G[j][t] = f32x4{0, 0, 0, 0}; U[j][t] = f32x4{0, 0, 0, 0}; }
#pragma unroll
  for (int j = 0; j < NA; ++j)
#pragma unroll
    for (int t = 0; t < 4; ++t) AB[j][t] = f32x4{0, 0, 0, 0};

  bf16x8 X0[4], X1[4], WG[NG], WU[NG], WD[NA];
  auto ldx = [&](bf16x8 (&X)[4], lptr st, int ks) {
#pragma unroll
    for (int t = 0; t < 4; ++t) X[t] = ldsr128(st + (ks ? vx1 : vx0) + (32 * (t >> 1) + 4 * (t & 1)) * 128);
  };
  auto ldg = [&](int j, lptr st, int ks) { lptr b = st + GO + ks * KSO + vw[j]; WG[j] = ldstr(b, b + 512); };
  auto ldu = [&](int j, lptr st, int ks) { lptr b = st + UO + ks * KSO + vw[j]; WU[j] = ldstr(b, b + 512); };
  auto ldd = [&](int j, lptr st, int ks) { WD[j] = ldsr128(st + (ks ? vd1 : vd0) + j * 16 * 128); };
  auto grp4 = [&](f32x4 (&C)[4], const bf16x8& W, bf16x8 (&X)[4], bool wfirst, auto mid) {
    if (wfirst) { mfma_ip(C[0], W, X[0]); mfma_ip(C[1], W, X[1]); } else { mfma_ip(C[0], X[0], W); mfma_ip(C[1], X[1], W); }
    SBAR(); mid(); SBAR();
    if (wfirst) { mfma_ip(C[2], W, X[2]); mfma_ip(C[3], W, X[3]); } else { mfma_ip(C[2], X[2], W); mfma_ip(C[3], X[3], W); }
    SBAR();
  };
  auto fill = [&]() { FILL(); };
  auto ldx1 = [&](bf16x8 (&X)[4], lptr st, int ks, int t) { X[t] = ldsr128(st + (ks ? vx1 : vx0) + (32 * (t >> 1) + 4 * (t & 1)) * 128); };
  auto blockA = [&](lptr st) {
#pragma unroll
    for (int j = 0; j < NG; ++j) {
      grp4(G[j], WG[j], X0, true, [&]() { if (2 * j < 4) ldx1(X1, st, 1, 2 * j); else FILL(); }); ldg(j, st, 1); SBAR();
      grp4(U[j], WU[j], X0, true, [&]() { if (2 * j + 1 < 4) ldx1(X1, st, 1, 2 * j + 1); else FILL(); }); ldu(j, st, 1); SBAR();
    }
#pragma unroll
    for (int j = 0; j < NA; ++j) { grp4(AB[j], WD[j], X0, false, fill); ldd(j, st, 1); SBAR(); }
  };
  auto blockB_head = [&]() { grp4(G[0], WG[0], X1, true, fill); FILL(); grp4(U[0], WU[0], X1, true, fill); };
  auto blockB_rest = [&](lptr sn, bool reload) {
    int k = 0;
    auto mid = [&]() {
      if (!reload) FILL();
      else if (k == 0) ldg(0, sn, 0);
      else if (k == 1) ldu(0, sn, 0);
      else if (k < 6) ldx1(X0, sn, 0, k - 2);
      else FILL();
      ++k;
    };
#pragma unroll
    for (int j = 1; j < NG; ++j) {
      grp4(G[j], WG[j], X1, true, mid); if (reload) { ldg(j, sn, 0); SBAR(); } else FILL();
      grp4(U[j], WU[j], X1, true, mid); if (reload) { ldu(j, sn, 0); SBAR(); } else FILL();
    }
#pragma unroll
    for (int j = 0; j < NA; ++j) { grp4(AB[j], WD[j], X1, false, mid); if (reload) { ldd(j, sn, 0); SBAR(); } else FILL(); }
  };

#pragma unroll
  for (int j = 0; j < NG; ++j)
#pragma unroll
    for (int t = 0; t < 4; ++t) { asm volatile("" : "+v"(G[j][t])); asm volatile("" : "+v"(U[j][t])); }
#pragma unroll
  for (int j = 0; j < NA; ++j)
#pragma unroll
    for (int t = 0; t < 4; ++t) asm volatile("" : "+v"(AB[j][t]));
  PAD_MFMA_IN();
  STAMP(1);
  WAITL0(); BAR();
  STAMP(2);
  ldx(X0, lds, 0);
#pragma unroll
  for (int j = 0; j < NG; ++j) { ldg(j, lds, 0); ldu(j, lds, 0); }
#pragma unroll
  for (int j = 0; j < NA; ++j) ldd(j, lds, 0);
  SBAR();
  blockA(lds);
  blockB_head();
  if (wm == 0 && wn == 0) {
    WAITV(0);
    asm volatile("" : "+v"(nonce));
    if (lane == 0) *(volatile LAS unsigned*)(lds + L_WORDS + 16) = (MODE == 0) ? nonce * 0x9E3779B1u + 0x7F4A7C15u : 0x51u;
  }
  WAITL0(); STAMP(6); BAR();
  STAMP(7);
  int slot = 1;
#pragma unroll 1
  for (int s = 0; s < K1_NST - 1; ++s) {
    lptr sn = lds + slot * K1_STAGE;
    slot = (slot == 2) ? 0 : slot + 1;
    blockB_rest(sn, true);
    blockA(sn);
    if (s >= PF0 && s < PF0 + 4) { pf4(cp, lds_addr(lds) + L_DUMMY); cp += 2 * 4096; }
    blockB_head();
    WAITL0();
    STAMP(20 + s);
    BAR();
    STAMP(8 + s);
  }
#pragma unroll
  for (int j = 0; j < NA; ++j) { grp4(AB[j], WD[j], X1, false, fill); FILL(); }
  PAD_MFMA_OUT();
  STAMP(3);
  const lptr cs = lds + L_CS;
  const lptr stats = lds + L_STATS;
  {
    const rsrc_t rab = mk_rsrc(ws + WS_AB, 2 * 28 * 64 * 1024);
#pragma unroll
    for (int pi = 0; pi < 2; ++pi) {
      const lptr sp = stats + (64 * wm + 32 * pi + 8 * g) * 8;
      const f32x4 s0 = *(LAS f32x4*)(sp), s1 = *(LAS f32x4*)(sp + 16), s2 = *(LAS f32x4*)(sp + 32), s3 = *(LAS f32x4*)(sp + 48);
#pragma unroll
      for (int j = 0; j < NA; ++j) {
        const float c = *(LAS float*)(cs + (2 * NBK + NA * wn + j) * 64 + i16 * 4);
        const f32x4 a_ = AB[j][2 * pi], b = AB[j][2 * pi + 1];
        const int ks = mt * 4 + wm * 2 + pi;
        u32x4 o;
        o[0] = pk_bf16((a_[0] - s0[0] * c) * s0[1], (a_[1] - s0[2] * c) * s0[3]);
        o[1] = pk_bf16((a_[2] - s1[0] * c) * s1[1], (a_[3] - s1[2] * c) * s1[3]);
        o[2] = pk_bf16((b[0] - s2[0] * c) * s2[1], (b[1] - s2[2] * c) * s2[3]);
        o[3] = pk_bf16((b[2] - s3[0] * c) * s3[1], (b[3] - s3[2] * c) * s3[3]);
        __builtin_amdgcn_raw_buffer_store_b128(o, rab, lane * 16 + ((a0 + NA * wn + j) * 64 + ks) * 1024, 0, STAUX);
      }
    }
  }
  SBAR();
#pragma unroll
  for (int j = 1; j < NG; ++j) { grp4(G[j], WG[j], X1, true, fill); FILL(); grp4(U[j], WU[j], X1, true, fill); FILL(); }
  PAD_MFMA_OUT();
  {
    lptr hd = lds + L_HD + (2 * wn + wm) * (NG * 4096) + lane * 16;
#pragma unroll
    for (int j = 0; j < NG; ++j)
#pragma unroll
      for (int tl = 0; tl < 2; ++tl) {
        *(LAS f32x4*)(hd + ((j * 2 + 0) * 2 + tl) * 1024) = G[j][2 + tl];
        *(LAS f32x4*)(hd + ((j * 2 + 1) * 2 + tl) * 1024) = U[j][2 + tl];
      }
  }
  WAITV(0);
  if (lane == 0) {
    const unsigned n = __hip_atomic_fetch_add((LAS unsigned*)(lds + L_WORDS), 1u, __ATOMIC_RELAXED, __HIP_MEMORY_SCOPE_WORKGROUP);
    if (n == 3u) st_flag(ws + WS_FAB + (mt * 16 + grp) * 64, get_tag(lds));
  }
  STAMP(5);
  WAITL0(); BAR();
  STAMP(14);
  const rsrc_t rh = mk_rsrc(ws + WS_H, 128 * 56 * 1024);
  const int tt0 = mt * 8 + wm * 4;
  f32x2 st[2];
#pragma unroll
  for (int t = 0; t < 2; ++t) st[t] = *(LAS f32x2*)(stats + (64 * wm + 8 * (i16 >> 2) + 4 * t + (i16 & 3)) * 8);
#pragma unroll
  for (int pp = 0; pp < NG / 2; ++pp) {
    const int ks = ((f0 >> 4) + NG * wn + 2 * pp) >> 1;
    f32x4 cg[2], cu[2], gq[2][2], uq[2][2];
#pragma unroll
    for (int j = 0; j < 2; ++j) {
      cg[j] = *(LAS f32x4*)(cs + (NG * wn + 2 * pp + j) * 64 + g * 16);
      cu[j] = *(LAS f32x4*)(cs + (NBK + NG * wn + 2 * pp + j) * 64 + g * 16);
      gq[j][0] = G[2 * pp + j][0]; gq[j][1] = G[2 * pp + j][1]; uq[j][0] = U[2 * pp + j][0]; uq[j][1] = U[2 * pp + j][1];
    }
    h_batch(gq, uq, cg, cu, st, rh, lane * 16 + (tt0 * 56 + ks) * 1024);
  }
  STAMP(4);
}

template <int NG>
__device__ __forceinline__ void p1_helper_epilogue(lptr lds, char* ws, int mt, int f0, int h, int lane) {
  constexpr int NBK = 2 * NG;
  const int wm = h & 1, wn = h >> 1, i16 = lane & 15, g = lane >> 4;
  const lptr cs = lds + L_CS, stats = lds + L_STATS;
  const rsrc_t rh = mk_rsrc(ws + WS_H, 128 * 56 * 1024);
  const int tt0 = mt * 8 + wm * 4 + 2;
  lptr hd = lds + L_HD + h * (NG * 4096) + lane * 16;
  f32x2 st[2];
#pragma unroll
  for (int t = 0; t < 2; ++t) st[t] = *(LAS f32x2*)(stats + (64 * wm + 32 + 8 * (i16 >> 2) + 4 * t + (i16 & 3)) * 8);
#pragma unroll
  for (int pp = 0; pp < NG / 2; ++pp) {
    const int ks = ((f0 >> 4) + NG * wn + 2 * pp) >> 1;
    f32x4 cg[2], cu[2], gq[2][2], uq[2][2];
#pragma unroll
    for (int j = 0; j < 2; ++j) {
      cg[j] = *(LAS f32x4*)(cs + (NG * wn + 2 * pp + j) * 64 + g * 16);
      cu[j] = *(LAS f32x4*)(cs + (NBK + NG * wn + 2 * pp + j) * 64 + g * 16);
#pragma unroll
      for (int tl = 0; tl < 2; ++tl) {
        gq[j][tl] = *(LAS f32x4*)(hd + (((2 * pp + j) * 2 + 0) * 2 + tl) * 1024);
        uq[j][tl] = *(LAS f32x4*)(hd + (((2 * pp + j) * 2 + 1) * 2 + tl) * 1024);
      }
    }
    h_batch(gq, uq, cg, cu, st, rh, lane * 16 + (tt0 * 56 + ks) * 1024);
  }
}

template <int MODE, bool TB>
__device__ __forceinline__ void p1_helper(lptr lds, Args a, int xcd, int idx, int mt, int grp, int f0, int a0, int h, int lane) {
  constexpr int NBK = TB ? 4 : 8, WSZ = NBK * 8 * 256;
  constexpr int NWP = TB ? 4 : 8, NDP = TB ? 4 : 1, PER = 4 + NWP + NDP;
  const int mh = xcd & 1, nq = xcd >> 1;
  const int up = h >> 1;
  const u32x4 rx = mk_srd(a.x, S * D * 2);
  const u32x4 rw = mk_srd(up ? a.Wu : a.Wg, D * F * 2);
  const u32x4 rd = mk_srd(a0 >= 28 ? a.Di : a.Dr, D * D * 2);
  const int e0 = (a0 >= 28 ? a0 - 28 : a0) * 16;
  const int r8 = lane >> 3, c8 = lane & 7;
  int vx[2];
  vx[0] = r8 * 896 + (c8 ^ (r8 & 3)) * 16;
  vx[1] = r8 * 896 + (c8 ^ ((r8 & 3) | 4)) * 16;
  const int vd = r8 * 896 + (c8 ^ r8) * 16;
  int vw[2];
  {
    const int f0_ = (r8 >> 1) & 1;
    vw[0] = r8 * (F * 2) + f0 * 2 + ((((c8 >> 1) ^ f0_) * 2) + (c8 & 1)) * 16;
    vw[1] = r8 * (F * 2) + f0 * 2 + ((((c8 >> 1) ^ (f0_ | 2)) * 2) + (c8 & 1)) * 16;
  }
  const unsigned l0 = lds_addr(lds);
  const unsigned dummy = l0 + L_DUMMY + (h & 1) * 256;
  const int sxb = (mt * 128 + 32 * h) * 896, swb = 32 * (h & 1) * (F * 2), sdb = (e0 + 8 * NDP * h) * 896;
  const unsigned lxb = l0 + 4 * h * 1024, lwb = l0 + K1_WO + up * WSZ + NWP * (h & 1) * 1024, ldb = l0 + K1_WO + 2 * WSZ + NDP * h * 1024;
  auto piece = [&](int s, int slot, int i) {
    const unsigned so = slot * K1_STAGE;
    if (i < 4) dma16(rx, lxb + so + i * 1024, vx[i & 1], sxb + s * 128 + i * 8 * 896);
    else if (i < 4 + NWP) {
      const int j = i - 4, kb = TB ? j : j >> 1, cq = TB ? 0 : j & 1;
      dma16(rw, lwb + so + j * 1024, vw[kb & 1], swb + s * 64 * (F * 2) + kb * 8 * (F * 2) + cq * 128);
    } else dma16(rd, ldb + so + (i - 4 - NWP) * 1024, vd, sdb + s * 128 + (i - 4 - NWP) * 8 * 896);
  };
  auto issue_a = [&](int s, int slot) {
#pragma unroll
    for (int i = 0; i < 8; ++i) piece(s, slot, i);
  };
  auto issue_b = [&](int s, int slot) {
#pragma unroll
    for (int i = 8; i < PER; ++i) piece(s, slot, i);
  };
  const int ln2 = lane >> 1, hb = (lane & 1) * 64;
  const char* pfx = (const char*)a.x + (size_t)(1024 * mh + 32 * idx + ln2) * 896 + hb;
  STAMP(1);
  issue_a(0, 0); issue_b(0, 0);
  STAMP(5);
  launder(a, xcd, idx);
  char* ws = a.ws;
  const bool wjob = idx < 28;
  const int wj_mat = idx >= 14, wj_cb = (idx - 14 * wj_mat) >> 1, wj_kh = idx & 1;
  u32x4 wx[7];
  {
    const u32x4 rj = mk_srd(wj_mat ? a.Wu : a.Wg, D * F * 2);
    const int vj = r8 * (F * 2) + c8 * 16 + (448 * nq + 64 * (wjob ? wj_cb : 0)) * 2;
    const int sj = (224 * (wjob ? wj_kh : 0) + 56 * h) * (F * 2);
    asm volatile("buffer_load_dwordx4 %0, %7, %8, %9 offen\n\tbuffer_load_dwordx4 %1, %7, %8, %10 offen\n\tbuffer_load_dwordx4 %2, %7, %8, %11 offen\n\t"
                 "buffer_load_dwordx4 %3, %7, %8, %12 offen\n\tbuffer_load_dwordx4 %4, %7, %8, %13 offen\n\tbuffer_load_dwordx4 %5, %7, %8, %14 offen\n\t"
                 "buffer_load_dwordx4 %6, %7, %8, %15 offen"
                 : "=&v"(wx[0]), "=&v"(wx[1]), "=&v"(wx[2]), "=&v"(wx[3]), "=&v"(wx[4]), "=&v"(wx[5]), "=&v"(wx[6])
                 : "v"(vj), "s"(rj), "s"(sj), "s"(sj + 8 * F * 2), "s"(sj + 16 * F * 2), "s"(sj + 24 * F * 2), "s"(sj + 32 * F * 2), "s"(sj + 40 * F * 2), "s"(sj + 48 * F * 2)
                 : "memory");
  }
  pf4(pfx + (1 + h) * 128, dummy);
  if (h == 3 && lane < 4) ((LAS unsigned*)(lds + L_WORDS))[lane] = 0u;
  WAITL0(); WAITV(8);
  STAMP(7); BAR();
  STAMP(2);
#pragma unroll
  for (int i = 0; i < PER; ++i) piece(1, 1, i);
  issue_a(2, 2);
  if (h < 2) pf4(pfx + (5 + h) * 128, dummy);
  WAITL0();
  if (h < 2) { WAITV(9); } else { WAITV(8); }
  asm volatile("" : "+v"(wx[0]), "+v"(wx[1]), "+v"(wx[2]), "+v"(wx[3]), "+v"(wx[4]), "+v"(wx[5]), "+v"(wx[6]));
  BAR();
  const unsigned tag1 = get_tag(lds);
  publish_jobs(lds, ws, tag1, xcd, idx, mt, grp, h, lane);
  float cw[8];
#pragma unroll
  for (int k = 0; k < 8; ++k) cw[k] = 0.f;
  constexpr bool TBc = TB;
  unsigned long long g0 = 0, g1 = 0, g2 = 0, g3 = 0;
  const bool wdjob = blockIdx.x < 196;
  const int wd_kp = blockIdx.x % 28, wd_cg = blockIdx.x / 28;
#pragma unroll
  for (int s = 1; s < K1_NST; ++s) {
    const int s1_ = (s + 1) % 3, s2_ = (s + 2) % 3;
    if (s == 1) {
#pragma unroll
      for (int i = 0; i < PER; ++i) {
        if (i < PER - 8) piece(2, s1_, 8 + i); else piece(3, s2_, i - (PER - 8));
        SBAR();
        if (i < 7) {
#pragma unroll
          for (int k = 0; k < 4; ++k) { cw[2 * k] += bf_lo(wx[i][k]); cw[2 * k + 1] += bf_hi(wx[i][k]); }
        }
        SBAR();
      }
    } else {
      if (s + 1 < K1_NST) issue_b(s + 1, s1_);

      if (s + 2 < K1_NST) issue_a(s + 2, s2_);
    }
    if (s == 1) {
      pf4((const char*)a.Wd + (size_t)(64 * wd_kp + 16 * h + ((lane >> 1) & 15)) * 896 + (wdjob ? wd_cg : 0) * 128 + (lane & 1) * 64, dummy);
#pragma unroll
      for (int k = 0; k < 8; ++k) {
        cw[k] += __builtin_bit_cast(float, __builtin_amdgcn_update_dpp(0, __builtin_bit_cast(int, cw[k]), 0x128, 0xf, 0xf, true));
        const auto p16 = __builtin_amdgcn_permlane16_swap(__float_as_uint(cw[k]), __float_as_uint(cw[k]), false, false);
        cw[k] = __uint_as_float(p16[0]) + __uint_as_float(p16[1]);
        const auto p32 = __builtin_amdgcn_permlane32_swap(__float_as_uint(cw[k]), __float_as_uint(cw[k]), false, false);
        cw[k] = __uint_as_float(p32[0]) + __uint_as_float(p32[1]);
      }
      if (lane < 8) {
        *(LAS f32x4*)(lds + L_CS + h * 256 + lane * 32) = f32x4{cw[0], cw[1], cw[2], cw[3]};
        *(LAS f32x4*)(lds + L_CS + h * 256 + lane * 32 + 16) = f32x4{cw[4], cw[5], cw[6], cw[7]};
      }
    }
    if (s == 2 && h == 0 && wjob) {
      const float t = (*(LAS float*)(lds + L_CS + lane * 4) + *(LAS float*)(lds + L_CS + 256 + lane * 4)) +
                      (*(LAS float*)(lds + L_CS + 512 + lane * 4) + *(LAS float*)(lds + L_CS + 768 + lane * 4));
      __hip_atomic_store((GAS unsigned long long*)(ws + WS_CW + (size_t)((wj_mat * 2 + wj_kh) * F + 448 * nq + 64 * wj_cb + lane) * 8),
                         ((unsigned long long)tag1 << 32) | __float_as_uint(t), __ATOMIC_RELAXED, __HIP_MEMORY_SCOPE_AGENT);
    }
    if (s == 4 && h == 0 && lane == 0) st_flag(ws + WS_ARRIVE + blockIdx.x * 64, tag1);
    if (s == 5) {
      if (wdjob) {
        const u32x4 rwd = mk_srd(a.Wd, F * D * 2);
        const int rs = (lane & 15) >> 1, half = lane & 1, b = lane >> 4;
        const int vq = rs * 896 + (64 * wd_cg + 16 * b + 8 * half) * 2;
#pragma unroll
        for (int i = 0; i < 2; ++i) dma16(rwd, l0 + s2_ * K1_STAGE + (2 * h + i) * 1024, vq, (64 * wd_kp + 8 * (2 * h + i)) * 896);
      }
    }
    const int gc = 64 * h + lane;
    constexpr int NWC = TBc ? 128 : 256, CPM = NWC / 2;
    const bool isw = gc < NWC;
    const char* p_st = ws + WS_ST + (size_t)(mt * 128 + 32 * h + (lane & 31)) * 8;
    const char* p_c0 = ws + (isw ? WS_CW + (size_t)((gc / CPM) * 2 * F + f0 + gc % CPM) * 8 : WS_CD + (size_t)(a0 * 16 + (gc - NWC)) * 8);
    const char* p_c1 = isw ? p_c0 + (size_t)F * 8 : p_c0;
    const char* p_d = ws + WS_CD + (size_t)(a0 * 16 + (lane & 31)) * 8;
    if (s == 5)
      asm volatile("global_load_dwordx2 %0, %4, off sc1\n\tglobal_load_dwordx2 %1, %5, off sc1\n\tglobal_load_dwordx2 %2, %6, off sc1\n\tglobal_load_dwordx2 %3, %7, off sc1"
                   : "=&v"(g0), "=&v"(g1), "=&v"(g2), "=&v"(g3) : "v"(p_st), "v"(p_c0), "v"(p_c1), "v"(p_d) : "memory");
    if (s == 6) {
      WAITV(0);
      asm volatile("" : "+v"(g0), "+v"(g1), "+v"(g2), "+v"(g3));
      auto ld_gran = [&](const char* p) { return __hip_atomic_load((const GAS unsigned long long*)p, __ATOMIC_RELAXED, __HIP_MEMORY_SCOPE_AGENT); };
      for (unsigned sp = 0; sp < SPIN_MAX; ++sp) {
        if (__all((unsigned)(g0 >> 32) == tag1 && (unsigned)(g1 >> 32) == tag1 && (unsigned)(g2 >> 32) == tag1 && (unsigned)(g3 >> 32) == tag1)) break;
        __builtin_amdgcn_s_sleep(2);
        g0 = ld_gran(p_st); g1 = ld_gran(p_c0); g2 = ld_gran(p_c1); g3 = ld_gran(p_d);
      }
      if (lane < 32) *(LAS f32x2*)(lds + L_STATS + (32 * h + lane) * 8) = f32x2{bf_lo((unsigned)g0), 1.0f / bf_hi((unsigned)g0)};
      *(LAS float*)(lds + L_CS + gc * 4) = isw ? __uint_as_float((unsigned)g1) + __uint_as_float((unsigned)g2) : __uint_as_float((unsigned)g1);
      if (!TBc && h == 0 && lane < 32) *(LAS float*)(lds + L_CS + (256 + lane) * 4) = __uint_as_float((unsigned)g3);
    }
    WAITL0();
    if (s == 1) { WAITV(9); } else if (s + 2 < K1_NST) { WAITV(8); } else if (s == 5) { if (wdjob) { WAITV(6); } else { WAITV(4); } }
    STAMP(7 + s);
    BAR();
  }
  __builtin_amdgcn_s_setprio(0);
  if (h == 3 && wdjob) {
    const int i16 = lane & 15, g = lane >> 4, q = i16 >> 2, p = i16 & 3;
    const rsrc_t rt = mk_rsrc(ws + WS_WDT, 28 * 56 * 1024);
    lptr b0 = lds + 1 * K1_STAGE + (g >> 1) * 1024 + ((g & 1) * 4 + q) * 32 + p * 8;
#pragma unroll
    for (int kk = 0; kk < 2; ++kk)
#pragma unroll
      for (int u = 0; u < 4; ++u) {
        const bf16x8 f = ldstr(b0 + kk * 4096 + u * 256, b0 + kk * 4096 + u * 256 + 2048);
        __builtin_amdgcn_raw_buffer_store_b128(__builtin_bit_cast(u32x4, f), rt, lane * 16 + ((4 * wd_cg + u) * 56 + 2 * wd_kp + kk) * 1024, 0, STAUX);
      }
    WAITL0();
  }
  BAR();
  p1_helper_epilogue<TB ? 2 : 4>(lds, ws, mt, f0, h, lane);
}

template <int MODE, int NB, int JO>
__device__ __forceinline__ void p2_loader(lptr lds, const Args& a, int xcd, int idx, int r, int m, int n, int lw, int lane) {
  char* ws = a.ws;
  constexpr int PER = 4 + NB;
  const u32x4 rc = mk_srd((r >> 1) ? a.Ci : a.Cr, S * S * 2);
  const u32x4 rb = mk_srd(ws + WS_AB, 2 * 28 * 64 * 1024);
  const u32x4 rh = mk_srd(ws + WS_H, 128 * 56 * 1024);
  const u32x4 rw = mk_srd(ws + WS_WDT, 28 * 56 * 1024);
  const int r8 = lane >> 3, c8 = lane & 7;
  int va[2];
  va[0] = r8 * 4096 + (c8 ^ (r8 & 3)) * 16;
  va[1] = r8 * 4096 + (c8 ^ ((r8 & 3) | 4)) * 16;
  const int vl = lane * 16;
  const int k0 = 1024 * (r & 1), p = r >> 1;
  const int j0 = (lw < 2) ? 4 * lw : 8 + 3 * (lw - 2);
  const unsigned l0 = lds_addr(lds);
  const unsigned dummy = l0 + L_DUMMY + (lw & 1) * 256;
  const int sab = (128 * m + 32 * lw) * 4096 + k0 * 2;
  const int shb = ((8 * m + 2 * lw) * 56 + 14 * r) * 1024;
  const unsigned lab = l0 + 4 * lw * 1024, lbb = l0 + 16384 + j0 * 1024;
  const int sbb = ((p * 28 + 7 * n + (j0 >> 1)) * 64 + (k0 >> 5)) * 1024, swb = ((7 * n + (j0 >> 1)) * 56 + 14 * r) * 1024;
  auto issueAy = [&](int s, int slot) {
    const unsigned so = slot * P_STAGE; const int b = sab + 128 * (s & 15);
#pragma unroll
    for (int i = 0; i < 4; ++i) dma16(rc, lab + so + i * 1024, va[i & 1], b + i * 8 * 4096);
  };
  auto issueAd = [&](int sd, int slot) {
    const unsigned so = slot * P_STAGE; const int b = shb + 2048 * sd;
#pragma unroll
    for (int i = 0; i < 4; ++i) dma16c(rh, lab + so + i * 1024, vl, b + (i >> 1) * 56 * 1024 + (i & 1) * 1024);
  };
  auto issueBy = [&](int s, int slot) {
    const unsigned so = slot * P_STAGE; const int b = sbb + 2048 * (s & 15);
#pragma unroll
    for (int j = 0; j < NB; ++j) dma16c(rb, lbb + so + j * 1024, vl, b + ((j + JO) >> 1) * 65536 + ((j + JO) & 1) * 1024);
  };
  auto issueBd = [&](int sd, int slot) {
    const unsigned so = slot * P_STAGE; const int b = swb + 2048 * sd;
#pragma unroll
    for (int j = 0; j < NB; ++j) dma16c(rw, lbb + so + j * 1024, vl, b + ((j + JO) >> 1) * 57344 + ((j + JO) & 1) * 1024);
  };
  __builtin_amdgcn_s_setprio(0);
  const unsigned tag = get_tag(lds);
  if (MODE != 2 && lw == 3 && blockIdx.x < 196) {
    WAITV(0);
    if (lane == 0) st_flag(ws + WS_FWD + blockIdx.x * 64, tag);
  }
  issueAy(0, 0); issueAy(1, 1);
  if (lw == 3) {
    const int mtp = 8 * (r & 1) + (lane & 7), j = (lane >> 3) & 3;
    const bool act = lane < 32 && ((n & 1) == 0 || j == 3);
    poll_all(ws + WS_FAB + (mtp * 16 + (2 * p + (n >> 1)) * 4 + j) * 64, act, tag);
    if (lane == 0) *(volatile LAS unsigned*)(lds + L_WORDS + 8) = 1u;
  } else lds_spin(lds + L_WORDS + 8);
  STAMP(16);
  issueBy(0, 0);
  {
    const int l = (lane >> 1) < 28 ? (lane >> 1) : 27;
    const int sl = l >= 14, li = 14 * idx + l - 14 * sl;
    const char* bp = ws + WS_AB + ((size_t)(p * 28 + (li >> 4)) * 64 + 32 * (r & 1) + 2 * (4 * lw + sl) + ((li >> 3) & 1)) * 1024 + (li & 7) * 128 + (lane & 1) * 64;
    pf4c(bp, dummy); pf4c(bp + 4 * 1024, dummy);
  }
  issueBy(1, 1);
  __builtin_amdgcn_s_setprio(3);
  WAITV(NB + 2); BAR();
  if (MODE == 0 && lane == 0) {
    const unsigned cnt = __hip_atomic_fetch_add((LAS unsigned*)(lds + L_WORDS + 4), 1u, __ATOMIC_RELAXED, __HIP_MEMORY_SCOPE_WORKGROUP);
    if (cnt == 7u) st_flag(ws + WS_FH + (((xcd & 1) * 8 + (idx & 7)) * 16 + (xcd >> 1) * 4 + (idx >> 3)) * 64, tag);
  }
  issueAy(2, 2); issueBy(2, 2);
  WAITV(PER); BAR();
  issueAy(3, 3); issueBy(3, 3); issueAy(4, 4); issueBy(4, 4);
  WAITV(2 * PER); BAR();
  int slot = 0;
  auto ystep = [&](int s) { issueAy(s + 3, slot); issueBy(s + 3, slot); slot = (slot == 4) ? 0 : slot + 1; WAITV(2 * PER); BAR(); };
  auto dstep = [&](int sd) { issueAd(sd, slot); issueBd(sd, slot); slot = (slot == 4) ? 0 : slot + 1; WAITV(2 * PER); BAR(); };
#pragma unroll 1
  for (int s = 2; s < P_NY - 9; ++s) ystep(s);
  const int cg0 = (112 * n) >> 6, cg1 = (112 * n + 111) >> 6, jj = lane - 4;
  const bool fact = lane < 4 || (jj < 21 && cg0 + jj / 7 <= cg1);
  const char* fp = lane < 4 ? ws + WS_FH + (m * 16 + 4 * r + lane) * 64 : ws + WS_FWD + ((fact ? cg0 + jj / 7 : cg0) * 28 + 7 * r + (fact ? jj % 7 : 0)) * 64;
  unsigned fval = 0;
  if (lw == 3) asm volatile("global_load_dword %0, %1, off sc1" : "+v"(fval) : "v"(fp) : "memory");
#pragma unroll 1
  for (int s = P_NY - 9; s < P_NY - 6; ++s) ystep(s);
  if (lw == 3) {
    asm volatile("" : "+v"(fval));
    if (!__all(!fact || fval == tag)) poll_all(fp, fact, tag);
    if (lane == 0) *(volatile LAS unsigned*)(lds + L_WORDS + 12) = 1u;
  }
#pragma unroll 1
  for (int s = P_NY - 6; s < P_NY - 3; ++s) ystep(s);
  if (lw != 3) lds_spin(lds + L_WORDS + 12);
#pragma unroll 1
  for (int sd = 0; sd < P_ND; ++sd) dstep(sd);
  WAITV(PER); BAR();
  WAITV(0); BAR();
}

template <int MODE>
__global__ __launch_bounds__(512, 2) void fused_kernel(Args a) {
  extern __shared__ __attribute__((aligned(1024))) char smem[];
  lptr lds = (lptr)smem;
  char* ws = a.ws;
  const int tid = threadIdx.x, lane = tid & 63;
  const int wid = __builtin_amdgcn_readfirstlane(tid >> 6);
  int xcd = blockIdx.x & 7, idx = blockIdx.x >> 3;
  STAMP(0); STAMPR(30);
  if (wid == 3) {
    unsigned long long pc;
    asm volatile("s_getpc_b64 %0" : "=s"(pc));
    const unsigned ln = 13 * idx + ((lane >> 1) < 13 ? (lane >> 1) : 12);
    pf4((const char*)(pc & ~127ull) + (ln * 128 < CODE_PF ? ln * 128 : 0) + (lane & 1) * 64, lds_addr(lds) + L_DUMMY);
  }
  if (wid >= 4) __builtin_amdgcn_s_setprio(3);

  if (MODE != 2) {
    const int nq = xcd >> 1;
    const int mt = (xcd & 1) * 8 + (idx & 7), j = idx >> 3;
    const bool tb = j == 3;
    const int grp = nq * 4 + j;
    const int f0 = 448 * nq + 128 * j, a0 = 14 * nq + 2 * j;
    if (wid < 4) {
      if (tb) p1_consumer<MODE, 2, 4>(lds, a, xcd, idx, mt, grp, f0, a0, wid & 1, wid >> 1, lane);
      else p1_consumer<MODE, 4, 1>(lds, a, xcd, idx, mt, grp, f0, a0, wid & 1, wid >> 1, lane);
    } else {
      const int h = wid - 4;
      if (tb) p1_helper<MODE, true>(lds, a, xcd, idx, mt, grp, f0, a0, h, lane);
      else p1_helper<MODE, false>(lds, a, xcd, idx, mt, grp, f0, a0, h, lane);
    }
  }
  if (MODE == 1) return;
  if (MODE == 2) { if (tid < 4) ((LAS unsigned*)(lds + L_WORDS))[tid] = 0u; if (tid == 4) ((LAS unsigned*)(lds + L_WORDS))[4] = 0x51u; __syncthreads(); }

  launder(a, xcd, idx);
  ws = a.ws;
  const int p1_mt = (xcd & 1) * 8 + (idx & 7), p1_grp = (xcd >> 1) * 4 + (idx >> 3);
  const int r = xcd >> 1, m = 8 * (xcd & 1) + (idx & 7), n = idx >> 3;
  const int tile = m * 4 + n;
  const int i16 = lane & 15, g = lane >> 4;
  f32x4 accY[7][2], acc[7][2];
  if (wid == 7) {
    p2_loader<MODE, 3, 1>(lds, a, xcd, idx, r, m, n, 3, lane);
  } else if (wid == 6) {
    p2_loader<MODE, 3, 0>(lds, a, xcd, idx, r, m, n, 2, lane);
  } else if (wid >= 4) {
    p2_loader<MODE, 4, 0>(lds, a, xcd, idx, r, m, n, wid - 4, lane);
  } else {
    const int w = wid;
    const int rowt = 32 * w + 8 * (i16 >> 2) + (i16 & 3);
    const int vt0 = rowt * 128 + ((g) ^ (i16 & 7)) * 16, vt1 = rowt * 128 + ((g + 4) ^ (i16 & 7)) * 16;
    const int ve = 16384 + lane * 16;
    const int vh = (4 * w) * 1024 + lane * 16;
    bf16x8 T0[4], T1[4], E[14];
    auto ldTy = [&](bf16x8 (&T)[4], lptr st) {
      T[0] = ldsr128(st + vt0); T[1] = ldsr128(st + vt0 + 512); T[2] = ldsr128(st + vt1); T[3] = ldsr128(st + vt1 + 512);
    };
    auto ldTd = [&](bf16x8 (&T)[4], lptr st) {
      T[0] = ldsr128(st + vh); T[1] = ldsr128(st + vh + 2048); T[2] = ldsr128(st + vh + 1024); T[3] = ldsr128(st + vh + 3072);
    };
    auto ldE = [&](int j, lptr st) { E[j] = ldsr128(st + ve + ((j % 7) * 2 + j / 7) * 1024); };
    auto mm = [&](int j, bf16x8 (&T)[4]) {
      mfma_ip(acc[j % 7][0], E[j], T[(j / 7) * 2]);
      mfma_ip(acc[j % 7][1], E[j], T[(j / 7) * 2 + 1]);
    };
#define GHEAD 3
    constexpr int GH = GHEAD;
    auto ldT1 = [&](bf16x8 (&T)[4], lptr st, int i, int NT) {
      if (NT == 0) T[i] = ldsr128(st + (i >= 2 ? vt1 : vt0) + (i & 1) * 512);
      else T[i] = ldsr128(st + vh + (i & 1) * 2048 + (i >> 1) * 1024);
    };
    auto rest = [&](bf16x8 (&Tc)[4], bf16x8 (&Tn)[4], lptr sn, int NT) {
#pragma unroll
      for (int j = GH; j < 14; ++j) {
        const int k = j - GH;
        mfma_ip(acc[j % 7][0], E[j], Tc[(j / 7) * 2]); SBAR();
        if (NT != 2 && k < 4 + GH) { if (k < 4) ldT1(Tn, sn, k, NT); else ldE(k - 4, sn); SBAR(); }
        mfma_ip(acc[j % 7][1], E[j], Tc[(j / 7) * 2 + 1]); SBAR();
        if (NT != 2) ldE(j, sn); else FILL();
        SBAR();
      }
    };
    auto head = [&](bf16x8 (&Tc)[4]) {
#pragma unroll
      for (int j = 0; j < GH; ++j) { mm(j, Tc); FILL(); }
      SBAR();
    };
#pragma unroll
    for (int u = 0; u < 7; ++u) { acc[u][0] = f32x4{0, 0, 0, 0}; acc[u][1] = f32x4{0, 0, 0, 0}; }
#pragma unroll
    for (int u = 0; u < 7; ++u) { asm volatile("" : "+v"(acc[u][0])); asm volatile("" : "+v"(acc[u][1])); }
    PAD_MFMA_IN();
    if (w != r) {
      const rsrc_t rp = mk_rsrc(ws + WS_PD + (size_t)((tile * 4 + r) * 4 + w) * 7168, 7168);
      const u32x4 sent = {0xffffffffu, 0xffffffffu, 0xffffffffu, 0xffffffffu};
#pragma unroll
      for (int u = 0; u < 7; ++u) {
        __builtin_amdgcn_raw_buffer_store_b128(sent, rp, u * 1024 + lane * 16, 0, 16);
      }
    }
    STAMP(16); STAMPR(28);
    BAR();
    STAMP(17); STAMPR(27);
    ldTy(T0, lds);
#pragma unroll
    for (int j = 0; j < 14; ++j) ldE(j, lds);
    SBAR();
    head(T0);
    WAITL0(); BAR();
    int slot = 1;
#pragma unroll 1
    for (int s = 0; s < P_NY - 2; s += 2) {
      lptr sn = lds + slot * P_STAGE; slot = (slot == 4) ? 0 : slot + 1;
      rest(T0, T1, sn, 0); head(T1);
      WAITL0(); BAR();
      sn = lds + slot * P_STAGE; slot = (slot == 4) ? 0 : slot + 1;
      rest(T1, T0, sn, 0); head(T0);
      WAITL0(); BAR();
      if (YREP > 1 && s == 14) { STAMP(23); }
      if (MODE == 0 && s == 0) {
        WAITV(0);
        if (lane == 0) {
          const unsigned cnt = __hip_atomic_fetch_add((LAS unsigned*)(lds + L_WORDS + 4), 1u, __ATOMIC_RELAXED, __HIP_MEMORY_SCOPE_WORKGROUP);
          if (cnt == 7u) st_flag(ws + WS_FH + (p1_mt * 16 + p1_grp) * 64, get_tag(lds));
        }
      }
    }
    {
      lptr sn = lds + slot * P_STAGE; slot = (slot == 4) ? 0 : slot + 1;
      rest(T0, T1, sn, 0); head(T1);
      WAITL0(); BAR();
      sn = lds + slot * P_STAGE; slot = (slot == 4) ? 0 : slot + 1;
      rest(T1, T0, sn, 1);
    }
    PAD_MFMA_OUT();
    STAMP(18);
    {
      const float sg = (r >> 1) ? -1.0f : 1.0f;
#pragma unroll
      for (int u = 0; u < 7; ++u) { accY[u][0] = acc[u][0] * sg; accY[u][1] = acc[u][1] * sg; acc[u][0] = f32x4{0, 0, 0, 0}; acc[u][1] = f32x4{0, 0, 0, 0}; }
      if (w != r) {
        const rsrc_t rp = mk_rsrc(ws + WS_PY + (size_t)((tile * 4 + r) * 4 + w) * 7168, 7168);
#pragma unroll
        for (int u = 0; u < 7; ++u) {
          const u32x4 o = {pk_bf16(accY[u][0][0], accY[u][0][1]), pk_bf16(accY[u][0][2], accY[u][0][3]),
                           pk_bf16(accY[u][1][0], accY[u][1][1]), pk_bf16(accY[u][1][2], accY[u][1][3])};
          __builtin_amdgcn_raw_buffer_store_b128(o, rp, u * 1024 + lane * 16, 0, 16);
        }
      }
    }
#pragma unroll
    for (int u = 0; u < 7; ++u) { asm volatile("" : "+v"(acc[u][0])); asm volatile("" : "+v"(acc[u][1])); }
    PAD_MFMA_IN();
    head(T0);
    WAITL0(); BAR();
#pragma unroll 1
    for (int s = 0; s < P_ND - 1; s += 2) {
      lptr sn = lds + slot * P_STAGE; slot = (slot == 4) ? 0 : slot + 1;
      rest(T0, T1, sn, 1); head(T1);
      WAITL0(); BAR();
      if (s == 0 && w != r) {
        WAITV(0);
        const unsigned tag = get_tag(lds);
        if (lane == 0) st_flag(ws + WS_FY + ((tile * 4 + r) * 4 + w) * 64, tag);
      }
      sn = lds + slot * P_STAGE; slot = (slot == 4) ? 0 : slot + 1;
      rest(T1, T0, sn, 1); head(T0);
      WAITL0(); if (s + 2 < P_ND - 1) BAR();
    }
    rest(T0, T1, lds, 2);
    PAD_MFMA_OUT();
    STAMP(19); STAMPR(29);
    if (w != r) {
      const rsrc_t rp = mk_rsrc(ws + WS_PD + (size_t)((tile * 4 + r) * 4 + w) * 7168, 7168);
#pragma unroll
      for (int u = 0; u < 7; ++u) {
        u32x4 o = {pk_bf16(acc[u][0][0], acc[u][0][1]), pk_bf16(acc[u][0][2], acc[u][0][3]),
                   pk_bf16(acc[u][1][0], acc[u][1][1]), pk_bf16(acc[u][1][2], acc[u][1][3])};
#pragma unroll
        for (int k = 0; k < 4; ++k) o[k] = o[k] < 0xfffffffeu ? o[k] : 0xfffffffeu;
        __builtin_amdgcn_raw_buffer_store_b128(o, rp, u * 1024 + lane * 16, 0, 16);
      }
    } else {
#pragma unroll
      for (int uu = 0; uu < 7; ++uu) {
        *(LAS f32x4*)(lds + (uu * 4 + 0) * 1024 + lane * 16) = accY[uu][0];
        *(LAS f32x4*)(lds + (uu * 4 + 1) * 1024 + lane * 16) = accY[uu][1];
        *(LAS f32x4*)(lds + (uu * 4 + 2) * 1024 + lane * 16) = acc[uu][0];
        *(LAS f32x4*)(lds + (uu * 4 + 3) * 1024 + lane * 16) = acc[uu][1];
      }
      WAITL0();
    }
    BAR();
  }
  if (wid >= 4) {
    __builtin_amdgcn_s_setprio(0);
    const int hw = wid - 4;
    const int nu = hw < 3 ? 2 : 1;
    const unsigned tag = get_tag(lds);
    const int tok0 = 128 * m + 32 * r + 8 * (i16 >> 2) + (i16 & 3);
    const rsrc_t ry = mk_rsrc(ws + WS_PY, 64 * 16 * 7168), rd = mk_rsrc(ws + WS_PD, 64 * 16 * 7168);
    u32x2 x0[2], x1[2];
    u32x4 fy[2][3], fd[2][3];
#pragma unroll
    for (int k = 0; k < 2; ++k) {
      const int u = 2 * hw + (k < nu ? k : 0);
      const int ecol = 112 * n + 16 * u + 4 * g;
      x0[k] = *(const u32x2*)(a.x + (size_t)tok0 * D + ecol); x1[k] = *(const u32x2*)(a.x + (size_t)(tok0 + 4) * D + ecol);
    }
    {
      const int rr = (r + 1 + (lane % 3)) & 3;
      poll_all(ws + WS_FY + ((tile * 4 + rr) * 4 + r) * 64, lane < 3, tag);
    }
#pragma unroll
    for (int k = 0; k < 2; ++k)
#pragma unroll
      for (int i = 0; i < 3; ++i)
        fy[k][i] = __builtin_amdgcn_raw_buffer_load_b128(ry, ((tile * 4 + ((r + 1 + i) & 3)) * 4 + r) * 7168 + (2 * hw + (k < nu ? k : 0)) * 1024 + lane * 16, 0, 16);
    STAMP(20);
    if (MODE == 0 && blockIdx.x == 0 && hw == 3) {
#pragma unroll 1
      for (int i = 0; i < 4; ++i) poll_all(ws + WS_ARRIVE + (64 * i + lane) * 64, true, tag);
      if (lane == 0) st_flag(ws + WS_NONCE, tag);
    }
    for (unsigned sp = 0; sp < (SPIN_MAX >> 3); ++sp) {
      const unsigned v = lane < 3 ? __builtin_amdgcn_raw_buffer_load_b32(rd, ((tile * 4 + ((r + 1 + lane) & 3)) * 4 + r) * 7168 + 6 * 1024 + 63 * 16 + 12, 0, 16) : 0u;
      if (__all(v != 0xffffffffu)) break;
      __builtin_amdgcn_s_sleep(1);
    }
    for (unsigned sp = 0; sp < (SPIN_MAX >> 3); ++sp) {
      bool ok = true;
#pragma unroll
      for (int k = 0; k < 2; ++k)
#pragma unroll
        for (int i = 0; i < 3; ++i) {
          fd[k][i] = __builtin_amdgcn_raw_buffer_load_b128(rd, ((tile * 4 + ((r + 1 + i) & 3)) * 4 + r) * 7168 + (2 * hw + (k < nu ? k : 0)) * 1024 + lane * 16, 0, 16);
        }
#pragma unroll
      for (int k = 0; k < 2; ++k)
#pragma unroll
        for (int i = 0; i < 3; ++i)
          ok = ok && fd[k][i][0] != 0xffffffffu && fd[k][i][1] != 0xffffffffu && fd[k][i][2] != 0xffffffffu && fd[k][i][3] != 0xffffffffu;
      if (__all(ok)) break;
      __builtin_amdgcn_s_sleep(2);
    }
    STAMP(21); STAMPR(29);
    BAR();
#pragma unroll
    for (int k = 0; k < 2; ++k) {
      if (k < nu) {
        const int u = 2 * hw + k;
        const int ecol = 112 * n + 16 * u + 4 * g;
        f32x4 y0 = *(LAS f32x4*)(lds + (u * 4 + 0) * 1024 + lane * 16), y1 = *(LAS f32x4*)(lds + (u * 4 + 1) * 1024 + lane * 16);
        f32x4 d0 = *(LAS f32x4*)(lds + (u * 4 + 2) * 1024 + lane * 16), d1 = *(LAS f32x4*)(lds + (u * 4 + 3) * 1024 + lane * 16);
#pragma unroll
        for (int i = 0; i < 3; ++i) {
          y0 += f32x4{bf_lo(fy[k][i][0]), bf_hi(fy[k][i][0]), bf_lo(fy[k][i][1]), bf_hi(fy[k][i][1])};
          y1 += f32x4{bf_lo(fy[k][i][2]), bf_hi(fy[k][i][2]), bf_lo(fy[k][i][3]), bf_hi(fy[k][i][3])};
          d0 += f32x4{bf_lo(fd[k][i][0]), bf_hi(fd[k][i][0]), bf_lo(fd[k][i][1]), bf_hi(fd[k][i][1])};
          d1 += f32x4{bf_lo(fd[k][i][2]), bf_hi(fd[k][i][2]), bf_lo(fd[k][i][3]), bf_hi(fd[k][i][3])};
        }
        const u32x2 o0 = {pk_bf16(bf_lo(x0[k][0]) + y0[0] * silu_f(d0[0]), bf_hi(x0[k][0]) + y0[1] * silu_f(d0[1])),
                          pk_bf16(bf_lo(x0[k][1]) + y0[2] * silu_f(d0[2]), bf_hi(x0[k][1]) + y0[3] * silu_f(d0[3]))};
        const u32x2 o1 = {pk_bf16(bf_lo(x1[k][0]) + y1[0] * silu_f(d1[0]), bf_hi(x1[k][0]) + y1[1] * silu_f(d1[1])),
                          pk_bf16(bf_lo(x1[k][1]) + y1[2] * silu_f(d1[2]), bf_hi(x1[k][1]) + y1[3] * silu_f(d1[3]))};
        *(u32x2*)(a.out + (size_t)tok0 * D + ecol) = o0;
        *(u32x2*)(a.out + (size_t)(tok0 + 4) * D + ecol) = o1;
      }
    }
  }
}

#define SPLIT 0
extern "C" void kernel_launch(void* const* d_in, const int* in_sizes, int n_in, void* d_out, int out_size, void* d_ws, size_t ws_size,
                              hipStream_t stream) {
  Args a;
  a.x = (const u16*)d_in[0]; a.Cr = (const u16*)d_in[1]; a.Ci = (const u16*)d_in[2]; a.Dr = (const u16*)d_in[3]; a.Di = (const u16*)d_in[4];
  a.Wg = (const u16*)d_in[6]; a.Wu = (const u16*)d_in[7]; a.Wd = (const u16*)d_in[8];
  a.ws = (char*)d_ws; a.out = (u16*)d_out;
  (void)hipFuncSetAttribute((const void*)fused_kernel<0>, hipFuncAttributeMaxDynamicSharedMemorySize, L_TOTAL);
  fused_kernel<0><<<256, 512, L_TOTAL, stream>>>(a);
}
```

```cpp
#include <hip/hip_runtime.h>
#include <hip/hip_bf16.h>
#include <stdint.h>

typedef unsigned short u16;
typedef __bf16 bf16x8 __attribute__((ext_vector_type(8)));
typedef short s16x8 __attribute__((ext_vector_type(8)));
typedef float f32x4 __attribute__((ext_vector_type(4)));
typedef float f32x2 __attribute__((ext_vector_type(2)));
typedef unsigned u32x4 __attribute__((ext_vector_type(4)));
typedef unsigned u32x2 __attribute__((ext_vector_type(2)));
#define LAS __attribute__((address_space(3)))
#define GAS __attribute__((address_space(1)))
typedef LAS char* lptr;
typedef __amdgpu_buffer_rsrc_t rsrc_t;

constexpr int S = 2048, D = 448, F = 1792;

constexpr size_t WS_NONCE  = 0;
constexpr size_t WS_ARRIVE = 4096;
constexpr size_t WS_FAB    = 32768;
constexpr size_t WS_FH     = 49152;
constexpr size_t WS_FWD    = 65536;
constexpr size_t WS_FY     = 81920;
constexpr size_t WS_FD     = 147456;
constexpr size_t WS_ST     = 212992;
constexpr size_t WS_CW     = 229376;
constexpr size_t WS_CD     = 286720;
constexpr size_t WS_AB     = 327680;
constexpr size_t WS_H      = WS_AB + (size_t)2 * 28 * 64 * 1024;
constexpr size_t WS_WDT    = WS_H + (size_t)128 * 56 * 1024;
constexpr size_t WS_PY     = WS_WDT + (size_t)28 * 56 * 1024;
constexpr size_t WS_PD     = WS_PY + (size_t)64 * 16 * 7 * 1024;

#define STAMP(i)
#define STAMPR(i)
#define SBAR() __builtin_amdgcn_sched_barrier(0)
#define BAR() __builtin_amdgcn_s_barrier()
#define WAITV(n) asm volatile("s_waitcnt vmcnt(%0)" ::"n"(n) : "memory")
#define WAITL0() __builtin_amdgcn_s_waitcnt(0xC07F)
#define CODE_PF 49152
#define DFTPF 0
#define PF0 1
#define STAUX 16
#define SPIN_MAX (1u << 18)

__device__ __forceinline__ rsrc_t mk_rsrc(const void* p, unsigned bytes) {
  return __builtin_amdgcn_make_buffer_rsrc((void*)p, 0, bytes, 0x00020000);
}
__device__ __forceinline__ u32x4 mk_srd(const void* p, unsigned bytes) {
  const unsigned long long a = (unsigned long long)p;
  return u32x4{(unsigned)a, (unsigned)(a >> 32) & 0xffffu, bytes, 0x00020000u};
}
__device__ __forceinline__ unsigned lds_addr(lptr p) { return (unsigned)(size_t)p; }
__device__ __forceinline__ void dma16(const u32x4& srd, unsigned dst, int voff, int soff) {
  asm volatile("s_mov_b32 m0, %0\n\ts_nop 0\n\tbuffer_load_dwordx4 %1, %2, %3 offen lds" ::"s"(dst), "v"(voff), "s"(srd), "s"(soff) : "memory");
}
__device__ __forceinline__ void dma16c(const u32x4& srd, unsigned dst, int voff, int soff) {
  asm volatile("s_mov_b32 m0, %0\n\ts_nop 0\n\tbuffer_load_dwordx4 %1, %2, %3 offen sc1 lds" ::"s"(dst), "v"(voff), "s"(srd), "s"(soff) : "memory");
}
__device__ __forceinline__ void pf4c(const char* p, unsigned dst) {
  asm volatile("s_mov_b32 m0, %0\n\ts_nop 0\n\tglobal_load_lds_dword %1, off sc1" ::"s"(dst), "v"(p) : "memory");
}
__device__ __forceinline__ void pf4(const char* p, unsigned dst) {
  asm volatile("s_mov_b32 m0, %0\n\ts_nop 0\n\tglobal_load_lds_dword %1, off" ::"s"(dst), "v"(p) : "memory");
}
__device__ __forceinline__ bf16x8 ldsr128(lptr p) { return *(const LAS bf16x8*)p; }
__device__ __forceinline__ bf16x8 ldstr(lptr p0, lptr p1) {
  typedef short v4i16_t __attribute__((ext_vector_type(4)));
  v4i16_t lo = __builtin_amdgcn_ds_read_tr16_b64_v4i16((LAS v4i16_t*)p0);
  v4i16_t hi = __builtin_amdgcn_ds_read_tr16_b64_v4i16((LAS v4i16_t*)p1);
  s16x8 v = __builtin_shufflevector(lo, hi, 0, 1, 2, 3, 4, 5, 6, 7);
  return __builtin_bit_cast(bf16x8, v);
}
__device__ __forceinline__ f32x4 mfma(bf16x8 a, bf16x8 b, f32x4 c) { return __builtin_amdgcn_mfma_f32_16x16x32_bf16(a, b, c, 0, 0, 0); }
__device__ __forceinline__ void mfma_ip(f32x4& c, const bf16x8& a, const bf16x8& b) {
  asm volatile("v_mfma_f32_16x16x32_bf16 %0, %1, %2, %0" : "+v"(c) : "v"(a), "v"(b));
}
#define FILL() asm volatile("s_nop 0")
#define PAD_MFMA_OUT() asm volatile("s_nop 7\n\ts_nop 3")
#define PAD_MFMA_IN() asm volatile("s_nop 3")
__device__ __forceinline__ unsigned pk_bf16(float lo, float hi) {
  typedef __bf16 bf16x2 __attribute__((ext_vector_type(2)));
  f32x2 v = {lo, hi};
  bf16x2 b = __builtin_convertvector(v, bf16x2);
  return __builtin_bit_cast(unsigned, b);
}
__device__ __forceinline__ float bf_lo(unsigned v) { return __uint_as_float(v << 16); }
__device__ __forceinline__ float bf_hi(unsigned v) { return __uint_as_float(v & 0xffff0000u); }
__device__ __forceinline__ float rbf(float f) { return bf_lo(pk_bf16(f, 0.f)); }
__device__ __forceinline__ float silu_f(float x) {
  float e = __builtin_amdgcn_exp2f(x * -1.4426950409f);
  return x * __builtin_amdgcn_rcpf(1.0f + e);
}
template <int N>
__device__ __forceinline__ void wave_sums(float (&v)[N]) {
#define DPP_STEP(ctrl, rmask) _Pragma("unroll") for (int i = 0; i < N; ++i) \
    v[i] += __builtin_bit_cast(float, __builtin_amdgcn_update_dpp(0, __builtin_bit_cast(int, v[i]), ctrl, rmask, 0xf, true));
  DPP_STEP(0x111, 0xf)
  DPP_STEP(0x112, 0xf)
  DPP_STEP(0x114, 0xf)
  DPP_STEP(0x118, 0xf)
  DPP_STEP(0x142, 0xa)
  DPP_STEP(0x143, 0xc)
#undef DPP_STEP
#pragma unroll
  for (int i = 0; i < N; ++i) v[i] = __builtin_bit_cast(float, __builtin_amdgcn_readlane(__builtin_bit_cast(int, v[i]), 63));
}
__device__ __forceinline__ unsigned ld_flag(const void* p) { return __hip_atomic_load((const GAS unsigned*)p, __ATOMIC_RELAXED, __HIP_MEMORY_SCOPE_AGENT); }
__device__ __forceinline__ void st_flag(void* p, unsigned v) { __hip_atomic_store((GAS unsigned*)p, v, __ATOMIC_RELAXED, __HIP_MEMORY_SCOPE_AGENT); }
__device__ __forceinline__ unsigned get_tag(lptr lds);
__device__ __forceinline__ void poll_all(const char* p, bool active, unsigned tag) {
  for (unsigned sp = 0; sp < SPIN_MAX; ++sp) {
    const unsigned v = active ? ld_flag(p) : tag;
    if (__all(v == tag)) break;
    __builtin_amdgcn_s_sleep(2);
  }
}
__device__ __forceinline__ void lds_spin(lptr p) {
  for (unsigned sp = 0; sp < SPIN_MAX * 4; ++sp) {
    if (*(volatile LAS unsigned*)p != 0u) break;
    __builtin_amdgcn_s_sleep(2);
  }
}

constexpr int K1_STAGE = 53248, K1_NST = 7, K1_WO = 16384;
constexpr int L_MISC = 3 * K1_STAGE;
constexpr int L_STATS = L_MISC;
constexpr int L_CS = L_MISC + 1024;
constexpr int L_WORDS = L_CS + 18 * 64;
constexpr int L_DUMMY = L_WORDS + 128;
constexpr int L_TOTAL = L_DUMMY + 512;
#define YREP 1
constexpr int P_STAGE = 30720, P_NY = 16 * YREP, P_ND = 7, P_NST = P_NY + P_ND;
static_assert(L_TOTAL <= 163840 && 5 * P_STAGE <= L_MISC, "LDS");

__device__ __forceinline__ unsigned get_tag(lptr lds) { return *(volatile LAS unsigned*)(lds + L_WORDS + 16); }

struct Args {
  const u16 *x, *Cr, *Ci, *Dr, *Di, *Wg, *Wu, *Wd;
  char* ws;
  u16* out;
};

__device__ __forceinline__ void launder(Args& a, int& xcd, int& idx) {
  asm volatile("" : "+s"(a.x), "+s"(a.Cr), "+s"(a.Ci), "+s"(a.Dr), "+s"(a.Di));
  asm volatile("" : "+s"(a.Wg), "+s"(a.Wu), "+s"(a.Wd), "+s"(a.ws), "+s"(a.out));
  asm volatile("" : "+s"(xcd), "+s"(idx));
}

struct RowJobs {
  u32x4 v[4];
  float f[2][8], sm[4];
  __device__ __forceinline__ void load(const Args& a, int nq, int idx, int mt, int grp, int h, int lane) {
    const int l56 = lane < 56 ? lane : 55;
    const u16* sp = a.x + (size_t)(mt * 128 + 8 * grp + 2 * h) * D + l56 * 8;
    const u16* dp = (nq >= 2 ? a.Di : a.Dr) + (size_t)(224 * (nq & 1) + 7 * idx + h) * D + l56 * 8;
    const u16* dq = dp + (h < 3 ? 4 * D : 0);
    asm volatile("global_load_dwordx4 %0, %4, off\n\tglobal_load_dwordx4 %1, %4, off offset:896\n\tglobal_load_dwordx4 %2, %5, off\n\tglobal_load_dwordx4 %3, %6, off"
                 : "=&v"(v[0]), "=&v"(v[1]), "=&v"(v[2]), "=&v"(v[3]) : "v"(sp), "v"(dp), "v"(dq) : "memory");
  }
  __device__ __forceinline__ void landed() { asm volatile("" : "+v"(v[0]), "+v"(v[1]), "+v"(v[2]), "+v"(v[3])); }
  __device__ __forceinline__ void sums(int lane) {
#pragma unroll
    for (int r = 0; r < 4; ++r) {
      float t[8];
#pragma unroll
      for (int i = 0; i < 4; ++i) { t[2 * i] = bf_lo(v[r][i]); t[2 * i + 1] = bf_hi(v[r][i]); }
      const float s = ((t[0] + t[1]) + (t[2] + t[3])) + ((t[4] + t[5]) + (t[6] + t[7]));
      sm[r] = lane < 56 ? s : 0.f;
      if (r < 2) {
#pragma unroll
        for (int i = 0; i < 8; ++i) f[r][i] = t[i];
      }
    }
    wave_sums<4>(sm);
  }
  __device__ __forceinline__ void finish(char* ws, unsigned tag, int nq, int idx, int mt, int grp, int h, int lane) {
    float q[2], mean[2];
#pragma unroll
    for (int r = 0; r < 2; ++r) {
      mean[r] = sm[r] * (1.0f / D);
      float t0 = 0.f, t1 = 0.f;
#pragma unroll
      for (int i = 0; i < 4; ++i) {
        const float d0 = f[r][2 * i] - mean[r], d1 = f[r][2 * i + 1] - mean[r];
        t0 = __builtin_fmaf(d0, d0, t0); t1 = __builtin_fmaf(d1, d1, t1);
      }
      q[r] = lane < 56 ? t0 + t1 : 0.f;
    }
    wave_sums<2>(q);
    const int r = lane & 1;
    const unsigned pay = pk_bf16(r ? mean[1] : mean[0], rbf(__builtin_amdgcn_sqrtf(rbf(rbf((r ? q[1] : q[0]) * (1.0f / D)) + 1e-5f))));
    if (lane < 2 || lane == 2 || (lane == 3 && h < 3)) {
      const unsigned v_ = lane < 2 ? pay : __float_as_uint(lane == 2 ? sm[2] : sm[3]);
      const size_t off = lane < 2 ? WS_ST + (size_t)(mt * 128 + 8 * grp + 2 * h + lane) * 8 : WS_CD + (size_t)(224 * nq + 7 * idx + h + 4 * (lane - 2)) * 8;
      __hip_atomic_store((GAS unsigned long long*)(ws + off), ((unsigned long long)tag << 32) | v_, __ATOMIC_RELAXED, __HIP_MEMORY_SCOPE_AGENT);
    }
  }
};

__device__ __forceinline__ void h_batch(const f32x4 (&g)[2][2], const f32x4 (&u)[2][2], const f32x4 (&cg)[2], const f32x4 (&cu)[2], const f32x2 (&st)[2],
                                        rsrc_t rh, int off0) {
  f32x2 gg[8], uu[8], z[8];
#pragma unroll
  for (int i = 0; i < 8; ++i) {
    const int t = i >> 2, j = (i >> 1) & 1, hh = i & 1;
    gg[i] = (f32x2{g[j][t][2 * hh], g[j][t][2 * hh + 1]} - f32x2{cg[j][2 * hh], cg[j][2 * hh + 1]} * st[t].x) * st[t].y;
  }
#pragma unroll
  for (int i = 0; i < 8; ++i) z[i] = gg[i] * ((gg[i] * gg[i]) * -0.1029432397f + -2.302208198f);
#pragma unroll
  for (int i = 0; i < 8; ++i) { z[i].x = __builtin_amdgcn_exp2f(z[i].x); z[i].y = __builtin_amdgcn_exp2f(z[i].y); }
#pragma unroll
  for (int i = 0; i < 8; ++i) {
    const int t = i >> 2, j = (i >> 1) & 1, hh = i & 1;
    uu[i] = ((f32x2{u[j][t][2 * hh], u[j][t][2 * hh + 1]} - f32x2{cu[j][2 * hh], cu[j][2 * hh + 1]} * st[t].x) * st[t].y) * gg[i];
    z[i] = z[i] + 1.0f;
  }
#pragma unroll
  for (int i = 0; i < 8; ++i) { z[i].x = __builtin_amdgcn_rcpf(z[i].x); z[i].y = __builtin_amdgcn_rcpf(z[i].y); }
#pragma unroll
  for (int i = 0; i < 8; ++i) uu[i] = uu[i] * z[i];
#pragma unroll
  for (int tl = 0; tl < 2; ++tl) {
    const u32x4 o = {pk_bf16(uu[4 * tl].x, uu[4 * tl].y), pk_bf16(uu[4 * tl + 1].x, uu[4 * tl + 1].y),
                     pk_bf16(uu[4 * tl + 2].x, uu[4 * tl + 2].y), pk_bf16(uu[4 * tl + 3].x, uu[4 * tl + 3].y)};
    __builtin_amdgcn_raw_buffer_store_b128(o, rh, off0 + tl * 56 * 1024, 0, STAUX);
  }
}
constexpr int L_HD = 61440;

template <int MODE, int NG, int NA>
__device__ __forceinline__ void p1_consumer(lptr lds, Args a, int xcd, int idx, int mt, int grp, int f0, int a0, int wm, int wn, int lane) {
  launder(a, xcd, idx);
  asm volatile("" : "+s"(mt), "+s"(grp), "+s"(f0), "+s"(a0));
  char* ws = a.ws;
  const char* cp = (const char*)((xcd >> 2) ? a.Ci : a.Cr) + (size_t)(1024 * (xcd & 1) + 32 * idx + 8 * (2 * wn + wm) + (lane >> 5)) * 4096 + 2048 * ((xcd >> 1) & 1) + (lane & 31) * 64;
  unsigned nonce = 0;
  if (MODE == 0 && wm == 0 && wn == 0) asm volatile("global_load_dword %0, %1, off sc1" : "+v"(nonce) : "v"(ws + WS_NONCE) : "memory");
  constexpr int NBK = 2 * NG, NCQ = NBK / 4, WSZ = NBK * 8 * 256, KSO = 4 * NCQ * 1024;
  constexpr int GO = K1_WO, UO = K1_WO + WSZ, DO = K1_WO + 2 * WSZ;
  const int i16 = lane & 15, g = lane >> 4, q = i16 >> 2, p = i16 & 3;
  const int rowx = 64 * wm + 8 * (i16 >> 2) + (i16 & 3);
  const int vx0 = rowx * 128 + ((g) ^ (i16 & 7)) * 16;
  const int vx1 = rowx * 128 + ((g + 4) ^ (i16 & 7)) * 16;
  const int fw = ((q >> 1) & 1) | ((g & 1) << 1);
  int vw[NG];
#pragma unroll
  for (int j = 0; j < NG; ++j) { const int nb = NG * wn + j; vw[j] = g * NCQ * 1024 + (nb >> 2) * 1024 + q * 128 + (((nb & 3) ^ fw) * 32) + p * 8; }
  const int vd0 = DO + (16 * NA * wn + i16) * 128 + ((g) ^ (i16 & 7)) * 16;
  const int vd1 = DO + (16 * NA * wn + i16) * 128 + ((g + 4) ^ (i16 & 7)) * 16;

  f32x4 G[NG][4], U[NG][4], AB[NA][4];
#pragma unroll
  for (int j = 0; j < NG; ++j)
#pragma unroll
    for (int t = 0; t < 4; ++t) { G[j][t] = f32x4{0, 0, 0, 0}; U[j][t] = f32x4{0, 0, 0, 0}; }
#pragma unroll
  for (int j = 0; j < NA; ++j)
#pragma unroll
    for (int t = 0; t < 4; ++t) AB[j][t] = f32x4{0, 0, 0, 0};

  bf16x8 X0[4], X1[4], WG[NG], WU[NG], WD[NA];
  auto ldx = [&](bf16x8 (&X)[4], lptr st, int ks) {
#pragma unroll
    for (int t = 0; t < 4; ++t) X[t] = ldsr128(st + (ks ? vx1 : vx0) + (32 * (t >> 1) + 4 * (t & 1)) * 128);
  };
  auto ldg = [&](int j, lptr st, int ks) { lptr b = st + GO + ks * KSO + vw[j]; WG[j] = ldstr(b, b + 512); };
  auto ldu = [&](int j, lptr st, int ks) { lptr b = st + UO + ks * KSO + vw[j]; WU[j] = ldstr(b, b + 512); };
  auto ldd = [&](int j, lptr st, int ks) { WD[j] = ldsr128(st + (ks ? vd1 : vd0) + j * 16 * 128); };
  auto grp4 = [&](f32x4 (&C)[4], const bf16x8& W, bf16x8 (&X)[4], bool wfirst, auto mid) {
    if (wfirst) { mfma_ip(C[0], W, X[0]); mfma_ip(C[1], W, X[1]); } else { mfma_ip(C[0], X[0], W); mfma_ip(C[1], X[1], W); }
    SBAR(); mid(); SBAR();
    if (wfirst) { mfma_ip(C[2], W, X[2]); mfma_ip(C[3], W, X[3]); } else { mfma_ip(C[2], X[2], W); mfma_ip(C[3], X[3], W); }
    SBAR();
  };
  auto fill = [&]() { FILL(); };
  auto ldx1 = [&](bf16x8 (&X)[4], lptr st, int ks, int t) { X[t] = ldsr128(st + (ks ? vx1 : vx0) + (32 * (t >> 1) + 4 * (t & 1)) * 128); };
  auto blockA = [&](lptr st) {
#pragma unroll
    for (int j = 0; j < NG; ++j) {
      grp4(G[j], WG[j], X0, true, [&]() { if (2 * j < 4) ldx1(X1, st, 1, 2 * j); else FILL(); }); ldg(j, st, 1); SBAR();
      grp4(U[j], WU[j], X0, true, [&]() { if (2 * j + 1 < 4) ldx1(X1, st, 1, 2 * j + 1); else FILL(); }); ldu(j, st, 1); SBAR();
    }
#pragma unroll
    for (int j = 0; j < NA; ++j) { grp4(AB[j], WD[j], X0, false, fill); ldd(j, st, 1); SBAR(); }
  };
  auto blockB_head = [&]() { grp4(G[0], WG[0], X1, true, fill); FILL(); grp4(U[0], WU[0], X1, true, fill); };
  auto blockB_rest = [&](lptr sn, bool reload) {
    int k = 0;
    auto mid = [&]() {
      if (!reload) FILL();
      else if (k == 0) ldg(0, sn, 0);
      else if (k == 1) ldu(0, sn, 0);
      else if (k < 6) ldx1(X0, sn, 0, k - 2);
      else FILL();
      ++k;
    };
#pragma unroll
    for (int j = 1; j < NG; ++j) {
      grp4(G[j], WG[j], X1, true, mid); if (reload) { ldg(j, sn, 0); SBAR(); } else FILL();
      grp4(U[j], WU[j], X1, true, mid); if (reload) { ldu(j, sn, 0); SBAR(); } else FILL();
    }
#pragma unroll
    for (int j = 0; j < NA; ++j) { grp4(AB[j], WD[j], X1, false, mid); if (reload) { ldd(j, sn, 0); SBAR(); } else FILL(); }
  };

#pragma unroll
  for (int j = 0; j < NG; ++j)
#pragma unroll
    for (int t = 0; t < 4; ++t) { asm volatile("" : "+v"(G[j][t])); asm volatile("" : "+v"(U[j][t])); }
#pragma unroll
  for (int j = 0; j < NA; ++j)
#pragma unroll
    for (int t = 0; t < 4; ++t) asm volatile("" : "+v"(AB[j][t]));
  PAD_MFMA_IN();
  STAMP(1);
  BAR();
  STAMP(2);
  ldg(0, lds, 0); ldu(0, lds, 0); ldx(X0, lds, 0);
#pragma unroll
  for (int j = 1; j < NG; ++j) { ldg(j, lds, 0); ldu(j, lds, 0); }
#pragma unroll
  for (int j = 0; j < NA; ++j) ldd(j, lds, 0);
  SBAR();
  int slot = 0;
#pragma unroll 1
  for (int s = 0; s < K1_NST; ++s) {
    blockA(lds + slot * K1_STAGE);
    if (s >= PF0 && s < PF0 + 4) { pf4(cp, lds_addr(lds) + L_DUMMY); cp += 2 * 4096; }
    blockB_head();
    if (s == 0 && wm == 0 && wn == 0) {
      if (PF0 == 0) { WAITV(1); } else { WAITV(0); }
      asm volatile("" : "+v"(nonce));
      if (lane == 0) *(volatile LAS unsigned*)(lds + L_WORDS + 16) = (MODE == 0) ? nonce * 0x9E3779B1u + 0x7F4A7C15u : 0x51u;
    }
    WAITL0();
    STAMP(19 + s);
    BAR();
    STAMP(7 + s);
    slot = (slot == 2) ? 0 : slot + 1;
    blockB_rest(lds + slot * K1_STAGE, true);
  }
  PAD_MFMA_OUT();
  STAMP(3);
  const lptr cs = lds + L_CS;
  const lptr stats = lds + L_STATS;
  {
    const rsrc_t rab = mk_rsrc(ws + WS_AB, 2 * 28 * 64 * 1024);
#pragma unroll
    for (int pi = 0; pi < 2; ++pi) {
      const lptr sp = stats + (64 * wm + 32 * pi + 8 * g) * 8;
      const f32x4 s0 = *(LAS f32x4*)(sp), s1 = *(LAS f32x4*)(sp + 16), s2 = *(LAS f32x4*)(sp + 32), s3 = *(LAS f32x4*)(sp + 48);
#pragma unroll
      for (int j = 0; j < NA; ++j) {
        const float c = *(LAS float*)(cs + (2 * NBK + NA * wn + j) * 64 + i16 * 4);
        const f32x4 a_ = AB[j][2 * pi], b = AB[j][2 * pi + 1];
        const int ks = mt * 4 + wm * 2 + pi;
        u32x4 o;
        o[0] = pk_bf16((a_[0] - s0[0] * c) * s0[1], (a_[1] - s0[2] * c) * s0[3]);
        o[1] = pk_bf16((a_[2] - s1[0] * c) * s1[1], (a_[3] - s1[2] * c) * s1[3]);
        o[2] = pk_bf16((b[0] - s2[0] * c) * s2[1], (b[1] - s2[2] * c) * s2[3]);
        o[3] = pk_bf16((b[2] - s3[0] * c) * s3[1], (b[3] - s3[2] * c) * s3[3]);
        __builtin_amdgcn_raw_buffer_store_b128(o, rab, lane * 16 + ((a0 + NA * wn + j) * 64 + ks) * 1024, 0, STAUX);
      }
    }
  }
  {
    lptr hd = lds + L_HD + (2 * wn + wm) * (NG * 4096) + lane * 16;
#pragma unroll
    for (int j = 0; j < NG; ++j)
#pragma unroll
      for (int tl = 0; tl < 2; ++tl) {
        *(LAS f32x4*)(hd + ((j * 2 + 0) * 2 + tl) * 1024) = G[j][2 + tl];
        *(LAS f32x4*)(hd + ((j * 2 + 1) * 2 + tl) * 1024) = U[j][2 + tl];
      }
  }
  WAITV(0);
  if (lane == 0) {
    const unsigned n = __hip_atomic_fetch_add((LAS unsigned*)(lds + L_WORDS), 1u, __ATOMIC_RELAXED, __HIP_MEMORY_SCOPE_WORKGROUP);
    if (n == 3u) st_flag(ws + WS_FAB + (mt * 16 + grp) * 64, get_tag(lds));
  }
  STAMP(5);
  WAITL0(); BAR();
  STAMP(14);
  const rsrc_t rh = mk_rsrc(ws + WS_H, 128 * 56 * 1024);
  const int tt0 = mt * 8 + wm * 4;
  f32x2 st[2];
#pragma unroll
  for (int t = 0; t < 2; ++t) st[t] = *(LAS f32x2*)(stats + (64 * wm + 8 * (i16 >> 2) + 4 * t + (i16 & 3)) * 8);
#pragma unroll
  for (int pp = 0; pp < NG / 2; ++pp) {
    const int ks = ((f0 >> 4) + NG * wn + 2 * pp) >> 1;
    f32x4 cg[2], cu[2], gq[2][2], uq[2][2];
#pragma unroll
    for (int j = 0; j < 2; ++j) {
      cg[j] = *(LAS f32x4*)(cs + (NG * wn + 2 * pp + j) * 64 + g * 16);
      cu[j] = *(LAS f32x4*)(cs + (NBK + NG * wn + 2 * pp + j) * 64 + g * 16);
      gq[j][0] = G[2 * pp + j][0]; gq[j][1] = G[2 * pp + j][1]; uq[j][0] = U[2 * pp + j][0]; uq[j][1] = U[2 * pp + j][1];
    }
    h_batch(gq, uq, cg, cu, st, rh, lane * 16 + (tt0 * 56 + ks) * 1024);
  }
  STAMP(4);
}

template <int NG>
__device__ __forceinline__ void p1_helper_epilogue(lptr lds, char* ws, int mt, int f0, int h, int lane) {
  constexpr int NBK = 2 * NG;
  const int wm = h & 1, wn = h >> 1, i16 = lane & 15, g = lane >> 4;
  const lptr cs = lds + L_CS, stats = lds + L_STATS;
  const rsrc_t rh = mk_rsrc(ws + WS_H, 128 * 56 * 1024);
  const int tt0 = mt * 8 + wm * 4 + 2;
  lptr hd = lds + L_HD + h * (NG * 4096) + lane * 16;
  f32x2 st[2];
#pragma unroll
  for (int t = 0; t < 2; ++t) st[t] = *(LAS f32x2*)(stats + (64 * wm + 32 + 8 * (i16 >> 2) + 4 * t + (i16 & 3)) * 8);
#pragma unroll
  for (int pp = 0; pp < NG / 2; ++pp) {
    const int ks = ((f0 >> 4) + NG * wn + 2 * pp) >> 1;
    f32x4 cg[2], cu[2], gq[2][2], uq[2][2];
#pragma unroll
    for (int j = 0; j < 2; ++j) {
      cg[j] = *(LAS f32x4*)(cs + (NG * wn + 2 * pp + j) * 64 + g * 16);
      cu[j] = *(LAS f32x4*)(cs + (NBK + NG * wn + 2 * pp + j) * 64 + g * 16);
#pragma unroll
      for (int tl = 0; tl < 2; ++tl) {
        gq[j][tl] = *(LAS f32x4*)(hd + (((2 * pp + j) * 2 + 0) * 2 + tl) * 1024);
        uq[j][tl] = *(LAS f32x4*)(hd + (((2 * pp + j) * 2 + 1) * 2 + tl) * 1024);
      }
    }
    h_batch(gq, uq, cg, cu, st, rh, lane * 16 + (tt0 * 56 + ks) * 1024);
  }
}

template <int MODE, bool TB>
__device__ __forceinline__ void p1_helper(lptr lds, Args a, int xcd, int idx, int mt, int grp, int f0, int a0, int h, int lane) {
  constexpr int NBK = TB ? 4 : 8, WSZ = NBK * 8 * 256;
  constexpr int NWP = TB ? 4 : 8, NDP = TB ? 4 : 1, PER = 4 + NWP + NDP;
  const int mh = xcd & 1, nq = xcd >> 1;
  const int up = h >> 1;
  const u32x4 rx = mk_srd(a.x, S * D * 2);
  const u32x4 rw = mk_srd(up ? a.Wu : a.Wg, D * F * 2);
  const u32x4 rd = mk_srd(a0 >= 28 ? a.Di : a.Dr, D * D * 2);
  const int e0 = (a0 >= 28 ? a0 - 28 : a0) * 16;
  const int r8 = lane >> 3, c8 = lane & 7;
  int vx[2];
  vx[0] = r8 * 896 + (c8 ^ (r8 & 3)) * 16;
  vx[1] = r8 * 896 + (c8 ^ ((r8 & 3) | 4)) * 16;
  const int vd = r8 * 896 + (c8 ^ r8) * 16;
  int vw[2];
  {
    const int f0_ = (r8 >> 1) & 1;
    vw[0] = r8 * (F * 2) + f0 * 2 + ((((c8 >> 1) ^ f0_) * 2) + (c8 & 1)) * 16;
    vw[1] = r8 * (F * 2) + f0 * 2 + ((((c8 >> 1) ^ (f0_ | 2)) * 2) + (c8 & 1)) * 16;
  }
  const unsigned l0 = lds_addr(lds);
  const unsigned dummy = l0 + L_DUMMY + (h & 1) * 256;
  const int sxb = (mt * 128 + 32 * h) * 896, swb = 32 * (h & 1) * (F * 2), sdb = (e0 + 8 * NDP * h) * 896;
  const unsigned lxb = l0 + 4 * h * 1024, lwb = l0 + K1_WO + up * WSZ + NWP * (h & 1) * 1024, ldb = l0 + K1_WO + 2 * WSZ + NDP * h * 1024;
  auto piece = [&](int s, int slot, int i) {
    const unsigned so = slot * K1_STAGE;
    if (i < 4) dma16(rx, lxb + so + i * 1024, vx[i & 1], sxb + s * 128 + i * 8 * 896);
    else if (i < 4 + NWP) {
      const int j = i - 4, kb = TB ? j : j >> 1, cq = TB ? 0 : j & 1;
      dma16(rw, lwb + so + j * 1024, vw[kb & 1], swb + s * 64 * (F * 2) + kb * 8 * (F * 2) + cq * 128);
    } else dma16(rd, ldb + so + (i - 4 - NWP) * 1024, vd, sdb + s * 128 + (i - 4 - NWP) * 8 * 896);
  };
  auto issue_a = [&](int s, int slot) {
#pragma unroll
    for (int i = 0; i < 8; ++i) piece(s, slot, i);
  };
  auto issue_b = [&](int s, int slot) {
#pragma unroll
    for (int i = 8; i < PER; ++i) piece(s, slot, i);
  };
  const int ln2 = lane >> 1, hb = (lane & 1) * 64;
  const char* pfx = (const char*)a.x + (size_t)(1024 * mh + 32 * idx + ln2) * 896 + hb;
  STAMP(1);
  issue_a(0, 0); issue_b(0, 0);
  STAMP(5);
  launder(a, xcd, idx);
  char* ws = a.ws;
  const bool wjob = idx < 28;
  const int wj_mat = idx >= 14, wj_cb = (idx - 14 * wj_mat) >> 1, wj_kh = idx & 1;
  u32x4 wx[7];
  {
    const u32x4 rj = mk_srd(wj_mat ? a.Wu : a.Wg, D * F * 2);
    const int vj = r8 * (F * 2) + c8 * 16 + (448 * nq + 64 * (wjob ? wj_cb : 0)) * 2;
    const int sj = (224 * (wjob ? wj_kh : 0) + 56 * h) * (F * 2);
    asm volatile("buffer_load_dwordx4 %0, %7, %8, %9 offen\n\tbuffer_load_dwordx4 %1, %7, %8, %10 offen\n\tbuffer_load_dwordx4 %2, %7, %8, %11 offen\n\t"
                 "buffer_load_dwordx4 %3, %7, %8, %12 offen\n\tbuffer_load_dwordx4 %4, %7, %8, %13 offen\n\tbuffer_load_dwordx4 %5, %7, %8, %14 offen\n\t"
                 "buffer_load_dwordx4 %6, %7, %8, %15 offen"
                 : "=&v"(wx[0]), "=&v"(wx[1]), "=&v"(wx[2]), "=&v"(wx[3]), "=&v"(wx[4]), "=&v"(wx[5]), "=&v"(wx[6])
                 : "v"(vj), "s"(rj), "s"(sj), "s"(sj + 8 * F * 2), "s"(sj + 16 * F * 2), "s"(sj + 24 * F * 2), "s"(sj + 32 * F * 2), "s"(sj + 40 * F * 2), "s"(sj + 48 * F * 2)
                 : "memory");
  }
  RowJobs rj_;
  rj_.load(a, nq, idx, mt, grp, h, lane);
  pf4(pfx + (1 + h) * 128, dummy);
  if (h == 3 && lane < 4) ((LAS unsigned*)(lds + L_WORDS))[lane] = 0u;
  WAITL0(); WAITV(12);
  STAMP(7); BAR();
  STAMP(2);
#pragma unroll
  for (int i = 0; i < PER; ++i) piece(1, 1, i);
  issue_a(2, 2);
  if (h < 2) pf4(pfx + (5 + h) * 128, dummy);
  WAITL0();
  if (h < 2) { WAITV(9); } else { WAITV(8); }
  asm volatile("" : "+v"(wx[0]), "+v"(wx[1]), "+v"(wx[2]), "+v"(wx[3]), "+v"(wx[4]), "+v"(wx[5]), "+v"(wx[6]));
  rj_.landed();
  BAR();
  const unsigned tag1 = get_tag(lds);
  float cw[8];
#pragma unroll
  for (int k = 0; k < 8; ++k) cw[k] = 0.f;
  constexpr bool TBc = TB;
  unsigned long long g0 = 0, g1 = 0, g2 = 0, g3 = 0;
  const bool wdjob = blockIdx.x < 196;
  const int wd_kp = blockIdx.x % 28, wd_cg = blockIdx.x / 28;
#pragma unroll
  for (int s = 1; s < K1_NST; ++s) {
    const int s1_ = (s + 1) % 3, s2_ = (s + 2) % 3;
    if (s == 1) {
#pragma unroll
      for (int i = 0; i < PER; ++i) {
        if (i < PER - 8) piece(2, s1_, 8 + i); else piece(3, s2_, i - (PER - 8));
        SBAR();
        if (i < 7) {
#pragma unroll
          for (int k = 0; k < 4; ++k) { cw[2 * k] += bf_lo(wx[i][k]); cw[2 * k + 1] += bf_hi(wx[i][k]); }
        }
        SBAR();
      }
    } else {
      if (s + 1 < K1_NST) issue_b(s + 1, s1_);

      if (s + 2 < K1_NST) issue_a(s + 2, s2_);
    }
    if (s == 1) {
      pf4((const char*)a.Wd + (size_t)(64 * wd_kp + 16 * h + ((lane >> 1) & 15)) * 896 + (wdjob ? wd_cg : 0) * 128 + (lane & 1) * 64, dummy);
#pragma unroll
      for (int k = 0; k < 8; ++k) {
        cw[k] += __builtin_bit_cast(float, __builtin_amdgcn_update_dpp(0, __builtin_bit_cast(int, cw[k]), 0x128, 0xf, 0xf, true));
        const auto p16 = __builtin_amdgcn_permlane16_swap(__float_as_uint(cw[k]), __float_as_uint(cw[k]), false, false);
        cw[k] = __uint_as_float(p16[0]) + __uint_as_float(p16[1]);
        const auto p32 = __builtin_amdgcn_permlane32_swap(__float_as_uint(cw[k]), __float_as_uint(cw[k]), false, false);
        cw[k] = __uint_as_float(p32[0]) + __uint_as_float(p32[1]);
      }
      if (lane < 8) {
        *(LAS f32x4*)(lds + L_CS + h * 256 + lane * 32) = f32x4{cw[0], cw[1], cw[2], cw[3]};
        *(LAS f32x4*)(lds + L_CS + h * 256 + lane * 32 + 16) = f32x4{cw[4], cw[5], cw[6], cw[7]};
      }
    }
    if (s == 2) rj_.sums(lane);
    if (s == 3) rj_.finish(ws, tag1, nq, idx, mt, grp, h, lane);
    if (s == 2 && h == 0 && wjob) {
      const float t = (*(LAS float*)(lds + L_CS + lane * 4) + *(LAS float*)(lds + L_CS + 256 + lane * 4)) +
                      (*(LAS float*)(lds + L_CS + 512 + lane * 4) + *(LAS float*)(lds + L_CS + 768 + lane * 4));
      __hip_atomic_store((GAS unsigned long long*)(ws + WS_CW + (size_t)((wj_mat * 2 + wj_kh) * F + 448 * nq + 64 * wj_cb + lane) * 8),
                         ((unsigned long long)tag1 << 32) | __float_as_uint(t), __ATOMIC_RELAXED, __HIP_MEMORY_SCOPE_AGENT);
    }
    if (s == 4 && h == 0 && lane == 0) st_flag(ws + WS_ARRIVE + blockIdx.x * 64, tag1);
    if (s == 5) {
      if (wdjob) {
        const u32x4 rwd = mk_srd(a.Wd, F * D * 2);
        const int rs = (lane & 15) >> 1, half = lane & 1, b = lane >> 4;
        const int vq = rs * 896 + (64 * wd_cg + 16 * b + 8 * half) * 2;
#pragma unroll
        for (int i = 0; i < 2; ++i) dma16(rwd, l0 + s2_ * K1_STAGE + (2 * h + i) * 1024, vq, (64 * wd_kp + 8 * (2 * h + i)) * 896);
      }
    }
    const int gc = 64 * h + lane;
    constexpr int NWC = TBc ? 128 : 256, CPM = NWC / 2;
    const bool isw = gc < NWC;
    const char* p_st = ws + WS_ST + (size_t)(mt * 128 + 32 * h + (lane & 31)) * 8;
    const char* p_c0 = ws + (isw ? WS_CW + (size_t)((gc / CPM) * 2 * F + f0 + gc % CPM) * 8 : WS_CD + (size_t)(a0 * 16 + (gc - NWC)) * 8);
    const char* p_c1 = isw ? p_c0 + (size_t)F * 8 : p_c0;
    const char* p_d = ws + WS_CD + (size_t)(a0 * 16 + (lane & 31)) * 8;
    if (s == 5)
      asm volatile("global_load_dwordx2 %0, %4, off sc1\n\tglobal_load_dwordx2 %1, %5, off sc1\n\tglobal_load_dwordx2 %2, %6, off sc1\n\tglobal_load_dwordx2 %3, %7, off sc1"
                   : "=&v"(g0), "=&v"(g1), "=&v"(g2), "=&v"(g3) : "v"(p_st), "v"(p_c0), "v"(p_c1), "v"(p_d) : "memory");
    if (s == 6) {
      WAITV(DFTPF == 2 ? 2 : 0);
      asm volatile("" : "+v"(g0), "+v"(g1), "+v"(g2), "+v"(g3));
      auto ld_gran = [&](const char* p) { return __hip_atomic_load((const GAS unsigned long long*)p, __ATOMIC_RELAXED, __HIP_MEMORY_SCOPE_AGENT); };
      for (unsigned sp = 0; sp < SPIN_MAX; ++sp) {
        if (__all((unsigned)(g0 >> 32) == tag1 && (unsigned)(g1 >> 32) == tag1 && (unsigned)(g2 >> 32) == tag1 && (unsigned)(g3 >> 32) == tag1)) break;
        __builtin_amdgcn_s_sleep(2);
        g0 = ld_gran(p_st); g1 = ld_gran(p_c0); g2 = ld_gran(p_c1); g3 = ld_gran(p_d);
      }
      if (lane < 32) *(LAS f32x2*)(lds + L_STATS + (32 * h + lane) * 8) = f32x2{bf_lo((unsigned)g0), 1.0f / bf_hi((unsigned)g0)};
      *(LAS float*)(lds + L_CS + gc * 4) = isw ? __uint_as_float((unsigned)g1) + __uint_as_float((unsigned)g2) : __uint_as_float((unsigned)g1);
      if (!TBc && h == 0 && lane < 32) *(LAS float*)(lds + L_CS + (256 + lane) * 4) = __uint_as_float((unsigned)g3);
    }
    WAITL0();
    if (s == 1) { WAITV(9); } else if (s + 2 < K1_NST) { WAITV(8); } else if (s == 5) { if (wdjob) { WAITV(6 + (DFTPF == 2 ? 2 : 0)); } else { WAITV(4 + (DFTPF == 2 ? 2 : 0)); } }
    STAMP(7 + s);
    BAR();
  }
  __builtin_amdgcn_s_setprio(0);
  if (h == 3 && wdjob) {
    const int i16 = lane & 15, g = lane >> 4, q = i16 >> 2, p = i16 & 3;
    const rsrc_t rt = mk_rsrc(ws + WS_WDT, 28 * 56 * 1024);
    lptr b0 = lds + 1 * K1_STAGE + (g >> 1) * 1024 + ((g & 1) * 4 + q) * 32 + p * 8;
#pragma unroll
    for (int kk = 0; kk < 2; ++kk)
#pragma unroll
      for (int u = 0; u < 4; ++u) {
        const bf16x8 f = ldstr(b0 + kk * 4096 + u * 256, b0 + kk * 4096 + u * 256 + 2048);
        __builtin_amdgcn_raw_buffer_store_b128(__builtin_bit_cast(u32x4, f), rt, lane * 16 + ((4 * wd_cg + u) * 56 + 2 * wd_kp + kk) * 1024, 0, STAUX);
      }
    WAITL0();
  }
  BAR();
  p1_helper_epilogue<TB ? 2 : 4>(lds, ws, mt, f0, h, lane);
}

template <int MODE, int NB, int JO>
__device__ __forceinline__ void p2_loader(lptr lds, const Args& a, int xcd, int idx, int r, int m, int n, int lw, int lane) {
  char* ws = a.ws;
  constexpr int PER = 4 + NB;
  const u32x4 rc = mk_srd((r >> 1) ? a.Ci : a.Cr, S * S * 2);
  const u32x4 rb = mk_srd(ws + WS_AB, 2 * 28 * 64 * 1024);
  const u32x4 rh = mk_srd(ws + WS_H, 128 * 56 * 1024);
  const u32x4 rw = mk_srd(ws + WS_WDT, 28 * 56 * 1024);
  const int r8 = lane >> 3, c8 = lane & 7;
  int va[2];
  va[0] = r8 * 4096 + (c8 ^ (r8 & 3)) * 16;
  va[1] = r8 * 4096 + (c8 ^ ((r8 & 3) | 4)) * 16;
  const int vl = lane * 16;
  const int k0 = 1024 * (r & 1), p = r >> 1;
  const int j0 = (lw < 2) ? 4 * lw : 8 + 3 * (lw - 2);
  const unsigned l0 = lds_addr(lds);
  const unsigned dummy = l0 + L_DUMMY + (lw & 1) * 256;
  const int sab = (128 * m + 32 * lw) * 4096 + k0 * 2;
  const int shb = ((8 * m + 2 * lw) * 56 + 14 * r) * 1024;
  const unsigned lab = l0 + 4 * lw * 1024, lbb = l0 + 16384 + j0 * 1024;
  const int sbb = ((p * 28 + 7 * n + (j0 >> 1)) * 64 + (k0 >> 5)) * 1024, swb = ((7 * n + (j0 >> 1)) * 56 + 14 * r) * 1024;
  auto issueAy = [&](int s, int slot) {
    const unsigned so = slot * P_STAGE; const int b = sab + 128 * (s & 15);
#pragma unroll
    for (int i = 0; i < 4; ++i) dma16(rc, lab + so + i * 1024, va[i & 1], b + i * 8 * 4096);
  };
  auto issueAd = [&](int sd, int slot) {
    const unsigned so = slot * P_STAGE; const int b = shb + 2048 * sd;
#pragma unroll
    for (int i = 0; i < 4; ++i) dma16c(rh, lab + so + i * 1024, vl, b + (i >> 1) * 56 * 1024 + (i & 1) * 1024);
  };
  auto issueBy = [&](int s, int slot) {
    const unsigned so = slot * P_STAGE; const int b = sbb + 2048 * (s & 15);
#pragma unroll
    for (int j = 0; j < NB; ++j) dma16c(rb, lbb + so + j * 1024, vl, b + ((j + JO) >> 1) * 65536 + ((j + JO) & 1) * 1024);
  };
  auto issueBd = [&](int sd, int slot) {
    const unsigned so = slot * P_STAGE; const int b = swb + 2048 * sd;
#pragma unroll
    for (int j = 0; j < NB; ++j) dma16c(rw, lbb + so + j * 1024, vl, b + ((j + JO) >> 1) * 57344 + ((j + JO) & 1) * 1024);
  };
  __builtin_amdgcn_s_setprio(0);
  const unsigned tag = get_tag(lds);
  if (MODE != 2 && lw == 3 && blockIdx.x < 196) {
    WAITV(0);
    if (lane == 0) st_flag(ws + WS_FWD + blockIdx.x * 64, tag);
  }
  issueAy(0, 0); issueAy(1, 1);
  if (lw == 3) {
    const int mtp = 8 * (r & 1) + (lane & 7), j = (lane >> 3) & 3;
    const bool act = lane < 32 && ((n & 1) == 0 || j == 3);
    poll_all(ws + WS_FAB + (mtp * 16 + (2 * p + (n >> 1)) * 4 + j) * 64, act, tag);
    if (lane == 0) *(volatile LAS unsigned*)(lds + L_WORDS + 8) = 1u;
  } else lds_spin(lds + L_WORDS + 8);
  STAMP(16);
  issueBy(0, 0);
  {
    const int l = (lane >> 1) < 28 ? (lane >> 1) : 27;
    const int sl = l >= 14, li = 14 * idx + l - 14 * sl;
    const char* bp = ws + WS_AB + ((size_t)(p * 28 + (li >> 4)) * 64 + 32 * (r & 1) + 2 * (4 * lw + sl) + ((li >> 3) & 1)) * 1024 + (li & 7) * 128 + (lane & 1) * 64;
    pf4c(bp, dummy); pf4c(bp + 4 * 1024, dummy);
  }
  issueBy(1, 1);
  __builtin_amdgcn_s_setprio(3);
  WAITV(NB + 2); BAR();
  if (MODE == 0 && lane == 0) {
    const unsigned cnt = __hip_atomic_fetch_add((LAS unsigned*)(lds + L_WORDS + 4), 1u, __ATOMIC_RELAXED, __HIP_MEMORY_SCOPE_WORKGROUP);
    if (cnt == 7u) st_flag(ws + WS_FH + (((xcd & 1) * 8 + (idx & 7)) * 16 + (xcd >> 1) * 4 + (idx >> 3)) * 64, tag);
  }
  issueAy(2, 2); issueBy(2, 2);
  WAITV(PER); BAR();
  issueAy(3, 3); issueBy(3, 3); issueAy(4, 4); issueBy(4, 4);
  WAITV(2 * PER); BAR();
  int slot = 0;
  auto ystep = [&](int s) { issueAy(s + 3, slot); issueBy(s + 3, slot); slot = (slot == 4) ? 0 : slot + 1; WAITV(2 * PER); BAR(); };
  auto dstep = [&](int sd) { issueAd(sd, slot); issueBd(sd, slot); slot = (slot == 4) ? 0 : slot + 1; WAITV(2 * PER); BAR(); };
#pragma unroll 1
  for (int s = 2; s < P_NY - 9; ++s) ystep(s);
  const int cg0 = (112 * n) >> 6, cg1 = (112 * n + 111) >> 6, jj = lane - 4;
  const bool fact = lane < 4 || (jj < 21 && cg0 + jj / 7 <= cg1);
  const char* fp = lane < 4 ? ws + WS_FH + (m * 16 + 4 * r + lane) * 64 : ws + WS_FWD + ((fact ? cg0 + jj / 7 : cg0) * 28 + 7 * r + (fact ? jj % 7 : 0)) * 64;
  unsigned fval = 0;
  if (lw == 3) asm volatile("global_load_dword %0, %1, off sc1" : "+v"(fval) : "v"(fp) : "memory");
#pragma unroll 1
  for (int s = P_NY - 9; s < P_NY - 6; ++s) ystep(s);
  if (lw == 3) {
    asm volatile("" : "+v"(fval));
    if (!__all(!fact || fval == tag)) poll_all(fp, fact, tag);
    if (lane == 0) *(volatile LAS unsigned*)(lds + L_WORDS + 12) = 1u;
  }
#pragma unroll 1
  for (int s = P_NY - 6; s < P_NY - 3; ++s) ystep(s);
  if (lw != 3) lds_spin(lds + L_WORDS + 12);
#pragma unroll 1
  for (int sd = 0; sd < P_ND; ++sd) dstep(sd);
  WAITV(PER); BAR();
  WAITV(0); BAR();
}

template <int MODE>
__global__ __launch_bounds__(512, 2) void fused_kernel(Args a) {
  extern __shared__ __attribute__((aligned(1024))) char smem[];
  lptr lds = (lptr)smem;
  char* ws = a.ws;
  const int tid = threadIdx.x, lane = tid & 63;
  const int wid = __builtin_amdgcn_readfirstlane(tid >> 6);
  int xcd = blockIdx.x & 7, idx = blockIdx.x >> 3;
  STAMP(0); STAMPR(30);
  { const size_t n_ = (size_t)27623424 / 4096; for (size_t i_ = (size_t)blockIdx.x * 512 + tid; i_ < n_; i_ += 131072) { unsigned v_ = *(const volatile unsigned*)((const char*)(ws) + i_ * 4096); asm volatile("" :: "v"(v_)); } }
  { const unsigned long long t0_ = __builtin_amdgcn_s_memtime(); while (__builtin_amdgcn_s_memtime() - t0_ < 100000ull) { asm volatile("v_nop\n\tv_nop\n\tv_nop\n\tv_nop"); } }
  if (wid == 3) {
    unsigned long long pc;
    asm volatile("s_getpc_b64 %0" : "=s"(pc));
    const unsigned ln = 13 * idx + ((lane >> 1) < 13 ? (lane >> 1) : 12);
    pf4((const char*)(pc & ~127ull) + (ln * 128 < CODE_PF ? ln * 128 : 0) + (lane & 1) * 64, lds_addr(lds) + L_DUMMY);
  }
  if (wid >= 4) __builtin_amdgcn_s_setprio(3);

  if (MODE != 2) {
    const int nq = xcd >> 1;
    const int mt = (xcd & 1) * 8 + (idx & 7), j = idx >> 3;
    const bool tb = j == 3;
    const int grp = nq * 4 + j;
    const int f0 = 448 * nq + 128 * j, a0 = 14 * nq + 2 * j;
    if (wid < 4) {
      if (tb) p1_consumer<MODE, 2, 4>(lds, a, xcd, idx, mt, grp, f0, a0, wid & 1, wid >> 1, lane);
      else p1_consumer<MODE, 4, 1>(lds, a, xcd, idx, mt, grp, f0, a0, wid & 1, wid >> 1, lane);
    } else {
      const int h = wid - 4;
      if (tb) p1_helper<MODE, true>(lds, a, xcd, idx, mt, grp, f0, a0, h, lane);
      else p1_helper<MODE, false>(lds, a, xcd, idx, mt, grp, f0, a0, h, lane);
    }
  }
  if (MODE == 1) return;
  if (MODE == 2) { if (tid < 4) ((LAS unsigned*)(lds + L_WORDS))[tid] = 0u; if (tid == 4) ((LAS unsigned*)(lds + L_WORDS))[4] = 0x51u; __syncthreads(); }

  launder(a, xcd, idx);
  ws = a.ws;
  const int p1_mt = (xcd & 1) * 8 + (idx & 7), p1_grp = (xcd >> 1) * 4 + (idx >> 3);
  const int r = xcd >> 1, m = 8 * (xcd & 1) + (idx & 7), n = idx >> 3;
  const int tile = m * 4 + n;
  const int i16 = lane & 15, g = lane >> 4;
  f32x4 accY[7][2], acc[7][2];
  if (wid == 7) {
    p2_loader<MODE, 3, 1>(lds, a, xcd, idx, r, m, n, 3, lane);
  } else if (wid == 6) {
    p2_loader<MODE, 3, 0>(lds, a, xcd, idx, r, m, n, 2, lane);
  } else if (wid >= 4) {
    p2_loader<MODE, 4, 0>(lds, a, xcd, idx, r, m, n, wid - 4, lane);
  } else {
    const int w = wid;
    const int rowt = 32 * w + 8 * (i16 >> 2) + (i16 & 3);
    const int vt0 = rowt * 128 + ((g) ^ (i16 & 7)) * 16, vt1 = rowt * 128 + ((g + 4) ^ (i16 & 7)) * 16;
    const int ve = 16384 + lane * 16;
    const int vh = (4 * w) * 1024 + lane * 16;
    bf16x8 T0[4], T1[4], E[14];
    auto ldTy = [&](bf16x8 (&T)[4], lptr st) {
      T[0] = ldsr128(st + vt0); T[1] = ldsr128(st + vt0 + 512); T[2] = ldsr128(st + vt1); T[3] = ldsr128(st + vt1 + 512);
    };
    auto ldTd = [&](bf16x8 (&T)[4], lptr st) {
      T[0] = ldsr128(st + vh); T[1] = ldsr128(st + vh + 2048); T[2] = ldsr128(st + vh + 1024); T[3] = ldsr128(st + vh + 3072);
    };
    auto ldE = [&](int j, lptr st) { E[j] = ldsr128(st + ve + ((j % 7) * 2 + j / 7) * 1024); };
    auto mm = [&](int j, bf16x8 (&T)[4]) {
      mfma_ip(acc[j % 7][0], E[j], T[(j / 7) * 2]);
      mfma_ip(acc[j % 7][1], E[j], T[(j / 7) * 2 + 1]);
    };
#define GHEAD 3
    constexpr int GH = GHEAD;
    auto ldT1 = [&](bf16x8 (&T)[4], lptr st, int i, int NT) {
      if (NT == 0) T[i] = ldsr128(st + (i >= 2 ? vt1 : vt0) + (i & 1) * 512);
      else T[i] = ldsr128(st + vh + (i & 1) * 2048 + (i >> 1) * 1024);
    };
    auto rest = [&](bf16x8 (&Tc)[4], bf16x8 (&Tn)[4], lptr sn, int NT) {
#pragma unroll
      for (int j = GH; j < 14; ++j) {
        const int k = j - GH;
        mfma_ip(acc[j % 7][0], E[j], Tc[(j / 7) * 2]); SBAR();
        if (NT != 2 && k < 4 + GH) { if (k < 4) ldT1(Tn, sn, k, NT); else ldE(k - 4, sn); SBAR(); }
        mfma_ip(acc[j % 7][1], E[j], Tc[(j / 7) * 2 + 1]); SBAR();
        if (NT != 2) ldE(j, sn); else FILL();
        SBAR();
      }
    };
    auto head = [&](bf16x8 (&Tc)[4]) {
#pragma unroll
      for (int j = 0; j < GH; ++j) { mm(j, Tc); FILL(); }
      SBAR();
    };
#pragma unroll
    for (int u = 0; u < 7; ++u) { acc[u][0] = f32x4{0, 0, 0, 0}; acc[u][1] = f32x4{0, 0, 0, 0}; }
#pragma unroll
    for (int u = 0; u < 7; ++u) { asm volatile("" : "+v"(acc[u][0])); asm volatile("" : "+v"(acc[u][1])); }
    PAD_MFMA_IN();
    if (w != r) {
      const rsrc_t rp = mk_rsrc(ws + WS_PD + (size_t)((tile * 4 + r) * 4 + w) * 7168, 7168);
      const u32x4 sent = {0xffffffffu, 0xffffffffu, 0xffffffffu, 0xffffffffu};
#pragma unroll
      for (int u = 0; u < 7; ++u) {
        __builtin_amdgcn_raw_buffer_store_b128(sent, rp, u * 1024 + lane * 16, 0, 16);
      }
    }
    STAMP(16); STAMPR(28);
    BAR();
    STAMP(17); STAMPR(27);
    ldTy(T0, lds);
#pragma unroll
    for (int j = 0; j < 14; ++j) ldE(j, lds);
    SBAR();
    head(T0);
    WAITL0(); BAR();
    int slot = 1;
#pragma unroll 1
    for (int s = 0; s < P_NY - 2; s += 2) {
      lptr sn = lds + slot * P_STAGE; slot = (slot == 4) ? 0 : slot + 1;
      rest(T0, T1, sn, 0); head(T1);
      WAITL0(); BAR();
      sn = lds + slot * P_STAGE; slot = (slot == 4) ? 0 : slot + 1;
      rest(T1, T0, sn, 0); head(T0);
      WAITL0(); BAR();
      if (YREP > 1 && s == 14) { STAMP(23); }
      if (MODE == 0 && s == 0) {
        WAITV(0);
        if (lane == 0) {
          const unsigned cnt = __hip_atomic_fetch_add((LAS unsigned*)(lds + L_WORDS + 4), 1u, __ATOMIC_RELAXED, __HIP_MEMORY_SCOPE_WORKGROUP);
          if (cnt == 7u) st_flag(ws + WS_FH + (p1_mt * 16 + p1_grp) * 64, get_tag(lds));
        }
      }
    }
    {
      lptr sn = lds + slot * P_STAGE; slot = (slot == 4) ? 0 : slot + 1;
      rest(T0, T1, sn, 0); head(T1);
      WAITL0(); BAR();
      sn = lds + slot * P_STAGE; slot = (slot == 4) ? 0 : slot + 1;
      rest(T1, T0, sn, 1);
    }
    PAD_MFMA_OUT();
    STAMP(18);
    {
      const float sg = (r >> 1) ? -1.0f : 1.0f;
#pragma unroll
      for (int u = 0; u < 7; ++u) { accY[u][0] = acc[u][0] * sg; accY[u][1] = acc[u][1] * sg; acc[u][0] = f32x4{0, 0, 0, 0}; acc[u][1] = f32x4{0, 0, 0, 0}; }
      if (w != r) {
        const rsrc_t rp = mk_rsrc(ws + WS_PY + (size_t)((tile * 4 + r) * 4 + w) * 7168, 7168);
#pragma unroll
        for (int u = 0; u < 7; ++u) {
          const u32x4 o = {pk_bf16(accY[u][0][0], accY[u][0][1]), pk_bf16(accY[u][0][2], accY[u][0][3]),
                           pk_bf16(accY[u][1][0], accY[u][1][1]), pk_bf16(accY[u][1][2], accY[u][1][3])};
          __builtin_amdgcn_raw_buffer_store_b128(o, rp, u * 1024 + lane * 16, 0, 16);
        }
      }
    }
#pragma unroll
    for (int u = 0; u < 7; ++u) { asm volatile("" : "+v"(acc[u][0])); asm volatile("" : "+v"(acc[u][1])); }
    PAD_MFMA_IN();
    head(T0);
    WAITL0(); BAR();
#pragma unroll 1
    for (int s = 0; s < P_ND - 1; s += 2) {
      lptr sn = lds + slot * P_STAGE; slot = (slot == 4) ? 0 : slot + 1;
      rest(T0, T1, sn, 1); head(T1);
      WAITL0(); BAR();
      if (s == 0 && w != r) {
        WAITV(0);
        const unsigned tag = get_tag(lds);
        if (lane == 0) st_flag(ws + WS_FY + ((tile * 4 + r) * 4 + w) * 64, tag);
      }
      sn = lds + slot * P_STAGE; slot = (slot == 4) ? 0 : slot + 1;
      rest(T1, T0, sn, 1); head(T0);
      WAITL0(); if (s + 2 < P_ND - 1) BAR();
    }
    rest(T0, T1, lds, 2);
    PAD_MFMA_OUT();
    STAMP(19); STAMPR(29);
    if (w != r) {
      const rsrc_t rp = mk_rsrc(ws + WS_PD + (size_t)((tile * 4 + r) * 4 + w) * 7168, 7168);
#pragma unroll
      for (int u = 0; u < 7; ++u) {
        u32x4 o = {pk_bf16(acc[u][0][0], acc[u][0][1]), pk_bf16(acc[u][0][2], acc[u][0][3]),
                   pk_bf16(acc[u][1][0], acc[u][1][1]), pk_bf16(acc[u][1][2], acc[u][1][3])};
#pragma unroll
        for (int k = 0; k < 4; ++k) o[k] = o[k] < 0xfffffffeu ? o[k] : 0xfffffffeu;
        __builtin_amdgcn_raw_buffer_store_b128(o, rp, u * 1024 + lane * 16, 0, 16);
      }
    } else {
#pragma unroll
      for (int uu = 0; uu < 7; ++uu) {
        *(LAS f32x4*)(lds + (uu * 4 + 0) * 1024 + lane * 16) = accY[uu][0];
        *(LAS f32x4*)(lds + (uu * 4 + 1) * 1024 + lane * 16) = accY[uu][1];
        *(LAS f32x4*)(lds + (uu * 4 + 2) * 1024 + lane * 16) = acc[uu][0];
        *(LAS f32x4*)(lds + (uu * 4 + 3) * 1024 + lane * 16) = acc[uu][1];
      }
      WAITL0();
    }
    BAR();
  }
  if (wid >= 4) {
    __builtin_amdgcn_s_setprio(0);
    const int hw = wid - 4;
    const int nu = hw < 3 ? 2 : 1;
    const unsigned tag = get_tag(lds);
    const int tok0 = 128 * m + 32 * r + 8 * (i16 >> 2) + (i16 & 3);
    const rsrc_t ry = mk_rsrc(ws + WS_PY, 64 * 16 * 7168), rd = mk_rsrc(ws + WS_PD, 64 * 16 * 7168);
    u32x2 x0[2], x1[2];
    u32x4 fy[2][3], fd[2][3];
#pragma unroll
    for (int k = 0; k < 2; ++k) {
      const int u = 2 * hw + (k < nu ? k : 0);
      const int ecol = 112 * n + 16 * u + 4 * g;
      x0[k] = *(const u32x2*)(a.x + (size_t)tok0 * D + ecol); x1[k] = *(const u32x2*)(a.x + (size_t)(tok0 + 4) * D + ecol);
    }
    {
      const int rr = (r + 1 + (lane % 3)) & 3;
      poll_all(ws + WS_FY + ((tile * 4 + rr) * 4 + r) * 64, lane < 3, tag);
    }
#pragma unroll
    for (int k = 0; k < 2; ++k)
#pragma unroll
      for (int i = 0; i < 3; ++i)
        fy[k][i] = __builtin_amdgcn_raw_buffer_load_b128(ry, ((tile * 4 + ((r + 1 + i) & 3)) * 4 + r) * 7168 + (2 * hw + (k < nu ? k : 0)) * 1024 + lane * 16, 0, 16);
    STAMP(20);
    if (MODE == 0 && blockIdx.x == 0 && hw == 3) {
#pragma unroll 1
      for (int i = 0; i < 4; ++i) poll_all(ws + WS_ARRIVE + (64 * i + lane) * 64, true, tag);
      if (lane == 0) st_flag(ws + WS_NONCE, tag);
    }
    for (unsigned sp = 0; sp < (SPIN_MAX >> 3); ++sp) {
      const unsigned v = lane < 3 ? __builtin_amdgcn_raw_buffer_load_b32(rd, ((tile * 4 + ((r + 1 + lane) & 3)) * 4 + r) * 7168 + 6 * 1024 + 63 * 16 + 12, 0, 16) : 0u;
      if (__all(v != 0xffffffffu)) break;
      __builtin_amdgcn_s_sleep(1);
    }
    for (unsigned sp = 0; sp < (SPIN_MAX >> 3); ++sp) {
      bool ok = true;
#pragma unroll
      for (int k = 0; k < 2; ++k)
#pragma unroll
        for (int i = 0; i < 3; ++i) {
          fd[k][i] = __builtin_amdgcn_raw_buffer_load_b128(rd, ((tile * 4 + ((r + 1 + i) & 3)) * 4 + r) * 7168 + (2 * hw + (k < nu ? k : 0)) * 1024 + lane * 16, 0, 16);
        }
#pragma unroll
      for (int k = 0; k < 2; ++k)
#pragma unroll
        for (int i = 0; i < 3; ++i)
          ok = ok && fd[k][i][0] != 0xffffffffu && fd[k][i][1] != 0xffffffffu && fd[k][i][2] != 0xffffffffu && fd[k][i][3] != 0xffffffffu;
      if (__all(ok)) break;
      __builtin_amdgcn_s_sleep(2);
    }
    STAMP(21); STAMPR(29);
    BAR();
#pragma unroll
    for (int k = 0; k < 2; ++k) {
      if (k < nu) {
        const int u = 2 * hw + k;
        const int ecol = 112 * n + 16 * u + 4 * g;
        f32x4 y0 = *(LAS f32x4*)(lds + (u * 4 + 0) * 1024 + lane * 16), y1 = *(LAS f32x4*)(lds + (u * 4 + 1) * 1024 + lane * 16);
        f32x4 d0 = *(LAS f32x4*)(lds + (u * 4 + 2) * 1024 + lane * 16), d1 = *(LAS f32x4*)(lds + (u * 4 + 3) * 1024 + lane * 16);
#pragma unroll
        for (int i = 0; i < 3; ++i) {
          y0 += f32x4{bf_lo(fy[k][i][0]), bf_hi(fy[k][i][0]), bf_lo(fy[k][i][1]), bf_hi(fy[k][i][1])};
          y1 += f32x4{bf_lo(fy[k][i][2]), bf_hi(fy[k][i][2]), bf_lo(fy[k][i][3]), bf_hi(fy[k][i][3])};
          d0 += f32x4{bf_lo(fd[k][i][0]), bf_hi(fd[k][i][0]), bf_lo(fd[k][i][1]), bf_hi(fd[k][i][1])};
          d1 += f32x4{bf_lo(fd[k][i][2]), bf_hi(fd[k][i][2]), bf_lo(fd[k][i][3]), bf_hi(fd[k][i][3])};
        }
        const u32x2 o0 = {pk_bf16(bf_lo(x0[k][0]) + y0[0] * silu_f(d0[0]), bf_hi(x0[k][0]) + y0[1] * silu_f(d0[1])),
                          pk_bf16(bf_lo(x0[k][1]) + y0[2] * silu_f(d0[2]), bf_hi(x0[k][1]) + y0[3] * silu_f(d0[3]))};
        const u32x2 o1 = {pk_bf16(bf_lo(x1[k][0]) + y1[0] * silu_f(d1[0]), bf_hi(x1[k][0]) + y1[1] * silu_f(d1[1])),
                          pk_bf16(bf_lo(x1[k][1]) + y1[2] * silu_f(d1[2]), bf_hi(x1[k][1]) + y1[3] * silu_f(d1[3]))};
        *(u32x2*)(a.out + (size_t)tok0 * D + ecol) = o0;
        *(u32x2*)(a.out + (size_t)(tok0 + 4) * D + ecol) = o1;
      }
    }
  }
}

#define SPLIT 0
extern "C" void kernel_launch(void* const* d_in, const int* in_sizes, int n_in, void* d_out, int out_size, void* d_ws, size_t ws_size,
                              hipStream_t stream) {
  Args a;
  a.x = (const u16*)d_in[0]; a.Cr = (const u16*)d_in[1]; a.Ci = (const u16*)d_in[2]; a.Dr = (const u16*)d_in[3]; a.Di = (const u16*)d_in[4];
  a.Wg = (const u16*)d_in[6]; a.Wu = (const u16*)d_in[7]; a.Wd = (const u16*)d_in[8];
  a.ws = (char*)d_ws; a.out = (u16*)d_out;
  (void)hipFuncSetAttribute((const void*)fused_kernel<0>, hipFuncAttributeMaxDynamicSharedMemorySize, L_TOTAL);
  fused_kernel<0><<<256, 512, L_TOTAL, stream>>>(a);
}
```

```cpp
#include <hip/hip_runtime.h>
#include <hip/hip_bf16.h>
#include <stdint.h>

typedef unsigned short u16;
typedef __bf16 bf16x8 __attribute__((ext_vector_type(8)));
typedef short s16x8 __attribute__((ext_vector_type(8)));
typedef float f32x4 __attribute__((ext_vector_type(4)));
typedef float f32x2 __attribute__((ext_vector_type(2)));
typedef unsigned u32x4 __attribute__((ext_vector_type(4)));
typedef unsigned u32x2 __attribute__((ext_vector_type(2)));
#define LAS __attribute__((address_space(3)))
#define GAS __attribute__((address_space(1)))
typedef LAS char* lptr;
typedef __amdgpu_buffer_rsrc_t rsrc_t;

constexpr int S = 2048, D = 448, F = 1792;

constexpr size_t WS_NONCE  = 0;
constexpr size_t WS_ARRIVE = 4096;
constexpr size_t WS_FAB    = 32768;
constexpr size_t WS_FH     = 49152;
constexpr size_t WS_FWD    = 65536;
constexpr size_t WS_FY     = 81920;
constexpr size_t WS_FD     = 147456;
constexpr size_t WS_ST     = 212992;
constexpr size_t WS_CW     = 229376;
constexpr size_t WS_CD     = 286720;
constexpr size_t WS_AB     = 327680;
constexpr size_t WS_H      = WS_AB + (size_t)2 * 28 * 64 * 1024;
constexpr size_t WS_WDT    = WS_H + (size_t)128 * 56 * 1024;
constexpr size_t WS_PY     = WS_WDT + (size_t)28 * 56 * 1024;
constexpr size_t WS_PD     = WS_PY + (size_t)64 * 16 * 7 * 1024;

#define STAMP(i)
#define STAMPR(i)
#define SBAR() __builtin_amdgcn_sched_barrier(0)
#define BAR() __builtin_amdgcn_s_barrier()
#define WAITV(n) asm volatile("s_waitcnt vmcnt(%0)" ::"n"(n) : "memory")
#define WAITL0() __builtin_amdgcn_s_waitcnt(0xC07F)
#define CODE_PF 49152
#define DFTPF 0
#define PF0 1
#define STAUX 16
#define SPIN_MAX (1u << 18)

__device__ __forceinline__ rsrc_t mk_rsrc(const void* p, unsigned bytes) {
  return __builtin_amdgcn_make_buffer_rsrc((void*)p, 0, bytes, 0x00020000);
}
__device__ __forceinline__ u32x4 mk_srd(const void* p, unsigned bytes) {
  const unsigned long long a = (unsigned long long)p;
  return u32x4{(unsigned)a, (unsigned)(a >> 32) & 0xffffu, bytes, 0x00020000u};
}
__device__ __forceinline__ unsigned lds_addr(lptr p) { return (unsigned)(size_t)p; }
__device__ __forceinline__ void dma16(const u32x4& srd, unsigned dst, int voff, int soff) {
  asm volatile("s_mov_b32 m0, %0\n\ts_nop 0\n\tbuffer_load_dwordx4 %1, %2, %3 offen lds" ::"s"(dst), "v"(voff), "s"(srd), "s"(soff) : "memory");
}
__device__ __forceinline__ void dma16c(const u32x4& srd, unsigned dst, int voff, int soff) {
  asm volatile("s_mov_b32 m0, %0\n\ts_nop 0\n\tbuffer_load_dwordx4 %1, %2, %3 offen sc1 lds" ::"s"(dst), "v"(voff), "s"(srd), "s"(soff) : "memory");
}
__device__ __forceinline__ void pf4c(const char* p, unsigned dst) {
  asm volatile("s_mov_b32 m0, %0\n\ts_nop 0\n\tglobal_load_lds_dword %1, off sc1" ::"s"(dst), "v"(p) : "memory");
}
__device__ __forceinline__ void pf4(const char* p, unsigned dst) {
  asm volatile("s_mov_b32 m0, %0\n\ts_nop 0\n\tglobal_load_lds_dword %1, off" ::"s"(dst), "v"(p) : "memory");
}
__device__ __forceinline__ bf16x8 ldsr128(lptr p) { return *(const LAS bf16x8*)p; }
__device__ __forceinline__ bf16x8 ldstr(lptr p0, lptr p1) {
  typedef short v4i16_t __attribute__((ext_vector_type(4)));
  v4i16_t lo = __builtin_amdgcn_ds_read_tr16_b64_v4i16((LAS v4i16_t*)p0);
  v4i16_t hi = __builtin_amdgcn_ds_read_tr16_b64_v4i16((LAS v4i16_t*)p1);
  s16x8 v = __builtin_shufflevector(lo, hi, 0, 1, 2, 3, 4, 5, 6, 7);
  return __builtin_bit_cast(bf16x8, v);
}
__device__ __forceinline__ f32x4 mfma(bf16x8 a, bf16x8 b, f32x4 c) { return __builtin_amdgcn_mfma_f32_16x16x32_bf16(a, b, c, 0, 0, 0); }
__device__ __forceinline__ void mfma_ip(f32x4& c, const bf16x8& a, const bf16x8& b) {
  asm volatile("v_mfma_f32_16x16x32_bf16 %0, %1, %2, %0" : "+v"(c) : "v"(a), "v"(b));
}
#define FILL() asm volatile("s_nop 0")
#define PAD_MFMA_OUT() asm volatile("s_nop 7\n\ts_nop 3")
#define PAD_MFMA_IN() asm volatile("s_nop 3")
__device__ __forceinline__ unsigned pk_bf16(float lo, float hi) {
  typedef __bf16 bf16x2 __attribute__((ext_vector_type(2)));
  f32x2 v = {lo, hi};
  bf16x2 b = __builtin_convertvector(v, bf16x2);
  return __builtin_bit_cast(unsigned, b);
}
__device__ __forceinline__ float bf_lo(unsigned v) { return __uint_as_float(v << 16); }
__device__ __forceinline__ float bf_hi(unsigned v) { return __uint_as_float(v & 0xffff0000u); }
__device__ __forceinline__ float rbf(float f) { return bf_lo(pk_bf16(f, 0.f)); }
__device__ __forceinline__ float silu_f(float x) {
  float e = __builtin_amdgcn_exp2f(x * -1.4426950409f);
  return x * __builtin_amdgcn_rcpf(1.0f + e);
}
template <int N>
__device__ __forceinline__ void wave_sums(float (&v)[N]) {
#define DPP_STEP(ctrl, rmask) _Pragma("unroll") for (int i = 0; i < N; ++i) \
    v[i] += __builtin_bit_cast(float, __builtin_amdgcn_update_dpp(0, __builtin_bit_cast(int, v[i]), ctrl, rmask, 0xf, true));
  DPP_STEP(0x111, 0xf)
  DPP_STEP(0x112, 0xf)
  DPP_STEP(0x114, 0xf)
  DPP_STEP(0x118, 0xf)
  DPP_STEP(0x142, 0xa)
  DPP_STEP(0x143, 0xc)
#undef DPP_STEP
#pragma unroll
  for (int i = 0; i < N; ++i) v[i] = __builtin_bit_cast(float, __builtin_amdgcn_readlane(__builtin_bit_cast(int, v[i]), 63));
}
__device__ __forceinline__ unsigned ld_flag(const void* p) { return __hip_atomic_load((const GAS unsigned*)p, __ATOMIC_RELAXED, __HIP_MEMORY_SCOPE_AGENT); }
__device__ __forceinline__ void st_flag(void* p, unsigned v) { __hip_atomic_store((GAS unsigned*)p, v, __ATOMIC_RELAXED, __HIP_MEMORY_SCOPE_AGENT); }
__device__ __forceinline__ unsigned get_tag(lptr lds);
__device__ __forceinline__ void poll_all(const char* p, bool active, unsigned tag) {
  for (unsigned sp = 0; sp < SPIN_MAX; ++sp) {
    const unsigned v = active ? ld_flag(p) : tag;
    if (__all(v == tag)) break;
    __builtin_amdgcn_s_sleep(2);
  }
}
__device__ __forceinline__ void lds_spin(lptr p) {
  for (unsigned sp = 0; sp < SPIN_MAX * 4; ++sp) {
    if (*(volatile LAS unsigned*)p != 0u) break;
    __builtin_amdgcn_s_sleep(2);
  }
}

constexpr int K1_STAGE = 53248, K1_NST = 7, K1_WO = 16384;
constexpr int L_MISC = 3 * K1_STAGE;
constexpr int L_STATS = L_MISC;
constexpr int L_CS = L_MISC + 1024;
constexpr int L_WORDS = L_CS + 18 * 64;
constexpr int L_DUMMY = L_WORDS + 128;
constexpr int L_TOTAL = L_DUMMY + 512;
#define YREP 1
constexpr int P_STAGE = 30720, P_NY = 16 * YREP, P_ND = 7, P_NST = P_NY + P_ND;
static_assert(L_TOTAL <= 163840 && 5 * P_STAGE <= L_MISC, "LDS");

__device__ __forceinline__ unsigned get_tag(lptr lds) { return *(volatile LAS unsigned*)(lds + L_WORDS + 16); }

struct Args {
  const u16 *x, *Cr, *Ci, *Dr, *Di, *Wg, *Wu, *Wd;
  char* ws;
  u16* out;
};

__device__ __forceinline__ void launder(Args& a, int& xcd, int& idx) {
  asm volatile("" : "+s"(a.x), "+s"(a.Cr), "+s"(a.Ci), "+s"(a.Dr), "+s"(a.Di));
  asm volatile("" : "+s"(a.Wg), "+s"(a.Wu), "+s"(a.Wd), "+s"(a.ws), "+s"(a.out));
  asm volatile("" : "+s"(xcd), "+s"(idx));
}

struct RowJobs {
  u32x4 v[4];
  float f[2][8], sm[4];
  __device__ __forceinline__ void load(const Args& a, int nq, int idx, int mt, int grp, int h, int lane) {
    const int l56 = lane < 56 ? lane : 55;
    const u16* sp = a.x + (size_t)(mt * 128 + 8 * grp + 2 * h) * D + l56 * 8;
    const u16* dp = (nq >= 2 ? a.Di : a.Dr) + (size_t)(224 * (nq & 1) + 7 * idx + h) * D + l56 * 8;
    const u16* dq = dp + (h < 3 ? 4 * D : 0);
    asm volatile("global_load_dwordx4 %0, %4, off\n\tglobal_load_dwordx4 %1, %4, off offset:896\n\tglobal_load_dwordx4 %2, %5, off\n\tglobal_load_dwordx4 %3, %6, off"
                 : "=&v"(v[0]), "=&v"(v[1]), "=&v"(v[2]), "=&v"(v[3]) : "v"(sp), "v"(dp), "v"(dq) : "memory");
  }
  __device__ __forceinline__ void landed() { asm volatile("" : "+v"(v[0]), "+v"(v[1]), "+v"(v[2]), "+v"(v[3])); }
  __device__ __forceinline__ void sums(int lane) {
#pragma unroll
    for (int r = 0; r < 4; ++r) {
      float t[8];
#pragma unroll
      for (int i = 0; i < 4; ++i) { t[2 * i] = bf_lo(v[r][i]); t[2 * i + 1] = bf_hi(v[r][i]); }
      const float s = ((t[0] + t[1]) + (t[2] + t[3])) + ((t[4] + t[5]) + (t[6] + t[7]));
      sm[r] = lane < 56 ? s : 0.f;
      if (r < 2) {
#pragma unroll
        for (int i = 0; i < 8; ++i) f[r][i] = t[i];
      }
    }
    wave_sums<4>(sm);
  }
  __device__ __forceinline__ void finish(char* ws, unsigned tag, int nq, int idx, int mt, int grp, int h, int lane) {
    float q[2], mean[2];
#pragma unroll
    for (int r = 0; r < 2; ++r) {
      mean[r] = sm[r] * (1.0f / D);
      float t0 = 0.f, t1 = 0.f;
#pragma unroll
      for (int i = 0; i < 4; ++i) {
        const float d0 = f[r][2 * i] - mean[r], d1 = f[r][2 * i + 1] - mean[r];
        t0 = __builtin_fmaf(d0, d0, t0); t1 = __builtin_fmaf(d1, d1, t1);
      }
      q[r] = lane < 56 ? t0 + t1 : 0.f;
    }
    wave_sums<2>(q);
    const int r = lane & 1;
    const unsigned pay = pk_bf16(r ? mean[1] : mean[0], rbf(__builtin_amdgcn_sqrtf(rbf(rbf((r ? q[1] : q[0]) * (1.0f / D)) + 1e-5f))));
    if (lane < 2 || lane == 2 || (lane == 3 && h < 3)) {
      const unsigned v_ = lane < 2 ? pay : __float_as_uint(lane == 2 ? sm[2] : sm[3]);
      const size_t off = lane < 2 ? WS_ST + (size_t)(mt * 128 + 8 * grp + 2 * h + lane) * 8 : WS_CD + (size_t)(224 * nq + 7 * idx + h + 4 * (lane - 2)) * 8;
      __hip_atomic_store((GAS unsigned long long*)(ws + off), ((unsigned long long)tag << 32) | v_, __ATOMIC_RELAXED, __HIP_MEMORY_SCOPE_AGENT);
    }
  }
};

__device__ __forceinline__ void h_batch(const f32x4 (&g)[2][2], const f32x4 (&u)[2][2], const f32x4 (&cg)[2], const f32x4 (&cu)[2], const f32x2 (&st)[2],
                                        rsrc_t rh, int off0) {
  f32x2 gg[8], uu[8], z[8];
#pragma unroll
  for (int i = 0; i < 8; ++i) {
    const int t = i >> 2, j = (i >> 1) & 1, hh = i & 1;
    gg[i] = (f32x2{g[j][t][2 * hh], g[j][t][2 * hh + 1]} - f32x2{cg[j][2 * hh], cg[j][2 * hh + 1]} * st[t].x) * st[t].y;
  }
#pragma unroll
  for (int i = 0; i < 8; ++i) z[i] = gg[i] * ((gg[i] * gg[i]) * -0.1029432397f + -2.302208198f);
#pragma unroll
  for (int i = 0; i < 8; ++i) { z[i].x = __builtin_amdgcn_exp2f(z[i].x); z[i].y = __builtin_amdgcn_exp2f(z[i].y); }
#pragma unroll
  for (int i = 0; i < 8; ++i) {
    const int t = i >> 2, j = (i >> 1) & 1, hh = i & 1;
    uu[i] = ((f32x2{u[j][t][2 * hh], u[j][t][2 * hh + 1]} - f32x2{cu[j][2 * hh], cu[j][2 * hh + 1]} * st[t].x) * st[t].y) * gg[i];
    z[i] = z[i] + 1.0f;
  }
#pragma unroll
  for (int i = 0; i < 8; ++i) { z[i].x = __builtin_amdgcn_rcpf(z[i].x); z[i].y = __builtin_amdgcn_rcpf(z[i].y); }
#pragma unroll
  for (int i = 0; i < 8; ++i) uu[i] = uu[i] * z[i];
#pragma unroll
  for (int tl = 0; tl < 2; ++tl) {
    const u32x4 o = {pk_bf16(uu[4 * tl].x, uu[4 * tl].y), pk_bf16(uu[4 * tl + 1].x, uu[4 * tl + 1].y),
                     pk_bf16(uu[4 * tl + 2].x, uu[4 * tl + 2].y), pk_bf16(uu[4 * tl + 3].x, uu[4 * tl + 3].y)};
    __builtin_amdgcn_raw_buffer_store_b128(o, rh, off0 + tl * 56 * 1024, 0, STAUX);
  }
}
constexpr int L_HD = 61440;

template <int MODE, int NG, int NA>
__device__ __forceinline__ void p1_consumer(lptr lds, Args a, int xcd, int idx, int mt, int grp, int f0, int a0, int wm, int wn, int lane) {
  launder(a, xcd, idx);
  asm volatile("" : "+s"(mt), "+s"(grp), "+s"(f0), "+s"(a0));
  char* ws = a.ws;
  const char* cp = (const char*)((xcd >> 2) ? a.Ci : a.Cr) + (size_t)(1024 * (xcd & 1) + 32 * idx + 8 * (2 * wn + wm) + (lane >> 5)) * 4096 + 2048 * ((xcd >> 1) & 1) + (lane & 31) * 64;
  unsigned nonce = 0;
  if (MODE == 0 && wm == 0 && wn == 0) asm volatile("global_load_dword %0, %1, off sc1" : "+v"(nonce) : "v"(ws + WS_NONCE) : "memory");
  constexpr int NBK = 2 * NG, NCQ = NBK / 4, WSZ = NBK * 8 * 256, KSO = 4 * NCQ * 1024;
  constexpr int GO = K1_WO, UO = K1_WO + WSZ, DO = K1_WO + 2 * WSZ;
  const int i16 = lane & 15, g = lane >> 4, q = i16 >> 2, p = i16 & 3;
  const int rowx = 64 * wm + 8 * (i16 >> 2) + (i16 & 3);
  const int vx0 = rowx * 128 + ((g) ^ (i16 & 7)) * 16;
  const int vx1 = rowx * 128 + ((g + 4) ^ (i16 & 7)) * 16;
  const int fw = ((q >> 1) & 1) | ((g & 1) << 1);
  int vw[NG];
#pragma unroll
  for (int j = 0; j < NG; ++j) { const int nb = NG * wn + j; vw[j] = g * NCQ * 1024 + (nb >> 2) * 1024 + q * 128 + (((nb & 3) ^ fw) * 32) + p * 8; }
  const int vd0 = DO + (16 * NA * wn + i16) * 128 + ((g) ^ (i16 & 7)) * 16;
  const int vd1 = DO + (16 * NA * wn + i16) * 128 + ((g + 4) ^ (i16 & 7)) * 16;

  f32x4 G[NG][4], U[NG][4], AB[NA][4];
#pragma unroll
  for (int j = 0; j < NG; ++j)
#pragma unroll
    for (int t = 0; t < 4; ++t) { G[j][t] = f32x4{0, 0, 0, 0}; U[j][t] = f32x4{0, 0, 0, 0}; }
#pragma unroll
  for (int j = 0; j < NA; ++j)
#pragma unroll
    for (int t = 0; t < 4; ++t) AB[j][t] = f32x4{0, 0, 0, 0};

  bf16x8 X0[4], X1[4], WG[NG], WU[NG], WD[NA];
  auto ldx = [&](bf16x8 (&X)[4], lptr st, int ks) {
#pragma unroll
    for (int t = 0; t < 4; ++t) X[t] = ldsr128(st + (ks ? vx1 : vx0) + (32 * (t >> 1) + 4 * (t & 1)) * 128);
  };
  auto ldg = [&](int j, lptr st, int ks) { lptr b = st + GO + ks * KSO + vw[j]; WG[j] = ldstr(b, b + 512); };
  auto ldu = [&](int j, lptr st, int ks) { lptr b = st + UO + ks * KSO + vw[j]; WU[j] = ldstr(b, b + 512); };
  auto ldd = [&](int j, lptr st, int ks) { WD[j] = ldsr128(st + (ks ? vd1 : vd0) + j * 16 * 128); };
  auto grp4 = [&](f32x4 (&C)[4], const bf16x8& W, bf16x8 (&X)[4], bool wfirst, auto mid) {
    if (wfirst) { mfma_ip(C[0], W, X[0]); mfma_ip(C[1], W, X[1]); } else { mfma_ip(C[0], X[0], W); mfma_ip(C[1], X[1], W); }
    SBAR(); mid(); SBAR();
    if (wfirst) { mfma_ip(C[2], W, X[2]); mfma_ip(C[3], W, X[3]); } else { mfma_ip(C[2], X[2], W); mfma_ip(C[3], X[3], W); }
    SBAR();
  };
  auto fill = [&]() { FILL(); };
  auto ldx1 = [&](bf16x8 (&X)[4], lptr st, int ks, int t) { X[t] = ldsr128(st + (ks ? vx1 : vx0) + (32 * (t >> 1) + 4 * (t & 1)) * 128); };
  auto blockA = [&](lptr st) {
#pragma unroll
    for (int j = 0; j < NG; ++j) {
      grp4(G[j], WG[j], X0, true, [&]() { if (2 * j < 4) ldx1(X1, st, 1, 2 * j); else FILL(); }); ldg(j, st, 1); SBAR();
      grp4(U[j], WU[j], X0, true, [&]() { if (2 * j + 1 < 4) ldx1(X1, st, 1, 2 * j + 1); else FILL(); }); ldu(j, st, 1); SBAR();
    }
#pragma unroll
    for (int j = 0; j < NA; ++j) { grp4(AB[j], WD[j], X0, false, fill); ldd(j, st, 1); SBAR(); }
  };
  auto blockB_head = [&]() { grp4(G[0], WG[0], X1, true, fill); FILL(); grp4(U[0], WU[0], X1, true, fill); };
  auto blockB_rest = [&](lptr sn, bool reload) {
    int k = 0;
    auto mid = [&]() {
      if (!reload) FILL();
      else if (k == 0) ldg(0, sn, 0);
      else if (k == 1) ldu(0, sn, 0);
      else if (k < 6) ldx1(X0, sn, 0, k - 2);
      else FILL();
      ++k;
    };
#pragma unroll
    for (int j = 1; j < NG; ++j) {
      grp4(G[j], WG[j], X1, true, mid); if (reload) { ldg(j, sn, 0); SBAR(); } else FILL();
      grp4(U[j], WU[j], X1, true, mid); if (reload) { ldu(j, sn, 0); SBAR(); } else FILL();
    }
#pragma unroll
    for (int j = 0; j < NA; ++j) { grp4(AB[j], WD[j], X1, false, mid); if (reload) { ldd(j, sn, 0); SBAR(); } else FILL(); }
  };

#pragma unroll
  for (int j = 0; j < NG; ++j)
#pragma unroll
    for (int t = 0; t < 4; ++t) { asm volatile("" : "+v"(G[j][t])); asm volatile("" : "+v"(U[j][t])); }
#pragma unroll
  for (int j = 0; j < NA; ++j)
#pragma unroll
    for (int t = 0; t < 4; ++t) asm volatile("" : "+v"(AB[j][t]));
  PAD_MFMA_IN();
  STAMP(1);
  BAR();
  STAMP(2);
  ldg(0, lds, 0); ldu(0, lds, 0); ldx(X0, lds, 0);
#pragma unroll
  for (int j = 1; j < NG; ++j) { ldg(j, lds, 0); ldu(j, lds, 0); }
#pragma unroll
  for (int j = 0; j < NA; ++j) ldd(j, lds, 0);
  SBAR();
  int slot = 0;
#pragma unroll 1
  for (int s = 0; s < K1_NST; ++s) {
    blockA(lds + slot * K1_STAGE);
    if (s >= PF0 && s < PF0 + 4) { pf4(cp, lds_addr(lds) + L_DUMMY); cp += 2 * 4096; }
    blockB_head();
    if (s == 0 && wm == 0 && wn == 0) {
      if (PF0 == 0) { WAITV(1); } else { WAITV(0); }
      asm volatile("" : "+v"(nonce));
      if (lane == 0) *(volatile LAS unsigned*)(lds + L_WORDS + 16) = (MODE == 0) ? nonce * 0x9E3779B1u + 0x7F4A7C15u : 0x51u;
    }
    WAITL0();
    STAMP(19 + s);
    BAR();
    STAMP(7 + s);
    slot = (slot == 2) ? 0 : slot + 1;
    blockB_rest(lds + slot * K1_STAGE, true);
  }
  PAD_MFMA_OUT();
  STAMP(3);
  const lptr cs = lds + L_CS;
  const lptr stats = lds + L_STATS;
  {
    const rsrc_t rab = mk_rsrc(ws + WS_AB, 2 * 28 * 64 * 1024);
#pragma unroll
    for (int pi = 0; pi < 2; ++pi) {
      const lptr sp = stats + (64 * wm + 32 * pi + 8 * g) * 8;
      const f32x4 s0 = *(LAS f32x4*)(sp), s1 = *(LAS f32x4*)(sp + 16), s2 = *(LAS f32x4*)(sp + 32), s3 = *(LAS f32x4*)(sp + 48);
#pragma unroll
      for (int j = 0; j < NA; ++j) {
        const float c = *(LAS float*)(cs + (2 * NBK + NA * wn + j) * 64 + i16 * 4);
        const f32x4 a_ = AB[j][2 * pi], b = AB[j][2 * pi + 1];
        const int ks = mt * 4 + wm * 2 + pi;
        u32x4 o;
        o[0] = pk_bf16((a_[0] - s0[0] * c) * s0[1], (a_[1] - s0[2] * c) * s0[3]);
        o[1] = pk_bf16((a_[2] - s1[0] * c) * s1[1], (a_[3] - s1[2] * c) * s1[3]);
        o[2] = pk_bf16((b[0] - s2[0] * c) * s2[1], (b[1] - s2[2] * c) * s2[3]);
        o[3] = pk_bf16((b[2] - s3[0] * c) * s3[1], (b[3] - s3[2] * c) * s3[3]);
        __builtin_amdgcn_raw_buffer_store_b128(o, rab, lane * 16 + ((a0 + NA * wn + j) * 64 + ks) * 1024, 0, STAUX);
      }
    }
  }
  {
    lptr hd = lds + L_HD + (2 * wn + wm) * (NG * 4096) + lane * 16;
#pragma unroll
    for (int j = 0; j < NG; ++j)
#pragma unroll
      for (int tl = 0; tl < 2; ++tl) {
        *(LAS f32x4*)(hd + ((j * 2 + 0) * 2 + tl) * 1024) = G[j][2 + tl];
        *(LAS f32x4*)(hd + ((j * 2 + 1) * 2 + tl) * 1024) = U[j][2 + tl];
      }
  }
  WAITV(0);
  if (lane == 0) {
    const unsigned n = __hip_atomic_fetch_add((LAS unsigned*)(lds + L_WORDS), 1u, __ATOMIC_RELAXED, __HIP_MEMORY_SCOPE_WORKGROUP);
    if (n == 3u) st_flag(ws + WS_FAB + (mt * 16 + grp) * 64, get_tag(lds));
  }
  STAMP(5);
  WAITL0(); BAR();
  STAMP(14);
  const rsrc_t rh = mk_rsrc(ws + WS_H, 128 * 56 * 1024);
  const int tt0 = mt * 8 + wm * 4;
  f32x2 st[2];
#pragma unroll
  for (int t = 0; t < 2; ++t) st[t] = *(LAS f32x2*)(stats + (64 * wm + 8 * (i16 >> 2) + 4 * t + (i16 & 3)) * 8);
#pragma unroll
  for (int pp = 0; pp < NG / 2; ++pp) {
    const int ks = ((f0 >> 4) + NG * wn + 2 * pp) >> 1;
    f32x4 cg[2], cu[2], gq[2][2], uq[2][2];
#pragma unroll
    for (int j = 0; j < 2; ++j) {
      cg[j] = *(LAS f32x4*)(cs + (NG * wn + 2 * pp + j) * 64 + g * 16);
      cu[j] = *(LAS f32x4*)(cs + (NBK + NG * wn + 2 * pp + j) * 64 + g * 16);
      gq[j][0] = G[2 * pp + j][0]; gq[j][1] = G[2 * pp + j][1]; uq[j][0] = U[2 * pp + j][0]; uq[j][1] = U[2 * pp + j][1];
    }
    h_batch(gq, uq, cg, cu, st, rh, lane * 16 + (tt0 * 56 + ks) * 1024);
  }
  STAMP(4);
}

template <int NG>
__device__ __forceinline__ void p1_helper_epilogue(lptr lds, char* ws, int mt, int f0, int h, int lane) {
  constexpr int NBK = 2 * NG;
  const int wm = h & 1, wn = h >> 1, i16 = lane & 15, g = lane >> 4;
  const lptr cs = lds + L_CS, stats = lds + L_STATS;
  const rsrc_t rh = mk_rsrc(ws + WS_H, 128 * 56 * 1024);
  const int tt0 = mt * 8 + wm * 4 + 2;
  lptr hd = lds + L_HD + h * (NG * 4096) + lane * 16;
  f32x2 st[2];
#pragma unroll
  for (int t = 0; t < 2; ++t) st[t] = *(LAS f32x2*)(stats + (64 * wm + 32 + 8 * (i16 >> 2) + 4 * t + (i16 & 3)) * 8);
#pragma unroll
  for (int pp = 0; pp < NG / 2; ++pp) {
    const int ks = ((f0 >> 4) + NG * wn + 2 * pp) >> 1;
    f32x4 cg[2], cu[2], gq[2][2], uq[2][2];
#pragma unroll
    for (int j = 0; j < 2; ++j) {
      cg[j] = *(LAS f32x4*)(cs + (NG * wn + 2 * pp + j) * 64 + g * 16);
      cu[j] = *(LAS f32x4*)(cs + (NBK + NG * wn + 2 * pp + j) * 64 + g * 16);
#pragma unroll
      for (int tl = 0; tl < 2; ++tl) {
        gq[j][tl] = *(LAS f32x4*)(hd + (((2 * pp + j) * 2 + 0) * 2 + tl) * 1024);
        uq[j][tl] = *(LAS f32x4*)(hd + (((2 * pp + j) * 2 + 1) * 2 + tl) * 1024);
      }
    }
    h_batch(gq, uq, cg, cu, st, rh, lane * 16 + (tt0 * 56 + ks) * 1024);
  }
}

template <int MODE, bool TB>
__device__ __forceinline__ void p1_helper(lptr lds, Args a, int xcd, int idx, int mt, int grp, int f0, int a0, int h, int lane) {
  constexpr int NBK = TB ? 4 : 8, WSZ = NBK * 8 * 256;
  constexpr int NWP = TB ? 4 : 8, NDP = TB ? 4 : 1, PER = 4 + NWP + NDP;
  const int mh = xcd & 1, nq = xcd >> 1;
  const int up = h >> 1;
  const u32x4 rx = mk_srd(a.x, S * D * 2);
  const u32x4 rw = mk_srd(up ? a.Wu : a.Wg, D * F * 2);
  const u32x4 rd = mk_srd(a0 >= 28 ? a.Di : a.Dr, D * D * 2);
  const int e0 = (a0 >= 28 ? a0 - 28 : a0) * 16;
  const int r8 = lane >> 3, c8 = lane & 7;
  int vx[2];
  vx[0] = r8 * 896 + (c8 ^ (r8 & 3)) * 16;
  vx[1] = r8 * 896 + (c8 ^ ((r8 & 3) | 4)) * 16;
  const int vd = r8 * 896 + (c8 ^ r8) * 16;
  int vw[2];
  {
    const int f0_ = (r8 >> 1) & 1;
    vw[0] = r8 * (F * 2) + f0 * 2 + ((((c8 >> 1) ^ f0_) * 2) + (c8 & 1)) * 16;
    vw[1] = r8 * (F * 2) + f0 * 2 + ((((c8 >> 1) ^ (f0_ | 2)) * 2) + (c8 & 1)) * 16;
  }
  const unsigned l0 = lds_addr(lds);
  const unsigned dummy = l0 + L_DUMMY + (h & 1) * 256;
  const int sxb = (mt * 128 + 32 * h) * 896, swb = 32 * (h & 1) * (F * 2), sdb = (e0 + 8 * NDP * h) * 896;
  const unsigned lxb = l0 + 4 * h * 1024, lwb = l0 + K1_WO + up * WSZ + NWP * (h & 1) * 1024, ldb = l0 + K1_WO + 2 * WSZ + NDP * h * 1024;
  auto piece = [&](int s, int slot, int i) {
    const unsigned so = slot * K1_STAGE;
    if (i < 4) dma16(rx, lxb + so + i * 1024, vx[i & 1], sxb + s * 128 + i * 8 * 896);
    else if (i < 4 + NWP) {
      const int j = i - 4, kb = TB ? j : j >> 1, cq = TB ? 0 : j & 1;
      dma16(rw, lwb + so + j * 1024, vw[kb & 1], swb + s * 64 * (F * 2) + kb * 8 * (F * 2) + cq * 128);
    } else dma16(rd, ldb + so + (i - 4 - NWP) * 1024, vd, sdb + s * 128 + (i - 4 - NWP) * 8 * 896);
  };
  auto issue_a = [&](int s, int slot) {
#pragma unroll
    for (int i = 0; i < 8; ++i) piece(s, slot, i);
  };
  auto issue_b = [&](int s, int slot) {
#pragma unroll
    for (int i = 8; i < PER; ++i) piece(s, slot, i);
  };
  const int ln2 = lane >> 1, hb = (lane & 1) * 64;
  const char* pfx = (const char*)a.x + (size_t)(1024 * mh + 32 * idx + ln2) * 896 + hb;
  STAMP(1);
  issue_a(0, 0); issue_b(0, 0);
  STAMP(5);
  launder(a, xcd, idx);
  char* ws = a.ws;
  const bool wjob = idx < 28;
  const int wj_mat = idx >= 14, wj_cb = (idx - 14 * wj_mat) >> 1, wj_kh = idx & 1;
  u32x4 wx[7];
  {
    const u32x4 rj = mk_srd(wj_mat ? a.Wu : a.Wg, D * F * 2);
    const int vj = r8 * (F * 2) + c8 * 16 + (448 * nq + 64 * (wjob ? wj_cb : 0)) * 2;
    const int sj = (224 * (wjob ? wj_kh : 0) + 56 * h) * (F * 2);
    asm volatile("buffer_load_dwordx4 %0, %7, %8, %9 offen\n\tbuffer_load_dwordx4 %1, %7, %8, %10 offen\n\tbuffer_load_dwordx4 %2, %7, %8, %11 offen\n\t"
                 "buffer_load_dwordx4 %3, %7, %8, %12 offen\n\tbuffer_load_dwordx4 %4, %7, %8, %13 offen\n\tbuffer_load_dwordx4 %5, %7, %8, %14 offen\n\t"
                 "buffer_load_dwordx4 %6, %7, %8, %15 offen"
                 : "=&v"(wx[0]), "=&v"(wx[1]), "=&v"(wx[2]), "=&v"(wx[3]), "=&v"(wx[4]), "=&v"(wx[5]), "=&v"(wx[6])
                 : "v"(vj), "s"(rj), "s"(sj), "s"(sj + 8 * F * 2), "s"(sj + 16 * F * 2), "s"(sj + 24 * F * 2), "s"(sj + 32 * F * 2), "s"(sj + 40 * F * 2), "s"(sj + 48 * F * 2)
                 : "memory");
  }
  RowJobs rj_;
  rj_.load(a, nq, idx, mt, grp, h, lane);
  pf4(pfx + (1 + h) * 128, dummy);
  if (h == 3 && lane < 4) ((LAS unsigned*)(lds + L_WORDS))[lane] = 0u;
  WAITL0(); WAITV(12);
  STAMP(7); BAR();
  STAMP(2);
#pragma unroll
  for (int i = 0; i < PER; ++i) piece(1, 1, i);
  issue_a(2, 2);
  if (h < 2) pf4(pfx + (5 + h) * 128, dummy);
  WAITL0();
  if (h < 2) { WAITV(9); } else { WAITV(8); }
  asm volatile("" : "+v"(wx[0]), "+v"(wx[1]), "+v"(wx[2]), "+v"(wx[3]), "+v"(wx[4]), "+v"(wx[5]), "+v"(wx[6]));
  rj_.landed();
  BAR();
  const unsigned tag1 = get_tag(lds);
  float cw[8];
#pragma unroll
  for (int k = 0; k < 8; ++k) cw[k] = 0.f;
  constexpr bool TBc = TB;
  unsigned long long g0 = 0, g1 = 0, g2 = 0, g3 = 0;
  const bool wdjob = blockIdx.x < 196;
  const int wd_kp = blockIdx.x % 28, wd_cg = blockIdx.x / 28;
#pragma unroll
  for (int s = 1; s < K1_NST; ++s) {
    const int s1_ = (s + 1) % 3, s2_ = (s + 2) % 3;
    if (s == 1) {
#pragma unroll
      for (int i = 0; i < PER; ++i) {
        if (i < PER - 8) piece(2, s1_, 8 + i); else piece(3, s2_, i - (PER - 8));
        SBAR();
        if (i < 7) {
#pragma unroll
          for (int k = 0; k < 4; ++k) { cw[2 * k] += bf_lo(wx[i][k]); cw[2 * k + 1] += bf_hi(wx[i][k]); }
        }
        SBAR();
      }
    } else {
      if (s + 1 < K1_NST) issue_b(s + 1, s1_);

      if (s + 2 < K1_NST) issue_a(s + 2, s2_);
    }
    if (s == 1) {
      pf4((const char*)a.Wd + (size_t)(64 * wd_kp + 16 * h + ((lane >> 1) & 15)) * 896 + (wdjob ? wd_cg : 0) * 128 + (lane & 1) * 64, dummy);
#pragma unroll
      for (int k = 0; k < 8; ++k) {
        cw[k] += __builtin_bit_cast(float, __builtin_amdgcn_update_dpp(0, __builtin_bit_cast(int, cw[k]), 0x128, 0xf, 0xf, true));
        const auto p16 = __builtin_amdgcn_permlane16_swap(__float_as_uint(cw[k]), __float_as_uint(cw[k]), false, false);
        cw[k] = __uint_as_float(p16[0]) + __uint_as_float(p16[1]);
        const auto p32 = __builtin_amdgcn_permlane32_swap(__float_as_uint(cw[k]), __float_as_uint(cw[k]), false, false);
        cw[k] = __uint_as_float(p32[0]) + __uint_as_float(p32[1]);
      }
      if (lane < 8) {
        *(LAS f32x4*)(lds + L_CS + h * 256 + lane * 32) = f32x4{cw[0], cw[1], cw[2], cw[3]};
        *(LAS f32x4*)(lds + L_CS + h * 256 + lane * 32 + 16) = f32x4{cw[4], cw[5], cw[6], cw[7]};
      }
    }
    if (s == 2) rj_.sums(lane);
    if (s == 3) rj_.finish(ws, tag1, nq, idx, mt, grp, h, lane);
    if (s == 2 && h == 0 && wjob) {
      const float t = (*(LAS float*)(lds + L_CS + lane * 4) + *(LAS float*)(lds + L_CS + 256 + lane * 4)) +
                      (*(LAS float*)(lds + L_CS + 512 + lane * 4) + *(LAS float*)(lds + L_CS + 768 + lane * 4));
      __hip_atomic_store((GAS unsigned long long*)(ws + WS_CW + (size_t)((wj_mat * 2 + wj_kh) * F + 448 * nq + 64 * wj_cb + lane) * 8),
                         ((unsigned long long)tag1 << 32) | __float_as_uint(t), __ATOMIC_RELAXED, __HIP_MEMORY_SCOPE_AGENT);
    }
    if (s == 4 && h == 0 && lane == 0) st_flag(ws + WS_ARRIVE + blockIdx.x * 64, tag1);
    if (s == 5) {
      if (wdjob) {
        const u32x4 rwd = mk_srd(a.Wd, F * D * 2);
        const int rs = (lane & 15) >> 1, half = lane & 1, b = lane >> 4;
        const int vq = rs * 896 + (64 * wd_cg + 16 * b + 8 * half) * 2;
#pragma unroll
        for (int i = 0; i < 2; ++i) dma16(rwd, l0 + s2_ * K1_STAGE + (2 * h + i) * 1024, vq, (64 * wd_kp + 8 * (2 * h + i)) * 896);
      }
    }
    const int gc = 64 * h + lane;
    constexpr int NWC = TBc ? 128 : 256, CPM = NWC / 2;
    const bool isw = gc < NWC;
    const char* p_st = ws + WS_ST + (size_t)(mt * 128 + 32 * h + (lane & 31)) * 8;
    const char* p_c0 = ws + (isw ? WS_CW + (size_t)((gc / CPM) * 2 * F + f0 + gc % CPM) * 8 : WS_CD + (size_t)(a0 * 16 + (gc - NWC)) * 8);
    const char* p_c1 = isw ? p_c0 + (size_t)F * 8 : p_c0;
    const char* p_d = ws + WS_CD + (size_t)(a0 * 16 + (lane & 31)) * 8;
    if (s == 5)
      asm volatile("global_load_dwordx2 %0, %4, off sc1\n\tglobal_load_dwordx2 %1, %5, off sc1\n\tglobal_load_dwordx2 %2, %6, off sc1\n\tglobal_load_dwordx2 %3, %7, off sc1"
                   : "=&v"(g0), "=&v"(g1), "=&v"(g2), "=&v"(g3) : "v"(p_st), "v"(p_c0), "v"(p_c1), "v"(p_d) : "memory");
    if (s == 6) {
      WAITV(DFTPF == 2 ? 2 : 0);
      asm volatile("" : "+v"(g0), "+v"(g1), "+v"(g2), "+v"(g3));
      auto ld_gran = [&](const char* p) { return __hip_atomic_load((const GAS unsigned long long*)p, __ATOMIC_RELAXED, __HIP_MEMORY_SCOPE_AGENT); };
      for (unsigned sp = 0; sp < SPIN_MAX; ++sp) {
        if (__all((unsigned)(g0 >> 32) == tag1 && (unsigned)(g1 >> 32) == tag1 && (unsigned)(g2 >> 32) == tag1 && (unsigned)(g3 >> 32) == tag1)) break;
        __builtin_amdgcn_s_sleep(2);
        g0 = ld_gran(p_st); g1 = ld_gran(p_c0); g2 = ld_gran(p_c1); g3 = ld_gran(p_d);
      }
      if (lane < 32) *(LAS f32x2*)(lds + L_STATS + (32 * h + lane) * 8) = f32x2{bf_lo((unsigned)g0), 1.0f / bf_hi((unsigned)g0)};
      *(LAS float*)(lds + L_CS + gc * 4) = isw ? __uint_as_float((unsigned)g1) + __uint_as_float((unsigned)g2) : __uint_as_float((unsigned)g1);
      if (!TBc && h == 0 && lane < 32) *(LAS float*)(lds + L_CS + (256 + lane) * 4) = __uint_as_float((unsigned)g3);
    }
    WAITL0();
    if (s == 1) { WAITV(9); } else if (s + 2 < K1_NST) { WAITV(8); } else if (s == 5) { if (wdjob) { WAITV(6 + (DFTPF == 2 ? 2 : 0)); } else { WAITV(4 + (DFTPF == 2 ? 2 : 0)); } }
    STAMP(7 + s);
    BAR();
  }
  __builtin_amdgcn_s_setprio(0);
  if (h == 3 && wdjob) {
    const int i16 = lane & 15, g = lane >> 4, q = i16 >> 2, p = i16 & 3;
    const rsrc_t rt = mk_rsrc(ws + WS_WDT, 28 * 56 * 1024);
    lptr b0 = lds + 1 * K1_STAGE + (g >> 1) * 1024 + ((g & 1) * 4 + q) * 32 + p * 8;
#pragma unroll
    for (int kk = 0; kk < 2; ++kk)
#pragma unroll
      for (int u = 0; u < 4; ++u) {
        const bf16x8 f = ldstr(b0 + kk * 4096 + u * 256, b0 + kk * 4096 + u * 256 + 2048);
        __builtin_amdgcn_raw_buffer_store_b128(__builtin_bit_cast(u32x4, f), rt, lane * 16 + ((4 * wd_cg + u) * 56 + 2 * wd_kp + kk) * 1024, 0, STAUX);
      }
    WAITL0();
  }
  BAR();
  p1_helper_epilogue<TB ? 2 : 4>(lds, ws, mt, f0, h, lane);
}

template <int MODE, int NB, int JO>
__device__ __forceinline__ void p2_loader(lptr lds, const Args& a, int xcd, int idx, int r, int m, int n, int lw, int lane) {
  char* ws = a.ws;
  constexpr int PER = 4 + NB;
  const u32x4 rc = mk_srd((r >> 1) ? a.Ci : a.Cr, S * S * 2);
  const u32x4 rb = mk_srd(ws + WS_AB, 2 * 28 * 64 * 1024);
  const u32x4 rh = mk_srd(ws + WS_H, 128 * 56 * 1024);
  const u32x4 rw = mk_srd(ws + WS_WDT, 28 * 56 * 1024);
  const int r8 = lane >> 3, c8 = lane & 7;
  int va[2];
  va[0] = r8 * 4096 + (c8 ^ (r8 & 3)) * 16;
  va[1] = r8 * 4096 + (c8 ^ ((r8 & 3) | 4)) * 16;
  const int vl = lane * 16;
  const int k0 = 1024 * (r & 1), p = r >> 1;
  const int j0 = (lw < 2) ? 4 * lw : 8 + 3 * (lw - 2);
  const unsigned l0 = lds_addr(lds);
  const unsigned dummy = l0 + L_DUMMY + (lw & 1) * 256;
  const int sab = (128 * m + 32 * lw) * 4096 + k0 * 2;
  const int shb = ((8 * m + 2 * lw) * 56 + 14 * r) * 1024;
  const unsigned lab = l0 + 4 * lw * 1024, lbb = l0 + 16384 + j0 * 1024;
  const int sbb = ((p * 28 + 7 * n + (j0 >> 1)) * 64 + (k0 >> 5)) * 1024, swb = ((7 * n + (j0 >> 1)) * 56 + 14 * r) * 1024;
  auto issueAy = [&](int s, int slot) {
    const unsigned so = slot * P_STAGE; const int b = sab + 128 * (s & 15);
#pragma unroll
    for (int i = 0; i < 4; ++i) dma16(rc, lab + so + i * 1024, va[i & 1], b + i * 8 * 4096);
  };
  auto issueAd = [&](int sd, int slot) {
    const unsigned so = slot * P_STAGE; const int b = shb + 2048 * sd;
#pragma unroll
    for (int i = 0; i < 4; ++i) dma16c(rh, lab + so + i * 1024, vl, b + (i >> 1) * 56 * 1024 + (i & 1) * 1024);
  };
  auto issueBy = [&](int s, int slot) {
    const unsigned so = slot * P_STAGE; const int b = sbb + 2048 * (s & 15);
#pragma unroll
    for (int j = 0; j < NB; ++j) dma16c(rb, lbb + so + j * 1024, vl, b + ((j + JO) >> 1) * 65536 + ((j + JO) & 1) * 1024);
  };
  auto issueBd = [&](int sd, int slot) {
    const unsigned so = slot * P_STAGE; const int b = swb + 2048 * sd;
#pragma unroll
    for (int j = 0; j < NB; ++j) dma16c(rw, lbb + so + j * 1024, vl, b + ((j + JO) >> 1) * 57344 + ((j + JO) & 1) * 1024);
  };
  __builtin_amdgcn_s_setprio(0);
  const unsigned tag = get_tag(lds);
  if (MODE != 2 && lw == 3 && blockIdx.x < 196) {
    WAITV(0);
    if (lane == 0) st_flag(ws + WS_FWD + blockIdx.x * 64, tag);
  }
  issueAy(0, 0); issueAy(1, 1);
  if (lw == 3) {
    const int mtp = 8 * (r & 1) + (lane & 7), j = (lane >> 3) & 3;
    const bool act = lane < 32 && ((n & 1) == 0 || j == 3);
    poll_all(ws + WS_FAB + (mtp * 16 + (2 * p + (n >> 1)) * 4 + j) * 64, act, tag);
    if (lane == 0) *(volatile LAS unsigned*)(lds + L_WORDS + 8) = 1u;
  } else lds_spin(lds + L_WORDS + 8);
  STAMP(16);
  issueBy(0, 0);
  {
    const int l = (lane >> 1) < 28 ? (lane >> 1) : 27;
    const int sl = l >= 14, li = 14 * idx + l - 14 * sl;
    const char* bp = ws + WS_AB + ((size_t)(p * 28 + (li >> 4)) * 64 + 32 * (r & 1) + 2 * (4 * lw + sl) + ((li >> 3) & 1)) * 1024 + (li & 7) * 128 + (lane & 1) * 64;
    pf4c(bp, dummy); pf4c(bp + 4 * 1024, dummy);
  }
  issueBy(1, 1);
  __builtin_amdgcn_s_setprio(3);
  WAITV(NB + 2); BAR();
  if (MODE == 0 && lane == 0) {
    const unsigned cnt = __hip_atomic_fetch_add((LAS unsigned*)(lds + L_WORDS + 4), 1u, __ATOMIC_RELAXED, __HIP_MEMORY_SCOPE_WORKGROUP);
    if (cnt == 7u) st_flag(ws + WS_FH + (((xcd & 1) * 8 + (idx & 7)) * 16 + (xcd >> 1) * 4 + (idx >> 3)) * 64, tag);
  }
  issueAy(2, 2); issueBy(2, 2);
  WAITV(PER); BAR();
  issueAy(3, 3); issueBy(3, 3); issueAy(4, 4); issueBy(4, 4);
  WAITV(2 * PER); BAR();
  int slot = 0;
  auto ystep = [&](int s) { issueAy(s + 3, slot); issueBy(s + 3, slot); slot = (slot == 4) ? 0 : slot + 1; WAITV(2 * PER); BAR(); };
  auto dstep = [&](int sd) { issueAd(sd, slot); issueBd(sd, slot); slot = (slot == 4) ? 0 : slot + 1; WAITV(2 * PER); BAR(); };
#pragma unroll 1
  for (int s = 2; s < P_NY - 9; ++s) ystep(s);
  const int cg0 = (112 * n) >> 6, cg1 = (112 * n + 111) >> 6, jj = lane - 4;
  const bool fact = lane < 4 || (jj < 21 && cg0 + jj / 7 <= cg1);
  const char* fp = lane < 4 ? ws + WS_FH + (m * 16 + 4 * r + lane) * 64 : ws + WS_FWD + ((fact ? cg0 + jj / 7 : cg0) * 28 + 7 * r + (fact ? jj % 7 : 0)) * 64;
  unsigned fval = 0;
  if (lw == 3) asm volatile("global_load_dword %0, %1, off sc1" : "+v"(fval) : "v"(fp) : "memory");
#pragma unroll 1
  for (int s = P_NY - 9; s < P_NY - 6; ++s) ystep(s);
  if (lw == 3) {
    asm volatile("" : "+v"(fval));
    if (!__all(!fact || fval == tag)) poll_all(fp, fact, tag);
    if (lane == 0) *(volatile LAS unsigned*)(lds + L_WORDS + 12) = 1u;
  }
#pragma unroll 1
  for (int s = P_NY - 6; s < P_NY - 3; ++s) ystep(s);
  if (lw != 3) lds_spin(lds + L_WORDS + 12);
#pragma unroll 1
  for (int sd = 0; sd < P_ND; ++sd) dstep(sd);
  WAITV(PER); BAR();
  WAITV(0); BAR();
}

template <int MODE>
__global__ __launch_bounds__(512, 2) void fused_kernel(Args a) {
  extern __shared__ __attribute__((aligned(1024))) char smem[];
  lptr lds = (lptr)smem;
  char* ws = a.ws;
  const int tid = threadIdx.x, lane = tid & 63;
  const int wid = __builtin_amdgcn_readfirstlane(tid >> 6);
  int xcd = blockIdx.x & 7, idx = blockIdx.x >> 3;
  STAMP(0); STAMPR(30);
  const unsigned long long t0_ = __builtin_amdgcn_s_memtime();
  { const size_t n_ = (size_t)1835008 / 64; for (size_t i_ = (size_t)blockIdx.x * 512 + tid; i_ < n_; i_ += 131072) { unsigned v_ = *(const volatile unsigned*)((const char*)(a.x) + i_ * 64); asm volatile("" :: "v"(v_)); } }
  { const size_t n_ = (size_t)8388608 / 64; for (size_t i_ = (size_t)blockIdx.x * 512 + tid; i_ < n_; i_ += 131072) { unsigned v_ = *(const volatile unsigned*)((const char*)(a.Cr) + i_ * 64); asm volatile("" :: "v"(v_)); } }
  { const size_t n_ = (size_t)8388608 / 64; for (size_t i_ = (size_t)blockIdx.x * 512 + tid; i_ < n_; i_ += 131072) { unsigned v_ = *(const volatile unsigned*)((const char*)(a.Ci) + i_ * 64); asm volatile("" :: "v"(v_)); } }
  { const size_t n_ = (size_t)401408 / 64; for (size_t i_ = (size_t)blockIdx.x * 512 + tid; i_ < n_; i_ += 131072) { unsigned v_ = *(const volatile unsigned*)((const char*)(a.Dr) + i_ * 64); asm volatile("" :: "v"(v_)); } }
  { const size_t n_ = (size_t)401408 / 64; for (size_t i_ = (size_t)blockIdx.x * 512 + tid; i_ < n_; i_ += 131072) { unsigned v_ = *(const volatile unsigned*)((const char*)(a.Di) + i_ * 64); asm volatile("" :: "v"(v_)); } }
  { const size_t n_ = (size_t)1605632 / 64; for (size_t i_ = (size_t)blockIdx.x * 512 + tid; i_ < n_; i_ += 131072) { unsigned v_ = *(const volatile unsigned*)((const char*)(a.Wg) + i_ * 64); asm volatile("" :: "v"(v_)); } }
  { const size_t n_ = (size_t)1605632 / 64; for (size_t i_ = (size_t)blockIdx.x * 512 + tid; i_ < n_; i_ += 131072) { unsigned v_ = *(const volatile unsigned*)((const char*)(a.Wu) + i_ * 64); asm volatile("" :: "v"(v_)); } }
  { const size_t n_ = (size_t)1605632 / 64; for (size_t i_ = (size_t)blockIdx.x * 512 + tid; i_ < n_; i_ += 131072) { unsigned v_ = *(const volatile unsigned*)((const char*)(a.Wd) + i_ * 64); asm volatile("" :: "v"(v_)); } }
  { const size_t n_ = (size_t)27623424 / 64; for (size_t i_ = (size_t)blockIdx.x * 512 + tid; i_ < n_; i_ += 131072) { unsigned v_ = *(const volatile unsigned*)((const char*)(ws) + i_ * 64); asm volatile("" :: "v"(v_)); } }
  asm volatile("s_waitcnt vmcnt(0)" ::: "memory");
  while (__builtin_amdgcn_s_memtime() - t0_ < 100000ull) { asm volatile("v_nop\n\tv_nop\n\tv_nop\n\tv_nop"); }
  if (wid == 3) {
    unsigned long long pc;
    asm volatile("s_getpc_b64 %0" : "=s"(pc));
    const unsigned ln = 13 * idx + ((lane >> 1) < 13 ? (lane >> 1) : 12);
    pf4((const char*)(pc & ~127ull) + (ln * 128 < CODE_PF ? ln * 128 : 0) + (lane & 1) * 64, lds_addr(lds) + L_DUMMY);
  }
  if (wid >= 4) __builtin_amdgcn_s_setprio(3);

  if (MODE != 2) {
    const int nq = xcd >> 1;
    const int mt = (xcd & 1) * 8 + (idx & 7), j = idx >> 3;
    const bool tb = j == 3;
    const int grp = nq * 4 + j;
    const int f0 = 448 * nq + 128 * j, a0 = 14 * nq + 2 * j;
    if (wid < 4) {
      if (tb) p1_consumer<MODE, 2, 4>(lds, a, xcd, idx, mt, grp, f0, a0, wid & 1, wid >> 1, lane);
      else p1_consumer<MODE, 4, 1>(lds, a, xcd, idx, mt, grp, f0, a0, wid & 1, wid >> 1, lane);
    } else {
      const int h = wid - 4;
      if (tb) p1_helper<MODE, true>(lds, a, xcd, idx, mt, grp, f0, a0, h, lane);
      else p1_helper<MODE, false>(lds, a, xcd, idx, mt, grp, f0, a0, h, lane);
    }
  }
  if (MODE == 1) return;
  if (MODE == 2) { if (tid < 4) ((LAS unsigned*)(lds + L_WORDS))[tid] = 0u; if (tid == 4) ((LAS unsigned*)(lds + L_WORDS))[4] = 0x51u; __syncthreads(); }

  launder(a, xcd, idx);
  ws = a.ws;
  const int p1_mt = (xcd & 1) * 8 + (idx & 7), p1_grp = (xcd >> 1) * 4 + (idx >> 3);
  const int r = xcd >> 1, m = 8 * (xcd & 1) + (idx & 7), n = idx >> 3;
  const int tile = m * 4 + n;
  const int i16 = lane & 15, g = lane >> 4;
  f32x4 accY[7][2], acc[7][2];
  if (wid == 7) {
    p2_loader<MODE, 3, 1>(lds, a, xcd, idx, r, m, n, 3, lane);
  } else if (wid == 6) {
    p2_loader<MODE, 3, 0>(lds, a, xcd, idx, r, m, n, 2, lane);
  } else if (wid >= 4) {
    p2_loader<MODE, 4, 0>(lds, a, xcd, idx, r, m, n, wid - 4, lane);
  } else {
    const int w = wid;
    const int rowt = 32 * w + 8 * (i16 >> 2) + (i16 & 3);
    const int vt0 = rowt * 128 + ((g) ^ (i16 & 7)) * 16, vt1 = rowt * 128 + ((g + 4) ^ (i16 & 7)) * 16;
    const int ve = 16384 + lane * 16;
    const int vh = (4 * w) * 1024 + lane * 16;
    bf16x8 T0[4], T1[4], E[14];
    auto ldTy = [&](bf16x8 (&T)[4], lptr st) {
      T[0] = ldsr128(st + vt0); T[1] = ldsr128(st + vt0 + 512); T[2] = ldsr128(st + vt1); T[3] = ldsr128(st + vt1 + 512);
    };
    auto ldTd = [&](bf16x8 (&T)[4], lptr st) {
      T[0] = ldsr128(st + vh); T[1] = ldsr128(st + vh + 2048); T[2] = ldsr128(st + vh + 1024); T[3] = ldsr128(st + vh + 3072);
    };
    auto ldE = [&](int j, lptr st) { E[j] = ldsr128(st + ve + ((j % 7) * 2 + j / 7) * 1024); };
    auto mm = [&](int j, bf16x8 (&T)[4]) {
      mfma_ip(acc[j % 7][0], E[j], T[(j / 7) * 2]);
      mfma_ip(acc[j % 7][1], E[j], T[(j / 7) * 2 + 1]);
    };
#define GHEAD 3
    constexpr int GH = GHEAD;
    auto ldT1 = [&](bf16x8 (&T)[4], lptr st, int i, int NT) {
      if (NT == 0) T[i] = ldsr128(st + (i >= 2 ? vt1 : vt0) + (i & 1) * 512);
      else T[i] = ldsr128(st + vh + (i & 1) * 2048 + (i >> 1) * 1024);
    };
    auto rest = [&](bf16x8 (&Tc)[4], bf16x8 (&Tn)[4], lptr sn, int NT) {
#pragma unroll
      for (int j = GH; j < 14; ++j) {
        const int k = j - GH;
        mfma_ip(acc[j % 7][0], E[j], Tc[(j / 7) * 2]); SBAR();
        if (NT != 2 && k < 4 + GH) { if (k < 4) ldT1(Tn, sn, k, NT); else ldE(k - 4, sn); SBAR(); }
        mfma_ip(acc[j % 7][1], E[j], Tc[(j / 7) * 2 + 1]); SBAR();
        if (NT != 2) ldE(j, sn); else FILL();
        SBAR();
      }
    };
    auto head = [&](bf16x8 (&Tc)[4]) {
#pragma unroll
      for (int j = 0; j < GH; ++j) { mm(j, Tc); FILL(); }
      SBAR();
    };
#pragma unroll
    for (int u = 0; u < 7; ++u) { acc[u][0] = f32x4{0, 0, 0, 0}; acc[u][1] = f32x4{0, 0, 0, 0}; }
#pragma unroll
    for (int u = 0; u < 7; ++u) { asm volatile("" : "+v"(acc[u][0])); asm volatile("" : "+v"(acc[u][1])); }
    PAD_MFMA_IN();
    if (w != r) {
      const rsrc_t rp = mk_rsrc(ws + WS_PD + (size_t)((tile * 4 + r) * 4 + w) * 7168, 7168);
      const u32x4 sent = {0xffffffffu, 0xffffffffu, 0xffffffffu, 0xffffffffu};
#pragma unroll
      for (int u = 0; u < 7; ++u) {
        __builtin_amdgcn_raw_buffer_store_b128(sent, rp, u * 1024 + lane * 16, 0, 16);
      }
    }
    STAMP(16); STAMPR(28);
    BAR();
    STAMP(17); STAMPR(27);
    ldTy(T0, lds);
#pragma unroll
    for (int j = 0; j < 14; ++j) ldE(j, lds);
    SBAR();
    head(T0);
    WAITL0(); BAR();
    int slot = 1;
#pragma unroll 1
    for (int s = 0; s < P_NY - 2; s += 2) {
      lptr sn = lds + slot * P_STAGE; slot = (slot == 4) ? 0 : slot + 1;
      rest(T0, T1, sn, 0); head(T1);
      WAITL0(); BAR();
      sn = lds + slot * P_STAGE; slot = (slot == 4) ? 0 : slot + 1;
      rest(T1, T0, sn, 0); head(T0);
      WAITL0(); BAR();
      if (YREP > 1 && s == 14) { STAMP(23); }
      if (MODE == 0 && s == 0) {
        WAITV(0);
        if (lane == 0) {
          const unsigned cnt = __hip_atomic_fetch_add((LAS unsigned*)(lds + L_WORDS + 4), 1u, __ATOMIC_RELAXED, __HIP_MEMORY_SCOPE_WORKGROUP);
          if (cnt == 7u) st_flag(ws + WS_FH + (p1_mt * 16 + p1_grp) * 64, get_tag(lds));
        }
      }
    }
    {
      lptr sn = lds + slot * P_STAGE; slot = (slot == 4) ? 0 : slot + 1;
      rest(T0, T1, sn, 0); head(T1);
      WAITL0(); BAR();
      sn = lds + slot * P_STAGE; slot = (slot == 4) ? 0 : slot + 1;
      rest(T1, T0, sn, 1);
    }
    PAD_MFMA_OUT();
    STAMP(18);
    {
      const float sg = (r >> 1) ? -1.0f : 1.0f;
#pragma unroll
      for (int u = 0; u < 7; ++u) { accY[u][0] = acc[u][0] * sg; accY[u][1] = acc[u][1] * sg; acc[u][0] = f32x4{0, 0, 0, 0}; acc[u][1] = f32x4{0, 0, 0, 0}; }
      if (w != r) {
        const rsrc_t rp = mk_rsrc(ws + WS_PY + (size_t)((tile * 4 + r) * 4 + w) * 7168, 7168);
#pragma unroll
        for (int u = 0; u < 7; ++u) {
          const u32x4 o = {pk_bf16(accY[u][0][0], accY[u][0][1]), pk_bf16(accY[u][0][2], accY[u][0][3]),
                           pk_bf16(accY[u][1][0], accY[u][1][1]), pk_bf16(accY[u][1][2], accY[u][1][3])};
          __builtin_amdgcn_raw_buffer_store_b128(o, rp, u * 1024 + lane * 16, 0, 16);
        }
      }
    }
#pragma unroll
    for (int u = 0; u < 7; ++u) { asm volatile("" : "+v"(acc[u][0])); asm volatile("" : "+v"(acc[u][1])); }
    PAD_MFMA_IN();
    head(T0);
    WAITL0(); BAR();
#pragma unroll 1
    for (int s = 0; s < P_ND - 1; s += 2) {
      lptr sn = lds + slot * P_STAGE; slot = (slot == 4) ? 0 : slot + 1;
      rest(T0, T1, sn, 1); head(T1);
      WAITL0(); BAR();
      if (s == 0 && w != r) {
        WAITV(0);
        const unsigned tag = get_tag(lds);
        if (lane == 0) st_flag(ws + WS_FY + ((tile * 4 + r) * 4 + w) * 64, tag);
      }
      sn = lds + slot * P_STAGE; slot = (slot == 4) ? 0 : slot + 1;
      rest(T1, T0, sn, 1); head(T0);
      WAITL0(); if (s + 2 < P_ND - 1) BAR();
    }
    rest(T0, T1, lds, 2);
    PAD_MFMA_OUT();
    STAMP(19); STAMPR(29);
    if (w != r) {
      const rsrc_t rp = mk_rsrc(ws + WS_PD + (size_t)((tile * 4 + r) * 4 + w) * 7168, 7168);
#pragma unroll
      for (int u = 0; u < 7; ++u) {
        u32x4 o = {pk_bf16(acc[u][0][0], acc[u][0][1]), pk_bf16(acc[u][0][2], acc[u][0][3]),
                   pk_bf16(acc[u][1][0], acc[u][1][1]), pk_bf16(acc[u][1][2], acc[u][1][3])};
#pragma unroll
        for (int k = 0; k < 4; ++k) o[k] = o[k] < 0xfffffffeu ? o[k] : 0xfffffffeu;
        __builtin_amdgcn_raw_buffer_store_b128(o, rp, u * 1024 + lane * 16, 0, 16);
      }
    } else {
#pragma unroll
      for (int uu = 0; uu < 7; ++uu) {
        *(LAS f32x4*)(lds + (uu * 4 + 0) * 1024 + lane * 16) = accY[uu][0];
        *(LAS f32x4*)(lds + (uu * 4 + 1) * 1024 + lane * 16) = accY[uu][1];
        *(LAS f32x4*)(lds + (uu * 4 + 2) * 1024 + lane * 16) = acc[uu][0];
        *(LAS f32x4*)(lds + (uu * 4 + 3) * 1024 + lane * 16) = acc[uu][1];
      }
      WAITL0();
    }
    BAR();
  }
  if (wid >= 4) {
    __builtin_amdgcn_s_setprio(0);
    const int hw = wid - 4;
    const int nu = hw < 3 ? 2 : 1;
    const unsigned tag = get_tag(lds);
    const int tok0 = 128 * m + 32 * r + 8 * (i16 >> 2) + (i16 & 3);
    const rsrc_t ry = mk_rsrc(ws + WS_PY, 64 * 16 * 7168), rd = mk_rsrc(ws + WS_PD, 64 * 16 * 7168);
    u32x2 x0[2], x1[2];
    u32x4 fy[2][3], fd[2][3];
#pragma unroll
    for (int k = 0; k < 2; ++k) {
      const int u = 2 * hw + (k < nu ? k : 0);
      const int ecol = 112 * n + 16 * u + 4 * g;
      x0[k] = *(const u32x2*)(a.x + (size_t)tok0 * D + ecol); x1[k] = *(const u32x2*)(a.x + (size_t)(tok0 + 4) * D + ecol);
    }
    {
      const int rr = (r + 1 + (lane % 3)) & 3;
      poll_all(ws + WS_FY + ((tile * 4 + rr) * 4 + r) * 64, lane < 3, tag);
    }
#pragma unroll
    for (int k = 0; k < 2; ++k)
#pragma unroll
      for (int i = 0; i < 3; ++i)
        fy[k][i] = __builtin_amdgcn_raw_buffer_load_b128(ry, ((tile * 4 + ((r + 1 + i) & 3)) * 4 + r) * 7168 + (2 * hw + (k < nu ? k : 0)) * 1024 + lane * 16, 0, 16);
    STAMP(20);
    if (MODE == 0 && blockIdx.x == 0 && hw == 3) {
#pragma unroll 1
      for (int i = 0; i < 4; ++i) poll_all(ws + WS_ARRIVE + (64 * i + lane) * 64, true, tag);
      if (lane == 0) st_flag(ws + WS_NONCE, tag);
    }
    for (unsigned sp = 0; sp < (SPIN_MAX >> 3); ++sp) {
      const unsigned v = lane < 3 ? __builtin_amdgcn_raw_buffer_load_b32(rd, ((tile * 4 + ((r + 1 + lane) & 3)) * 4 + r) * 7168 + 6 * 1024 + 63 * 16 + 12, 0, 16) : 0u;
      if (__all(v != 0xffffffffu)) break;
      __builtin_amdgcn_s_sleep(1);
    }
    for (unsigned sp = 0; sp < (SPIN_MAX >> 3); ++sp) {
      bool ok = true;
#pragma unroll
      for (int k = 0; k < 2; ++k)
#pragma unroll
        for (int i = 0; i < 3; ++i) {
          fd[k][i] = __builtin_amdgcn_raw_buffer_load_b128(rd, ((tile * 4 + ((r + 1 + i) & 3)) * 4 + r) * 7168 + (2 * hw + (k < nu ? k : 0)) * 1024 + lane * 16, 0, 16);
        }
#pragma unroll
      for (int k = 0; k < 2; ++k)
#pragma unroll
        for (int i = 0; i < 3; ++i)
          ok = ok && fd[k][i][0] != 0xffffffffu && fd[k][i][1] != 0xffffffffu && fd[k][i][2] != 0xffffffffu && fd[k][i][3] != 0xffffffffu;
      if (__all(ok)) break;
      __builtin_amdgcn_s_sleep(2);
    }
    STAMP(21); STAMPR(29);
    BAR();
#pragma unroll
    for (int k = 0; k < 2; ++k) {
      if (k < nu) {
        const int u = 2 * hw + k;
        const int ecol = 112 * n + 16 * u + 4 * g;
        f32x4 y0 = *(LAS f32x4*)(lds + (u * 4 + 0) * 1024 + lane * 16), y1 = *(LAS f32x4*)(lds + (u * 4 + 1) * 1024 + lane * 16);
        f32x4 d0 = *(LAS f32x4*)(lds + (u * 4 + 2) * 1024 + lane * 16), d1 = *(LAS f32x4*)(lds + (u * 4 + 3) * 1024 + lane * 16);
#pragma unroll
        for (int i = 0; i < 3; ++i) {
          y0 += f32x4{bf_lo(fy[k][i][0]), bf_hi(fy[k][i][0]), bf_lo(fy[k][i][1]), bf_hi(fy[k][i][1])};
          y1 += f32x4{bf_lo(fy[k][i][2]), bf_hi(fy[k][i][2]), bf_lo(fy[k][i][3]), bf_hi(fy[k][i][3])};
          d0 += f32x4{bf_lo(fd[k][i][0]), bf_hi(fd[k][i][0]), bf_lo(fd[k][i][1]), bf_hi(fd[k][i][1])};
          d1 += f32x4{bf_lo(fd[k][i][2]), bf_hi(fd[k][i][2]), bf_lo(fd[k][i][3]), bf_hi(fd[k][i][3])};
        }
        const u32x2 o0 = {pk_bf16(bf_lo(x0[k][0]) + y0[0] * silu_f(d0[0]), bf_hi(x0[k][0]) + y0[1] * silu_f(d0[1])),
                          pk_bf16(bf_lo(x0[k][1]) + y0[2] * silu_f(d0[2]), bf_hi(x0[k][1]) + y0[3] * silu_f(d0[3]))};
        const u32x2 o1 = {pk_bf16(bf_lo(x1[k][0]) + y1[0] * silu_f(d1[0]), bf_hi(x1[k][0]) + y1[1] * silu_f(d1[1])),
                          pk_bf16(bf_lo(x1[k][1]) + y1[2] * silu_f(d1[2]), bf_hi(x1[k][1]) + y1[3] * silu_f(d1[3]))};
        *(u32x2*)(a.out + (size_t)tok0 * D + ecol) = o0;
        *(u32x2*)(a.out + (size_t)(tok0 + 4) * D + ecol) = o1;
      }
    }
  }
}

#define SPLIT 0
extern "C" void kernel_launch(void* const* d_in, const int* in_sizes, int n_in, void* d_out, int out_size, void* d_ws, size_t ws_size,
                              hipStream_t stream) {
  Args a;
  a.x = (const u16*)d_in[0]; a.Cr = (const u16*)d_in[1]; a.Ci = (const u16*)d_in[2]; a.Dr = (const u16*)d_in[3]; a.Di = (const u16*)d_in[4];
  a.Wg = (const u16*)d_in[6]; a.Wu = (const u16*)d_in[7]; a.Wd = (const u16*)d_in[8];
  a.ws = (char*)d_ws; a.out = (u16*)d_out;
  (void)hipFuncSetAttribute((const void*)fused_kernel<0>, hipFuncAttributeMaxDynamicSharedMemorySize, L_TOTAL);
  fused_kernel<0><<<256, 512, L_TOTAL, stream>>>(a);
}
```

```cpp
#include <hip/hip_runtime.h>
#include <hip/hip_bf16.h>
#include <stdint.h>

typedef unsigned short u16;
typedef __bf16 bf16x8 __attribute__((ext_vector_type(8)));
typedef short s16x8 __attribute__((ext_vector_type(8)));
typedef float f32x4 __attribute__((ext_vector_type(4)));
typedef float f32x2 __attribute__((ext_vector_type(2)));
typedef unsigned u32x4 __attribute__((ext_vector_type(4)));
typedef unsigned u32x2 __attribute__((ext_vector_type(2)));
#define LAS __attribute__((address_space(3)))
#define GAS __attribute__((address_space(1)))
typedef LAS char* lptr;
typedef __amdgpu_buffer_rsrc_t rsrc_t;

constexpr int S = 2048, D = 448, F = 1792;

constexpr size_t WS_NONCE  = 0;
constexpr size_t WS_ARRIVE = 4096;
constexpr size_t WS_FAB    = 32768;
constexpr size_t WS_FH     = 49152;
constexpr size_t WS_FWD    = 65536;
constexpr size_t WS_FY     = 81920;
constexpr size_t WS_FD     = 147456;
constexpr size_t WS_ST     = 212992;
constexpr size_t WS_CW     = 229376;
constexpr size_t WS_CD     = 286720;
constexpr size_t WS_AB     = 327680;
constexpr size_t WS_H      = WS_AB + (size_t)2 * 28 * 64 * 1024;
constexpr size_t WS_WDT    = WS_H + (size_t)128 * 56 * 1024;
constexpr size_t WS_PY     = WS_WDT + (size_t)28 * 56 * 1024;
constexpr size_t WS_PD     = WS_PY + (size_t)64 * 16 * 7 * 1024;

#define STAMP(i)
#define STAMPR(i)
#define SBAR() __builtin_amdgcn_sched_barrier(0)
#define BAR() __builtin_amdgcn_s_barrier()
#define WAITV(n) asm volatile("s_waitcnt vmcnt(%0)" ::"n"(n) : "memory")
#define WAITL0() __builtin_amdgcn_s_waitcnt(0xC07F)
#define CODE_PF 49152
#define DFTPF 0
#define PF0 1
#define STAUX 16
#define SPIN_MAX (1u << 18)

__device__ __forceinline__ rsrc_t mk_rsrc(const void* p, unsigned bytes) {
  return __builtin_amdgcn_make_buffer_rsrc((void*)p, 0, bytes, 0x00020000);
}
__device__ __forceinline__ u32x4 mk_srd(const void* p, unsigned bytes) {
  const unsigned long long a = (unsigned long long)p;
  return u32x4{(unsigned)a, (unsigned)(a >> 32) & 0xffffu, bytes, 0x00020000u};
}
__device__ __forceinline__ unsigned lds_addr(lptr p) { return (unsigned)(size_t)p; }
__device__ __forceinline__ void dma16(const u32x4& srd, unsigned dst, int voff, int soff) {
  asm volatile("s_mov_b32 m0, %0\n\ts_nop 0\n\tbuffer_load_dwordx4 %1, %2, %3 offen lds" ::"s"(dst), "v"(voff), "s"(srd), "s"(soff) : "memory");
}
__device__ __forceinline__ void dma16c(const u32x4& srd, unsigned dst, int voff, int soff) {
  asm volatile("s_mov_b32 m0, %0\n\ts_nop 0\n\tbuffer_load_dwordx4 %1, %2, %3 offen sc1 lds" ::"s"(dst), "v"(voff), "s"(srd), "s"(soff) : "memory");
}
__device__ __forceinline__ void pf4c(const char* p, unsigned dst) {
  asm volatile("s_mov_b32 m0, %0\n\ts_nop 0\n\tglobal_load_lds_dword %1, off sc1" ::"s"(dst), "v"(p) : "memory");
}
__device__ __forceinline__ void pf4(const char* p, unsigned dst) {
  asm volatile("s_mov_b32 m0, %0\n\ts_nop 0\n\tglobal_load_lds_dword %1, off" ::"s"(dst), "v"(p) : "memory");
}
__device__ __forceinline__ bf16x8 ldsr128(lptr p) { return *(const LAS bf16x8*)p; }
__device__ __forceinline__ bf16x8 ldstr(lptr p0, lptr p1) {
  typedef short v4i16_t __attribute__((ext_vector_type(4)));
  v4i16_t lo = __builtin_amdgcn_ds_read_tr16_b64_v4i16((LAS v4i16_t*)p0);
  v4i16_t hi = __builtin_amdgcn_ds_read_tr16_b64_v4i16((LAS v4i16_t*)p1);
  s16x8 v = __builtin_shufflevector(lo, hi, 0, 1, 2, 3, 4, 5, 6, 7);
  return __builtin_bit_cast(bf16x8, v);
}
__device__ __forceinline__ f32x4 mfma(bf16x8 a, bf16x8 b, f32x4 c) { return __builtin_amdgcn_mfma_f32_16x16x32_bf16(a, b, c, 0, 0, 0); }
__device__ __forceinline__ void mfma_ip(f32x4& c, const bf16x8& a, const bf16x8& b) {
  asm volatile("v_mfma_f32_16x16x32_bf16 %0, %1, %2, %0" : "+v"(c) : "v"(a), "v"(b));
}
#define FILL() asm volatile("s_nop 0")
#define PAD_MFMA_OUT() asm volatile("s_nop 7\n\ts_nop 3")
#define PAD_MFMA_IN() asm volatile("s_nop 3")
__device__ __forceinline__ unsigned pk_bf16(float lo, float hi) {
  typedef __bf16 bf16x2 __attribute__((ext_vector_type(2)));
  f32x2 v = {lo, hi};
  bf16x2 b = __builtin_convertvector(v, bf16x2);
  return __builtin_bit_cast(unsigned, b);
}
__device__ __forceinline__ float bf_lo(unsigned v) { return __uint_as_float(v << 16); }
__device__ __forceinline__ float bf_hi(unsigned v) { return __uint_as_float(v & 0xffff0000u); }
__device__ __forceinline__ float rbf(float f) { return bf_lo(pk_bf16(f, 0.f)); }
__device__ __forceinline__ float silu_f(float x) {
  float e = __builtin_amdgcn_exp2f(x * -1.4426950409f);
  return x * __builtin_amdgcn_rcpf(1.0f + e);
}
template <int N>
__device__ __forceinline__ void wave_sums(float (&v)[N]) {
#define DPP_STEP(ctrl, rmask) _Pragma("unroll") for (int i = 0; i < N; ++i) \
    v[i] += __builtin_bit_cast(float, __builtin_amdgcn_update_dpp(0, __builtin_bit_cast(int, v[i]), ctrl, rmask, 0xf, true));
  DPP_STEP(0x111, 0xf)
  DPP_STEP(0x112, 0xf)
  DPP_STEP(0x114, 0xf)
  DPP_STEP(0x118, 0xf)
  DPP_STEP(0x142, 0xa)
  DPP_STEP(0x143, 0xc)
#undef DPP_STEP
#pragma unroll
  for (int i = 0; i < N; ++i) v[i] = __builtin_bit_cast(float, __builtin_amdgcn_readlane(__builtin_bit_cast(int, v[i]), 63));
}
__device__ __forceinline__ unsigned ld_flag(const void* p) { return __hip_atomic_load((const GAS unsigned*)p, __ATOMIC_RELAXED, __HIP_MEMORY_SCOPE_AGENT); }
__device__ __forceinline__ void st_flag(void* p, unsigned v) { __hip_atomic_store((GAS unsigned*)p, v, __ATOMIC_RELAXED, __HIP_MEMORY_SCOPE_AGENT); }
__device__ __forceinline__ unsigned get_tag(lptr lds);
__device__ __forceinline__ void poll_all(const char* p, bool active, unsigned tag) {
  for (unsigned sp = 0; sp < SPIN_MAX; ++sp) {
    const unsigned v = active ? ld_flag(p) : tag;
    if (__all(v == tag)) break;
    __builtin_amdgcn_s_sleep(2);
  }
}
__device__ __forceinline__ void lds_spin(lptr p) {
  for (unsigned sp = 0; sp < SPIN_MAX * 4; ++sp) {
    if (*(volatile LAS unsigned*)p != 0u) break;
    __builtin_amdgcn_s_sleep(2);
  }
}

constexpr int K1_STAGE = 53248, K1_NST = 7, K1_WO = 16384;
constexpr int L_MISC = 3 * K1_STAGE;
constexpr int L_STATS = L_MISC;
constexpr int L_CS = L_MISC + 1024;
constexpr int L_WORDS = L_CS + 18 * 64;
constexpr int L_DUMMY = L_WORDS + 128;
constexpr int L_TOTAL = L_DUMMY + 512;
#define YREP 1
constexpr int P_STAGE = 30720, P_NY = 16 * YREP, P_ND = 7, P_NST = P_NY + P_ND;
static_assert(L_TOTAL <= 163840 && 5 * P_STAGE <= L_MISC, "LDS");

__device__ __forceinline__ unsigned get_tag(lptr lds) { return *(volatile LAS unsigned*)(lds + L_WORDS + 16); }

struct Args {
  const u16 *x, *Cr, *Ci, *Dr, *Di, *Wg, *Wu, *Wd;
  char* ws;
  u16* out;
};

__device__ __forceinline__ void launder(Args& a, int& xcd, int& idx) {
  asm volatile("" : "+s"(a.x), "+s"(a.Cr), "+s"(a.Ci), "+s"(a.Dr), "+s"(a.Di));
  asm volatile("" : "+s"(a.Wg), "+s"(a.Wu), "+s"(a.Wd), "+s"(a.ws), "+s"(a.out));
  asm volatile("" : "+s"(xcd), "+s"(idx));
}

struct RowJobs {
  u32x4 v[4];
  float f[2][8], sm[4];
  __device__ __forceinline__ void load(const Args& a, int nq, int idx, int mt, int grp, int h, int lane) {
    const int l56 = lane < 56 ? lane : 55;
    const u16* sp = a.x + (size_t)(mt * 128 + 8 * grp + 2 * h) * D + l56 * 8;
    const u16* dp = (nq >= 2 ? a.Di : a.Dr) + (size_t)(224 * (nq & 1) + 7 * idx + h) * D + l56 * 8;
    const u16* dq = dp + (h < 3 ? 4 * D : 0);
    asm volatile("global_load_dwordx4 %0, %4, off\n\tglobal_load_dwordx4 %1, %4, off offset:896\n\tglobal_load_dwordx4 %2, %5, off\n\tglobal_load_dwordx4 %3, %6, off"
                 : "=&v"(v[0]), "=&v"(v[1]), "=&v"(v[2]), "=&v"(v[3]) : "v"(sp), "v"(dp), "v"(dq) : "memory");
  }
  __device__ __forceinline__ void landed() { asm volatile("" : "+v"(v[0]), "+v"(v[1]), "+v"(v[2]), "+v"(v[3])); }
  __device__ __forceinline__ void sums(int lane) {
#pragma unroll
    for (int r = 0; r < 4; ++r) {
      float t[8];
#pragma unroll
      for (int i = 0; i < 4; ++i) { t[2 * i] = bf_lo(v[r][i]); t[2 * i + 1] = bf_hi(v[r][i]); }
      const float s = ((t[0] + t[1]) + (t[2] + t[3])) + ((t[4] + t[5]) + (t[6] + t[7]));
      sm[r] = lane < 56 ? s : 0.f;
      if (r < 2) {
#pragma unroll
        for (int i = 0; i < 8; ++i) f[r][i] = t[i];
      }
    }
    wave_sums<4>(sm);
  }
  __device__ __forceinline__ void finish(char* ws, unsigned tag, int nq, int idx, int mt, int grp, int h, int lane) {
    float q[2], mean[2];
#pragma unroll
    for (int r = 0; r < 2; ++r) {
      mean[r] = sm[r] * (1.0f / D);
      float t0 = 0.f, t1 = 0.f;
#pragma unroll
      for (int i = 0; i < 4; ++i) {
        const float d0 = f[r][2 * i] - mean[r], d1 = f[r][2 * i + 1] - mean[r];
        t0 = __builtin_fmaf(d0, d0, t0); t1 = __builtin_fmaf(d1, d1, t1);
      }
      q[r] = lane < 56 ? t0 + t1 : 0.f;
    }
    wave_sums<2>(q);
    const int r = lane & 1;
    const unsigned pay = pk_bf16(r ? mean[1] : mean[0], rbf(__builtin_amdgcn_sqrtf(rbf(rbf((r ? q[1] : q[0]) * (1.0f / D)) + 1e-5f))));
    if (lane < 2 || lane == 2 || (lane == 3 && h < 3)) {
      const unsigned v_ = lane < 2 ? pay : __float_as_uint(lane == 2 ? sm[2] : sm[3]);
      const size_t off = lane < 2 ? WS_ST + (size_t)(mt * 128 + 8 * grp + 2 * h + lane) * 8 : WS_CD + (size_t)(224 * nq + 7 * idx + h + 4 * (lane - 2)) * 8;
      __hip_atomic_store((GAS unsigned long long*)(ws + off), ((unsigned long long)tag << 32) | v_, __ATOMIC_RELAXED, __HIP_MEMORY_SCOPE_AGENT);
    }
  }
};

__device__ __forceinline__ void h_batch(const f32x4 (&g)[2][2], const f32x4 (&u)[2][2], const f32x4 (&cg)[2], const f32x4 (&cu)[2], const f32x2 (&st)[2],
                                        rsrc_t rh, int off0) {
  f32x2 gg[8], uu[8], z[8];
#pragma unroll
  for (int i = 0; i < 8; ++i) {
    const int t = i >> 2, j = (i >> 1) & 1, hh = i & 1;
    gg[i] = (f32x2{g[j][t][2 * hh], g[j][t][2 * hh + 1]} - f32x2{cg[j][2 * hh], cg[j][2 * hh + 1]} * st[t].x) * st[t].y;
  }
#pragma unroll
  for (int i = 0; i < 8; ++i) z[i] = gg[i] * ((gg[i] * gg[i]) * -0.1029432397f + -2.302208198f);
#pragma unroll
  for (int i = 0; i < 8; ++i) { z[i].x = __builtin_amdgcn_exp2f(z[i].x); z[i].y = __builtin_amdgcn_exp2f(z[i].y); }
#pragma unroll
  for (int i = 0; i < 8; ++i) {
    const int t = i >> 2, j = (i >> 1) & 1, hh = i & 1;
    uu[i] = ((f32x2{u[j][t][2 * hh], u[j][t][2 * hh + 1]} - f32x2{cu[j][2 * hh], cu[j][2 * hh + 1]} * st[t].x) * st[t].y) * gg[i];
    z[i] = z[i] + 1.0f;
  }
#pragma unroll
  for (int i = 0; i < 8; ++i) { z[i].x = __builtin_amdgcn_rcpf(z[i].x); z[i].y = __builtin_amdgcn_rcpf(z[i].y); }
#pragma unroll
  for (int i = 0; i < 8; ++i) uu[i] = uu[i] * z[i];
#pragma unroll
  for (int tl = 0; tl < 2; ++tl) {
    const u32x4 o = {pk_bf16(uu[4 * tl].x, uu[4 * tl].y), pk_bf16(uu[4 * tl + 1].x, uu[4 * tl + 1].y),
                     pk_bf16(uu[4 * tl + 2].x, uu[4 * tl + 2].y), pk_bf16(uu[4 * tl + 3].x, uu[4 * tl + 3].y)};
    __builtin_amdgcn_raw_buffer_store_b128(o, rh, off0 + tl * 56 * 1024, 0, STAUX);
  }
}
constexpr int L_HD = 61440;

template <int MODE, int NG, int NA>
__device__ __forceinline__ void p1_consumer(lptr lds, Args a, int xcd, int idx, int mt, int grp, int f0, int a0, int wm, int wn, int lane) {
  launder(a, xcd, idx);
  asm volatile("" : "+s"(mt), "+s"(grp), "+s"(f0), "+s"(a0));
  char* ws = a.ws;
  const char* cp = (const char*)((xcd >> 2) ? a.Ci : a.Cr) + (size_t)(1024 * (xcd & 1) + 32 * idx + 8 * (2 * wn + wm) + (lane >> 5)) * 4096 + 2048 * ((xcd >> 1) & 1) + (lane & 31) * 64;
  unsigned nonce = 0;
  if (MODE == 0 && wm == 0 && wn == 0) asm volatile("global_load_dword %0, %1, off sc1" : "+v"(nonce) : "v"(ws + WS_NONCE) : "memory");
  constexpr int NBK = 2 * NG, NCQ = NBK / 4, WSZ = NBK * 8 * 256, KSO = 4 * NCQ * 1024;
  constexpr int GO = K1_WO, UO = K1_WO + WSZ, DO = K1_WO + 2 * WSZ;
  const int i16 = lane & 15, g = lane >> 4, q = i16 >> 2, p = i16 & 3;
  const int rowx = 64 * wm + 8 * (i16 >> 2) + (i16 & 3);
  const int vx0 = rowx * 128 + ((g) ^ (i16 & 7)) * 16;
  const int vx1 = rowx * 128 + ((g + 4) ^ (i16 & 7)) * 16;
  const int fw = ((q >> 1) & 1) | ((g & 1) << 1);
  int vw[NG];
#pragma unroll
  for (int j = 0; j < NG; ++j) { const int nb = NG * wn + j; vw[j] = g * NCQ * 1024 + (nb >> 2) * 1024 + q * 128 + (((nb & 3) ^ fw) * 32) + p * 8; }
  const int vd0 = DO + (16 * NA * wn + i16) * 128 + ((g) ^ (i16 & 7)) * 16;
  const int vd1 = DO + (16 * NA * wn + i16) * 128 + ((g + 4) ^ (i16 & 7)) * 16;

  f32x4 G[NG][4], U[NG][4], AB[NA][4];
#pragma unroll
  for (int j = 0; j < NG; ++j)
#pragma unroll
    for (int t = 0; t < 4; ++t) { G[j][t] = f32x4{0, 0, 0, 0}; U[j][t] = f32x4{0, 0, 0, 0}; }
#pragma unroll
  for (int j = 0; j < NA; ++j)
#pragma unroll
    for (int t = 0; t < 4; ++t) AB[j][t] = f32x4{0, 0, 0, 0};

  bf16x8 X0[4], X1[4], WG[NG], WU[NG], WD[NA];
  auto ldx = [&](bf16x8 (&X)[4], lptr st, int ks) {
#pragma unroll
    for (int t = 0; t < 4; ++t) X[t] = ldsr128(st + (ks ? vx1 : vx0) + (32 * (t >> 1) + 4 * (t & 1)) * 128);
  };
  auto ldg = [&](int j, lptr st, int ks) { lptr b = st + GO + ks * KSO + vw[j]; WG[j] = ldstr(b, b + 512); };
  auto ldu = [&](int j, lptr st, int ks) { lptr b = st + UO + ks * KSO + vw[j]; WU[j] = ldstr(b, b + 512); };
  auto ldd = [&](int j, lptr st, int ks) { WD[j] = ldsr128(st + (ks ? vd1 : vd0) + j * 16 * 128); };
  auto grp4 = [&](f32x4 (&C)[4], const bf16x8& W, bf16x8 (&X)[4], bool wfirst, auto mid) {
    if (wfirst) { mfma_ip(C[0], W, X[0]); mfma_ip(C[1], W, X[1]); } else { mfma_ip(C[0], X[0], W); mfma_ip(C[1], X[1], W); }
    SBAR(); mid(); SBAR();
    if (wfirst) { mfma_ip(C[2], W, X[2]); mfma_ip(C[3], W, X[3]); } else { mfma_ip(C[2], X[2], W); mfma_ip(C[3], X[3], W); }
    SBAR();
  };
  auto fill = [&]() { FILL(); };
  auto ldx1 = [&](bf16x8 (&X)[4], lptr st, int ks, int t) { X[t] = ldsr128(st + (ks ? vx1 : vx0) + (32 * (t >> 1) + 4 * (t & 1)) * 128); };
  auto blockA = [&](lptr st) {
#pragma unroll
    for (int j = 0; j < NG; ++j) {
      grp4(G[j], WG[j], X0, true, [&]() { if (2 * j < 4) ldx1(X1, st, 1, 2 * j); else FILL(); }); ldg(j, st, 1); SBAR();
      grp4(U[j], WU[j], X0, true, [&]() { if (2 * j + 1 < 4) ldx1(X1, st, 1, 2 * j + 1); else FILL(); }); ldu(j, st, 1); SBAR();
    }
#pragma unroll
    for (int j = 0; j < NA; ++j) { grp4(AB[j], WD[j], X0, false, fill); ldd(j, st, 1); SBAR(); }
  };
  auto blockB_head = [&]() { grp4(G[0], WG[0], X1, true, fill); FILL(); grp4(U[0], WU[0], X1, true, fill); };
  auto blockB_rest = [&](lptr sn, bool reload) {
    int k = 0;
    auto mid = [&]() {
      if (!reload) FILL();
      else if (k == 0) ldg(0, sn, 0);
      else if (k == 1) ldu(0, sn, 0);
      else if (k < 6) ldx1(X0, sn, 0, k - 2);
      else FILL();
      ++k;
    };
#pragma unroll
    for (int j = 1; j < NG; ++j) {
      grp4(G[j], WG[j], X1, true, mid); if (reload) { ldg(j, sn, 0); SBAR(); } else FILL();
      grp4(U[j], WU[j], X1, true, mid); if (reload) { ldu(j, sn, 0); SBAR(); } else FILL();
    }
#pragma unroll
    for (int j = 0; j < NA; ++j) { grp4(AB[j], WD[j], X1, false, mid); if (reload) { ldd(j, sn, 0); SBAR(); } else FILL(); }
  };

#pragma unroll
  for (int j = 0; j < NG; ++j)
#pragma unroll
    for (int t = 0; t < 4; ++t) { asm volatile("" : "+v"(G[j][t])); asm volatile("" : "+v"(U[j][t])); }
#pragma unroll
  for (int j = 0; j < NA; ++j)
#pragma unroll
    for (int t = 0; t < 4; ++t) asm volatile("" : "+v"(AB[j][t]));
  PAD_MFMA_IN();
  STAMP(1);
  BAR();
  STAMP(2);
  ldg(0, lds, 0); ldu(0, lds, 0); ldx(X0, lds, 0);
#pragma unroll
  for (int j = 1; j < NG; ++j) { ldg(j, lds, 0); ldu(j, lds, 0); }
#pragma unroll
  for (int j = 0; j < NA; ++j) ldd(j, lds, 0);
  SBAR();
  int slot = 0;
#pragma unroll 1
  for (int s = 0; s < K1_NST; ++s) {
    blockA(lds + slot * K1_STAGE);
    if (s >= PF0 && s < PF0 + 4) { pf4(cp, lds_addr(lds) + L_DUMMY); cp += 2 * 4096; }
    blockB_head();
    if (s == 0 && wm == 0 && wn == 0) {
      if (PF0 == 0) { WAITV(1); } else { WAITV(0); }
      asm volatile("" : "+v"(nonce));
      if (lane == 0) *(volatile LAS unsigned*)(lds + L_WORDS + 16) = (MODE == 0) ? nonce * 0x9E3779B1u + 0x7F4A7C15u : 0x51u;
    }
    WAITL0();
    STAMP(19 + s);
    BAR();
    STAMP(7 + s);
    slot = (slot == 2) ? 0 : slot + 1;
    blockB_rest(lds + slot * K1_STAGE, true);
  }
  PAD_MFMA_OUT();
  STAMP(3);
  const lptr cs = lds + L_CS;
  const lptr stats = lds + L_STATS;
  {
    const rsrc_t rab = mk_rsrc(ws + WS_AB, 2 * 28 * 64 * 1024);
#pragma unroll
    for (int pi = 0; pi < 2; ++pi) {
      const lptr sp = stats + (64 * wm + 32 * pi + 8 * g) * 8;
      const f32x4 s0 = *(LAS f32x4*)(sp), s1 = *(LAS f32x4*)(sp + 16), s2 = *(LAS f32x4*)(sp + 32), s3 = *(LAS f32x4*)(sp + 48);
#pragma unroll
      for (int j = 0; j < NA; ++j) {
        const float c = *(LAS float*)(cs + (2 * NBK + NA * wn + j) * 64 + i16 * 4);
        const f32x4 a_ = AB[j][2 * pi], b = AB[j][2 * pi + 1];
        const int ks = mt * 4 + wm * 2 + pi;
        u32x4 o;
        o[0] = pk_bf16((a_[0] - s0[0] * c) * s0[1], (a_[1] - s0[2] * c) * s0[3]);
        o[1] = pk_bf16((a_[2] - s1[0] * c) * s1[1], (a_[3] - s1[2] * c) * s1[3]);
        o[2] = pk_bf16((b[0] - s2[0] * c) * s2[1], (b[1] - s2[2] * c) * s2[3]);
        o[3] = pk_bf16((b[2] - s3[0] * c) * s3[1], (b[3] - s3[2] * c) * s3[3]);
        __builtin_amdgcn_raw_buffer_store_b128(o, rab, lane * 16 + ((a0 + NA * wn + j) * 64 + ks) * 1024, 0, STAUX);
      }
    }
  }
  {
    lptr hd = lds + L_HD + (2 * wn + wm) * (NG * 4096) + lane * 16;
#pragma unroll
    for (int j = 0; j < NG; ++j)
#pragma unroll
      for (int tl = 0; tl < 2; ++tl) {
        *(LAS f32x4*)(hd + ((j * 2 + 0) * 2 + tl) * 1024) = G[j][2 + tl];
        *(LAS f32x4*)(hd + ((j * 2 + 1) * 2 + tl) * 1024) = U[j][2 + tl];
      }
  }
  WAITV(0);
  if (lane == 0) {
    const unsigned n = __hip_atomic_fetch_add((LAS unsigned*)(lds + L_WORDS), 1u, __ATOMIC_RELAXED, __HIP_MEMORY_SCOPE_WORKGROUP);
    if (n == 3u) st_flag(ws + WS_FAB + (mt * 16 + grp) * 64, get_tag(lds));
  }
  STAMP(5);
  WAITL0(); BAR();
  STAMP(14);
  const rsrc_t rh = mk_rsrc(ws + WS_H, 128 * 56 * 1024);
  const int tt0 = mt * 8 + wm * 4;
  f32x2 st[2];
#pragma unroll
  for (int t = 0; t < 2; ++t) st[t] = *(LAS f32x2*)(stats + (64 * wm + 8 * (i16 >> 2) + 4 * t + (i16 & 3)) * 8);
#pragma unroll
  for (int pp = 0; pp < NG / 2; ++pp) {
    const int ks = ((f0 >> 4) + NG * wn + 2 * pp) >> 1;
    f32x4 cg[2], cu[2], gq[2][2], uq[2][2];
#pragma unroll
    for (int j = 0; j < 2; ++j) {
      cg[j] = *(LAS f32x4*)(cs + (NG * wn + 2 * pp + j) * 64 + g * 16);
      cu[j] = *(LAS f32x4*)(cs + (NBK + NG * wn + 2 * pp + j) * 64 + g * 16);
      gq[j][0] = G[2 * pp + j][0]; gq[j][1] = G[2 * pp + j][1]; uq[j][0] = U[2 * pp + j][0]; uq[j][1] = U[2 * pp + j][1];
    }
    h_batch(gq, uq, cg, cu, st, rh, lane * 16 + (tt0 * 56 + ks) * 1024);
  }
  STAMP(4);
}

template <int NG>
__device__ __forceinline__ void p1_helper_epilogue(lptr lds, char* ws, int mt, int f0, int h, int lane) {
  constexpr int NBK = 2 * NG;
  const int wm = h & 1, wn = h >> 1, i16 = lane & 15, g = lane >> 4;
  const lptr cs = lds + L_CS, stats = lds + L_STATS;
  const rsrc_t rh = mk_rsrc(ws + WS_H, 128 * 56 * 1024);
  const int tt0 = mt * 8 + wm * 4 + 2;
  lptr hd = lds + L_HD + h * (NG * 4096) + lane * 16;
  f32x2 st[2];
#pragma unroll
  for (int t = 0; t < 2; ++t) st[t] = *(LAS f32x2*)(stats + (64 * wm + 32 + 8 * (i16 >> 2) + 4 * t + (i16 & 3)) * 8);
#pragma unroll
  for (int pp = 0; pp < NG / 2; ++pp) {
    const int ks = ((f0 >> 4) + NG * wn + 2 * pp) >> 1;
    f32x4 cg[2], cu[2], gq[2][2], uq[2][2];
#pragma unroll
    for (int j = 0; j < 2; ++j) {
      cg[j] = *(LAS f32x4*)(cs + (NG * wn + 2 * pp + j) * 64 + g * 16);
      cu[j] = *(LAS f32x4*)(cs + (NBK + NG * wn + 2 * pp + j) * 64 + g * 16);
#pragma unroll
      for (int tl = 0; tl < 2; ++tl) {
        gq[j][tl] = *(LAS f32x4*)(hd + (((2 * pp + j) * 2 + 0) * 2 + tl) * 1024);
        uq[j][tl] = *(LAS f32x4*)(hd + (((2 * pp + j) * 2 + 1) * 2 + tl) * 1024);
      }
    }
    h_batch(gq, uq, cg, cu, st, rh, lane * 16 + (tt0 * 56 + ks) * 1024);
  }
}

template <int MODE, bool TB>
__device__ __forceinline__ void p1_helper(lptr lds, Args a, int xcd, int idx, int mt, int grp, int f0, int a0, int h, int lane) {
  constexpr int NBK = TB ? 4 : 8, WSZ = NBK * 8 * 256;
  constexpr int NWP = TB ? 4 : 8, NDP = TB ? 4 : 1, PER = 4 + NWP + NDP;
  const int mh = xcd & 1, nq = xcd >> 1;
  const int up = h >> 1;
  const u32x4 rx = mk_srd(a.x, S * D * 2);
  const u32x4 rw = mk_srd(up ? a.Wu : a.Wg, D * F * 2);
  const u32x4 rd = mk_srd(a0 >= 28 ? a.Di : a.Dr, D * D * 2);
  const int e0 = (a0 >= 28 ? a0 - 28 : a0) * 16;
  const int r8 = lane >> 3, c8 = lane & 7;
  int vx[2];
  vx[0] = r8 * 896 + (c8 ^ (r8 & 3)) * 16;
  vx[1] = r8 * 896 + (c8 ^ ((r8 & 3) | 4)) * 16;
  const int vd = r8 * 896 + (c8 ^ r8) * 16;
  int vw[2];
  {
    const int f0_ = (r8 >> 1) & 1;
    vw[0] = r8 * (F * 2) + f0 * 2 + ((((c8 >> 1) ^ f0_) * 2) + (c8 & 1)) * 16;
    vw[1] = r8 * (F * 2) + f0 * 2 + ((((c8 >> 1) ^ (f0_ | 2)) * 2) + (c8 & 1)) * 16;
  }
  const unsigned l0 = lds_addr(lds);
  const unsigned dummy = l0 + L_DUMMY + (h & 1) * 256;
  const int sxb = (mt * 128 + 32 * h) * 896, swb = 32 * (h & 1) * (F * 2), sdb = (e0 + 8 * NDP * h) * 896;
  const unsigned lxb = l0 + 4 * h * 1024, lwb = l0 + K1_WO + up * WSZ + NWP * (h & 1) * 1024, ldb = l0 + K1_WO + 2 * WSZ + NDP * h * 1024;
  auto piece = [&](int s, int slot, int i) {
    const unsigned so = slot * K1_STAGE;
    if (i < 4) dma16(rx, lxb + so + i * 1024, vx[i & 1], sxb + s * 128 + i * 8 * 896);
    else if (i < 4 + NWP) {
      const int j = i - 4, kb = TB ? j : j >> 1, cq = TB ? 0 : j & 1;
      dma16(rw, lwb + so + j * 1024, vw[kb & 1], swb + s * 64 * (F * 2) + kb * 8 * (F * 2) + cq * 128);
    } else dma16(rd, ldb + so + (i - 4 - NWP) * 1024, vd, sdb + s * 128 + (i - 4 - NWP) * 8 * 896);
  };
  auto issue_a = [&](int s, int slot) {
#pragma unroll
    for (int i = 0; i < 8; ++i) piece(s, slot, i);
  };
  auto issue_b = [&](int s, int slot) {
#pragma unroll
    for (int i = 8; i < PER; ++i) piece(s, slot, i);
  };
  const int ln2 = lane >> 1, hb = (lane & 1) * 64;
  const char* pfx = (const char*)a.x + (size_t)(1024 * mh + 32 * idx + ln2) * 896 + hb;
  STAMP(1);
  issue_a(0, 0); issue_b(0, 0);
  STAMP(5);
  launder(a, xcd, idx);
  char* ws = a.ws;
  const bool wjob = idx < 28;
  const int wj_mat = idx >= 14, wj_cb = (idx - 14 * wj_mat) >> 1, wj_kh = idx & 1;
  u32x4 wx[7];
  {
    const u32x4 rj = mk_srd(wj_mat ? a.Wu : a.Wg, D * F * 2);
    const int vj = r8 * (F * 2) + c8 * 16 + (448 * nq + 64 * (wjob ? wj_cb : 0)) * 2;
    const int sj = (224 * (wjob ? wj_kh : 0) + 56 * h) * (F * 2);
    asm volatile("buffer_load_dwordx4 %0, %7, %8, %9 offen\n\tbuffer_load_dwordx4 %1, %7, %8, %10 offen\n\tbuffer_load_dwordx4 %2, %7, %8, %11 offen\n\t"
                 "buffer_load_dwordx4 %3, %7, %8, %12 offen\n\tbuffer_load_dwordx4 %4, %7, %8, %13 offen\n\tbuffer_load_dwordx4 %5, %7, %8, %14 offen\n\t"
                 "buffer_load_dwordx4 %6, %7, %8, %15 offen"
                 : "=&v"(wx[0]), "=&v"(wx[1]), "=&v"(wx[2]), "=&v"(wx[3]), "=&v"(wx[4]), "=&v"(wx[5]), "=&v"(wx[6])
                 : "v"(vj), "s"(rj), "s"(sj), "s"(sj + 8 * F * 2), "s"(sj + 16 * F * 2), "s"(sj + 24 * F * 2), "s"(sj + 32 * F * 2), "s"(sj + 40 * F * 2), "s"(sj + 48 * F * 2)
                 : "memory");
  }
  RowJobs rj_;
  rj_.load(a, nq, idx, mt, grp, h, lane);
  pf4(pfx + (1 + h) * 128, dummy);
  if (h == 3 && lane < 4) ((LAS unsigned*)(lds + L_WORDS))[lane] = 0u;
  WAITL0(); WAITV(12);
  STAMP(7); BAR();
  STAMP(2);
#pragma unroll
  for (int i = 0; i < PER; ++i) piece(1, 1, i);
  issue_a(2, 2);
  if (h < 2) pf4(pfx + (5 + h) * 128, dummy);
  WAITL0();
  if (h < 2) { WAITV(9); } else { WAITV(8); }
  asm volatile("" : "+v"(wx[0]), "+v"(wx[1]), "+v"(wx[2]), "+v"(wx[3]), "+v"(wx[4]), "+v"(wx[5]), "+v"(wx[6]));
  rj_.landed();
  BAR();
  const unsigned tag1 = get_tag(lds);
  float cw[8];
#pragma unroll
  for (int k = 0; k < 8; ++k) cw[k] = 0.f;
  constexpr bool TBc = TB;
  unsigned long long g0 = 0, g1 = 0, g2 = 0, g3 = 0;
  const bool wdjob = blockIdx.x < 196;
  const int wd_kp = blockIdx.x % 28, wd_cg = blockIdx.x / 28;
#pragma unroll
  for (int s = 1; s < K1_NST; ++s) {
    const int s1_ = (s + 1) % 3, s2_ = (s + 2) % 3;
    if (s == 1) {
#pragma unroll
      for (int i = 0; i < PER; ++i) {
        if (i < PER - 8) piece(2, s1_, 8 + i); else piece(3, s2_, i - (PER - 8));
        SBAR();
        if (i < 7) {
#pragma unroll
          for (int k = 0; k < 4; ++k) { cw[2 * k] += bf_lo(wx[i][k]); cw[2 * k + 1] += bf_hi(wx[i][k]); }
        }
        SBAR();
      }
    } else {
      if (s + 1 < K1_NST) issue_b(s + 1, s1_);

      if (s + 2 < K1_NST) issue_a(s + 2, s2_);
    }
    if (s == 1) {
      pf4((const char*)a.Wd + (size_t)(64 * wd_kp + 16 * h + ((lane >> 1) & 15)) * 896 + (wdjob ? wd_cg : 0) * 128 + (lane & 1) * 64, dummy);
#pragma unroll
      for (int k = 0; k < 8; ++k) {
        cw[k] += __builtin_bit_cast(float, __builtin_amdgcn_update_dpp(0, __builtin_bit_cast(int, cw[k]), 0x128, 0xf, 0xf, true));
        const auto p16 = __builtin_amdgcn_permlane16_swap(__float_as_uint(cw[k]), __float_as_uint(cw[k]), false, false);
        cw[k] = __uint_as_float(p16[0]) + __uint_as_float(p16[1]);
        const auto p32 = __builtin_amdgcn_permlane32_swap(__float_as_uint(cw[k]), __float_as_uint(cw[k]), false, false);
        cw[k] = __uint_as_float(p32[0]) + __uint_as_float(p32[1]);
      }
      if (lane < 8) {
        *(LAS f32x4*)(lds + L_CS + h * 256 + lane * 32) = f32x4{cw[0], cw[1], cw[2], cw[3]};
        *(LAS f32x4*)(lds + L_CS + h * 256 + lane * 32 + 16) = f32x4{cw[4], cw[5], cw[6], cw[7]};
      }
    }
    if (s == 2) rj_.sums(lane);
    if (s == 3) rj_.finish(ws, tag1, nq, idx, mt, grp, h, lane);
    if (s == 2 && h == 0 && wjob) {
      const float t = (*(LAS float*)(lds + L_CS + lane * 4) + *(LAS float*)(lds + L_CS + 256 + lane * 4)) +
                      (*(LAS float*)(lds + L_CS + 512 + lane * 4) + *(LAS float*)(lds + L_CS + 768 + lane * 4));
      __hip_atomic_store((GAS unsigned long long*)(ws + WS_CW + (size_t)((wj_mat * 2 + wj_kh) * F + 448 * nq + 64 * wj_cb + lane) * 8),
                         ((unsigned long long)tag1 << 32) | __float_as_uint(t), __ATOMIC_RELAXED, __HIP_MEMORY_SCOPE_AGENT);
    }
    if (s == 4 && h == 0 && lane == 0) st_flag(ws + WS_ARRIVE + blockIdx.x * 64, tag1);
    if (s == 5) {
      if (wdjob) {
        const u32x4 rwd = mk_srd(a.Wd, F * D * 2);
        const int rs = (lane & 15) >> 1, half = lane & 1, b = lane >> 4;
        const int vq = rs * 896 + (64 * wd_cg + 16 * b + 8 * half) * 2;
#pragma unroll
        for (int i = 0; i < 2; ++i) dma16(rwd, l0 + s2_ * K1_STAGE + (2 * h + i) * 1024, vq, (64 * wd_kp + 8 * (2 * h + i)) * 896);
      }
    }
    const int gc = 64 * h + lane;
    constexpr int NWC = TBc ? 128 : 256, CPM = NWC / 2;
    const bool isw = gc < NWC;
    const char* p_st = ws + WS_ST + (size_t)(mt * 128 + 32 * h + (lane & 31)) * 8;
    const char* p_c0 = ws + (isw ? WS_CW + (size_t)((gc / CPM) * 2 * F + f0 + gc % CPM) * 8 : WS_CD + (size_t)(a0 * 16 + (gc - NWC)) * 8);
    const char* p_c1 = isw ? p_c0 + (size_t)F * 8 : p_c0;
    const char* p_d = ws + WS_CD + (size_t)(a0 * 16 + (lane & 31)) * 8;
    if (s == 5)
      asm volatile("global_load_dwordx2 %0, %4, off sc1\n\tglobal_load_dwordx2 %1, %5, off sc1\n\tglobal_load_dwordx2 %2, %6, off sc1\n\tglobal_load_dwordx2 %3, %7, off sc1"
                   : "=&v"(g0), "=&v"(g1), "=&v"(g2), "=&v"(g3) : "v"(p_st), "v"(p_c0), "v"(p_c1), "v"(p_d) : "memory");
    if (s == 6) {
      WAITV(DFTPF == 2 ? 2 : 0);
      asm volatile("" : "+v"(g0), "+v"(g1), "+v"(g2), "+v"(g3));
      auto ld_gran = [&](const char* p) { return __hip_atomic_load((const GAS unsigned long long*)p, __ATOMIC_RELAXED, __HIP_MEMORY_SCOPE_AGENT); };
      for (unsigned sp = 0; sp < SPIN_MAX; ++sp) {
        if (__all((unsigned)(g0 >> 32) == tag1 && (unsigned)(g1 >> 32) == tag1 && (unsigned)(g2 >> 32) == tag1 && (unsigned)(g3 >> 32) == tag1)) break;
        __builtin_amdgcn_s_sleep(2);
        g0 = ld_gran(p_st); g1 = ld_gran(p_c0); g2 = ld_gran(p_c1); g3 = ld_gran(p_d);
      }
      if (lane < 32) *(LAS f32x2*)(lds + L_STATS + (32 * h + lane) * 8) = f32x2{bf_lo((unsigned)g0), 1.0f / bf_hi((unsigned)g0)};
      *(LAS float*)(lds + L_CS + gc * 4) = isw ? __uint_as_float((unsigned)g1) + __uint_as_float((unsigned)g2) : __uint_as_float((unsigned)g1);
      if (!TBc && h == 0 && lane < 32) *(LAS float*)(lds + L_CS + (256 + lane) * 4) = __uint_as_float((unsigned)g3);
    }
    WAITL0();
    if (s == 1) { WAITV(9); } else if (s + 2 < K1_NST) { WAITV(8); } else if (s == 5) { if (wdjob) { WAITV(6 + (DFTPF == 2 ? 2 : 0)); } else { WAITV(4 + (DFTPF == 2 ? 2 : 0)); } }
    STAMP(7 + s);
    BAR();
  }
  __builtin_amdgcn_s_setprio(0);
  if (h == 3 && wdjob) {
    const int i16 = lane & 15, g = lane >> 4, q = i16 >> 2, p = i16 & 3;
    const rsrc_t rt = mk_rsrc(ws + WS_WDT, 28 * 56 * 1024);
    lptr b0 = lds + 1 * K1_STAGE + (g >> 1) * 1024 + ((g & 1) * 4 + q) * 32 + p * 8;
#pragma unroll
    for (int kk = 0; kk < 2; ++kk)
#pragma unroll
      for (int u = 0; u < 4; ++u) {
        const bf16x8 f = ldstr(b0 + kk * 4096 + u * 256, b0 + kk * 4096 + u * 256 + 2048);
        __builtin_amdgcn_raw_buffer_store_b128(__builtin_bit_cast(u32x4, f), rt, lane * 16 + ((4 * wd_cg + u) * 56 + 2 * wd_kp + kk) * 1024, 0, STAUX);
      }
    WAITL0();
  }
  BAR();
  p1_helper_epilogue<TB ? 2 : 4>(lds, ws, mt, f0, h, lane);
}

template <int MODE, int NB, int JO>
__device__ __forceinline__ void p2_loader(lptr lds, const Args& a, int xcd, int idx, int r, int m, int n, int lw, int lane) {
  char* ws = a.ws;
  constexpr int PER = 4 + NB;
  const u32x4 rc = mk_srd((r >> 1) ? a.Ci : a.Cr, S * S * 2);
  const u32x4 rb = mk_srd(ws + WS_AB, 2 * 28 * 64 * 1024);
  const u32x4 rh = mk_srd(ws + WS_H, 128 * 56 * 1024);
  const u32x4 rw = mk_srd(ws + WS_WDT, 28 * 56 * 1024);
  const int r8 = lane >> 3, c8 = lane & 7;
  int va[2];
  va[0] = r8 * 4096 + (c8 ^ (r8 & 3)) * 16;
  va[1] = r8 * 4096 + (c8 ^ ((r8 & 3) | 4)) * 16;
  const int vl = lane * 16;
  const int k0 = 1024 * (r & 1), p = r >> 1;
  const int j0 = (lw < 2) ? 4 * lw : 8 + 3 * (lw - 2);
  const unsigned l0 = lds_addr(lds);
  const unsigned dummy = l0 + L_DUMMY + (lw & 1) * 256;
  const int sab = (128 * m + 32 * lw) * 4096 + k0 * 2;
  const int shb = ((8 * m + 2 * lw) * 56 + 14 * r) * 1024;
  const unsigned lab = l0 + 4 * lw * 1024, lbb = l0 + 16384 + j0 * 1024;
  const int sbb = ((p * 28 + 7 * n + (j0 >> 1)) * 64 + (k0 >> 5)) * 1024, swb = ((7 * n + (j0 >> 1)) * 56 + 14 * r) * 1024;
  auto issueAy = [&](int s, int slot) {
    const unsigned so = slot * P_STAGE; const int b = sab + 128 * (s & 15);
#pragma unroll
    for (int i = 0; i < 4; ++i) dma16(rc, lab + so + i * 1024, va[i & 1], b + i * 8 * 4096);
  };
  auto issueAd = [&](int sd, int slot) {
    const unsigned so = slot * P_STAGE; const int b = shb + 2048 * sd;
#pragma unroll
    for (int i = 0; i < 4; ++i) dma16c(rh, lab + so + i * 1024, vl, b + (i >> 1) * 56 * 1024 + (i & 1) * 1024);
  };
  auto issueBy = [&](int s, int slot) {
    const unsigned so = slot * P_STAGE; const int b = sbb + 2048 * (s & 15);
#pragma unroll
    for (int j = 0; j < NB; ++j) dma16c(rb, lbb + so + j * 1024, vl, b + ((j + JO) >> 1) * 65536 + ((j + JO) & 1) * 1024);
  };
  auto issueBd = [&](int sd, int slot) {
    const unsigned so = slot * P_STAGE; const int b = swb + 2048 * sd;
#pragma unroll
    for (int j = 0; j < NB; ++j) dma16c(rw, lbb + so + j * 1024, vl, b + ((j + JO) >> 1) * 57344 + ((j + JO) & 1) * 1024);
  };
  __builtin_amdgcn_s_setprio(0);
  const unsigned tag = get_tag(lds);
  if (MODE != 2 && lw == 3 && blockIdx.x < 196) {
    WAITV(0);
    if (lane == 0) st_flag(ws + WS_FWD + blockIdx.x * 64, tag);
  }
  issueAy(0, 0); issueAy(1, 1);
  if (lw == 3) {
    const int mtp = 8 * (r & 1) + (lane & 7), j = (lane >> 3) & 3;
    const bool act = lane < 32 && ((n & 1) == 0 || j == 3);
    poll_all(ws + WS_FAB + (mtp * 16 + (2 * p + (n >> 1)) * 4 + j) * 64, act, tag);
    if (lane == 0) *(volatile LAS unsigned*)(lds + L_WORDS + 8) = 1u;
  } else lds_spin(lds + L_WORDS + 8);
  STAMP(16);
  issueBy(0, 0);
  {
    const int l = (lane >> 1) < 28 ? (lane >> 1) : 27;
    const int sl = l >= 14, li = 14 * idx + l - 14 * sl;
    const char* bp = ws + WS_AB + ((size_t)(p * 28 + (li >> 4)) * 64 + 32 * (r & 1) + 2 * (4 * lw + sl) + ((li >> 3) & 1)) * 1024 + (li & 7) * 128 + (lane & 1) * 64;
    pf4c(bp, dummy); pf4c(bp + 4 * 1024, dummy);
  }
  issueBy(1, 1);
  __builtin_amdgcn_s_setprio(3);
  WAITV(NB + 2); BAR();
  if (MODE == 0 && lane == 0) {
    const unsigned cnt = __hip_atomic_fetch_add((LAS unsigned*)(lds + L_WORDS + 4), 1u, __ATOMIC_RELAXED, __HIP_MEMORY_SCOPE_WORKGROUP);
    if (cnt == 7u) st_flag(ws + WS_FH + (((xcd & 1) * 8 + (idx & 7)) * 16 + (xcd >> 1) * 4 + (idx >> 3)) * 64, tag);
  }
  issueAy(2, 2); issueBy(2, 2);
  WAITV(PER); BAR();
  issueAy(3, 3); issueBy(3, 3); issueAy(4, 4); issueBy(4, 4);
  WAITV(2 * PER); BAR();
  int slot = 0;
  auto ystep = [&](int s) { issueAy(s + 3, slot); issueBy(s + 3, slot); slot = (slot == 4) ? 0 : slot + 1; WAITV(2 * PER); BAR(); };
  auto dstep = [&](int sd) { issueAd(sd, slot); issueBd(sd, slot); slot = (slot == 4) ? 0 : slot + 1; WAITV(2 * PER); BAR(); };
#pragma unroll 1
  for (int s = 2; s < P_NY - 9; ++s) ystep(s);
  const int cg0 = (112 * n) >> 6, cg1 = (112 * n + 111) >> 6, jj = lane - 4;
  const bool fact = lane < 4 || (jj < 21 && cg0 + jj / 7 <= cg1);
  const char* fp = lane < 4 ? ws + WS_FH + (m * 16 + 4 * r + lane) * 64 : ws + WS_FWD + ((fact ? cg0 + jj / 7 : cg0) * 28 + 7 * r + (fact ? jj % 7 : 0)) * 64;
  unsigned fval = 0;
  if (lw == 3) asm volatile("global_load_dword %0, %1, off sc1" : "+v"(fval) : "v"(fp) : "memory");
#pragma unroll 1
  for (int s = P_NY - 9; s < P_NY - 6; ++s) ystep(s);
  if (lw == 3) {
    asm volatile("" : "+v"(fval));
    if (!__all(!fact || fval == tag)) poll_all(fp, fact, tag);
    if (lane == 0) *(volatile LAS unsigned*)(lds + L_WORDS + 12) = 1u;
  }
#pragma unroll 1
  for (int s = P_NY - 6; s < P_NY - 3; ++s) ystep(s);
  if (lw != 3) lds_spin(lds + L_WORDS + 12);
#pragma unroll 1
  for (int sd = 0; sd < P_ND; ++sd) dstep(sd);
  WAITV(PER); BAR();
  WAITV(0); BAR();
}

template <int MODE>
__global__ __launch_bounds__(512, 2) void fused_kernel(Args a) {
  extern __shared__ __attribute__((aligned(1024))) char smem[];
  lptr lds = (lptr)smem;
  char* ws = a.ws;
  const int tid = threadIdx.x, lane = tid & 63;
  const int wid = __builtin_amdgcn_readfirstlane(tid >> 6);
  int xcd = blockIdx.x & 7, idx = blockIdx.x >> 3;
  STAMP(0); STAMPR(30);
  const unsigned long long t0_ = __builtin_amdgcn_s_memtime();
  if (wid == 3) {
    unsigned long long pc;
    asm volatile("s_getpc_b64 %0" : "=s"(pc));
    const unsigned ln = 13 * idx + ((lane >> 1) < 13 ? (lane >> 1) : 12);
    pf4((const char*)(pc & ~127ull) + (ln * 128 < CODE_PF ? ln * 128 : 0) + (lane & 1) * 64, lds_addr(lds) + L_DUMMY);
  }
  if (wid >= 4) __builtin_amdgcn_s_setprio(3);

  if (MODE != 2) {
    const int nq = xcd >> 1;
    const int mt = (xcd & 1) * 8 + (idx & 7), j = idx >> 3;
    const bool tb = j == 3;
    const int grp = nq * 4 + j;
    const int f0 = 448 * nq + 128 * j, a0 = 14 * nq + 2 * j;
    if (wid < 4) {
      if (tb) p1_consumer<MODE, 2, 4>(lds, a, xcd, idx, mt, grp, f0, a0, wid & 1, wid >> 1, lane);
      else p1_consumer<MODE, 4, 1>(lds, a, xcd, idx, mt, grp, f0, a0, wid & 1, wid >> 1, lane);
    } else {
      const int h = wid - 4;
      if (tb) p1_helper<MODE, true>(lds, a, xcd, idx, mt, grp, f0, a0, h, lane);
      else p1_helper<MODE, false>(lds, a, xcd, idx, mt, grp, f0, a0, h, lane);
    }
  }
  if (MODE == 1) return;
  if (MODE == 2) { if (tid < 4) ((LAS unsigned*)(lds + L_WORDS))[tid] = 0u; if (tid == 4) ((LAS unsigned*)(lds + L_WORDS))[4] = 0x51u; __syncthreads(); }

  launder(a, xcd, idx);
  ws = a.ws;
  const int p1_mt = (xcd & 1) * 8 + (idx & 7), p1_grp = (xcd >> 1) * 4 + (idx >> 3);
  const int r = xcd >> 1, m = 8 * (xcd & 1) + (idx & 7), n = idx >> 3;
  const int tile = m * 4 + n;
  const int i16 = lane & 15, g = lane >> 4;
  f32x4 accY[7][2], acc[7][2];
  if (wid == 7) {
    p2_loader<MODE, 3, 1>(lds, a, xcd, idx, r, m, n, 3, lane);
  } else if (wid == 6) {
    p2_loader<MODE, 3, 0>(lds, a, xcd, idx, r, m, n, 2, lane);
  } else if (wid >= 4) {
    p2_loader<MODE, 4, 0>(lds, a, xcd, idx, r, m, n, wid - 4, lane);
  } else {
    const int w = wid;
    const int rowt = 32 * w + 8 * (i16 >> 2) + (i16 & 3);
    const int vt0 = rowt * 128 + ((g) ^ (i16 & 7)) * 16, vt1 = rowt * 128 + ((g + 4) ^ (i16 & 7)) * 16;
    const int ve = 16384 + lane * 16;
    const int vh = (4 * w) * 1024 + lane * 16;
    bf16x8 T0[4], T1[4], E[14];
    auto ldTy = [&](bf16x8 (&T)[4], lptr st) {
      T[0] = ldsr128(st + vt0); T[1] = ldsr128(st + vt0 + 512); T[2] = ldsr128(st + vt1); T[3] = ldsr128(st + vt1 + 512);
    };
    auto ldTd = [&](bf16x8 (&T)[4], lptr st) {
      T[0] = ldsr128(st + vh); T[1] = ldsr128(st + vh + 2048); T[2] = ldsr128(st + vh + 1024); T[3] = ldsr128(st + vh + 3072);
    };
    auto ldE = [&](int j, lptr st) { E[j] = ldsr128(st + ve + ((j % 7) * 2 + j / 7) * 1024); };
    auto mm = [&](int j, bf16x8 (&T)[4]) {
      mfma_ip(acc[j % 7][0], E[j], T[(j / 7) * 2]);
      mfma_ip(acc[j % 7][1], E[j], T[(j / 7) * 2 + 1]);
    };
#define GHEAD 3
    constexpr int GH = GHEAD;
    auto ldT1 = [&](bf16x8 (&T)[4], lptr st, int i, int NT) {
      if (NT == 0) T[i] = ldsr128(st + (i >= 2 ? vt1 : vt0) + (i & 1) * 512);
      else T[i] = ldsr128(st + vh + (i & 1) * 2048 + (i >> 1) * 1024);
    };
    auto rest = [&](bf16x8 (&Tc)[4], bf16x8 (&Tn)[4], lptr sn, int NT) {
#pragma unroll
      for (int j = GH; j < 14; ++j) {
        const int k = j - GH;
        mfma_ip(acc[j % 7][0], E[j], Tc[(j / 7) * 2]); SBAR();
        if (NT != 2 && k < 4 + GH) { if (k < 4) ldT1(Tn, sn, k, NT); else ldE(k - 4, sn); SBAR(); }
        mfma_ip(acc[j % 7][1], E[j], Tc[(j / 7) * 2 + 1]); SBAR();
        if (NT != 2) ldE(j, sn); else FILL();
        SBAR();
      }
    };
    auto head = [&](bf16x8 (&Tc)[4]) {
#pragma unroll
      for (int j = 0; j < GH; ++j) { mm(j, Tc); FILL(); }
      SBAR();
    };
#pragma unroll
    for (int u = 0; u < 7; ++u) { acc[u][0] = f32x4{0, 0, 0, 0}; acc[u][1] = f32x4{0, 0, 0, 0}; }
#pragma unroll
    for (int u = 0; u < 7; ++u) { asm volatile("" : "+v"(acc[u][0])); asm volatile("" : "+v"(acc[u][1])); }
    PAD_MFMA_IN();
    if (w != r) {
      const rsrc_t rp = mk_rsrc(ws + WS_PD + (size_t)((tile * 4 + r) * 4 + w) * 7168, 7168);
      const u32x4 sent = {0xffffffffu, 0xffffffffu, 0xffffffffu, 0xffffffffu};
#pragma unroll
      for (int u = 0; u < 7; ++u) {
        __builtin_amdgcn_raw_buffer_store_b128(sent, rp, u * 1024 + lane * 16, 0, 16);
      }
    }
    STAMP(16); STAMPR(28);
    BAR();
    STAMP(17); STAMPR(27);
    ldTy(T0, lds);
#pragma unroll
    for (int j = 0; j < 14; ++j) ldE(j, lds);
    SBAR();
    head(T0);
    WAITL0(); BAR();
    int slot = 1;
#pragma unroll 1
    for (int s = 0; s < P_NY - 2; s += 2) {
      lptr sn = lds + slot * P_STAGE; slot = (slot == 4) ? 0 : slot + 1;
      rest(T0, T1, sn, 0); head(T1);
      WAITL0(); BAR();
      sn = lds + slot * P_STAGE; slot = (slot == 4) ? 0 : slot + 1;
      rest(T1, T0, sn, 0); head(T0);
      WAITL0(); BAR();
      if (YREP > 1 && s == 14) { STAMP(23); }
      if (MODE == 0 && s == 0) {
        WAITV(0);
        if (lane == 0) {
          const unsigned cnt = __hip_atomic_fetch_add((LAS unsigned*)(lds + L_WORDS + 4), 1u, __ATOMIC_RELAXED, __HIP_MEMORY_SCOPE_WORKGROUP);
          if (cnt == 7u) st_flag(ws + WS_FH + (p1_mt * 16 + p1_grp) * 64, get_tag(lds));
        }
      }
    }
    {
      lptr sn = lds + slot * P_STAGE; slot = (slot == 4) ? 0 : slot + 1;
      rest(T0, T1, sn, 0); head(T1);
      WAITL0(); BAR();
      sn = lds + slot * P_STAGE; slot = (slot == 4) ? 0 : slot + 1;
      rest(T1, T0, sn, 1);
    }
    PAD_MFMA_OUT();
    STAMP(18);
    {
      const float sg = (r >> 1) ? -1.0f : 1.0f;
#pragma unroll
      for (int u = 0; u < 7; ++u) { accY[u][0] = acc[u][0] * sg; accY[u][1] = acc[u][1] * sg; acc[u][0] = f32x4{0, 0, 0, 0}; acc[u][1] = f32x4{0, 0, 0, 0}; }
      if (w != r) {
        const rsrc_t rp = mk_rsrc(ws + WS_PY + (size_t)((tile * 4 + r) * 4 + w) * 7168, 7168);
#pragma unroll
        for (int u = 0; u < 7; ++u) {
          const u32x4 o = {pk_bf16(accY[u][0][0], accY[u][0][1]), pk_bf16(accY[u][0][2], accY[u][0][3]),
                           pk_bf16(accY[u][1][0], accY[u][1][1]), pk_bf16(accY[u][1][2], accY[u][1][3])};
          __builtin_amdgcn_raw_buffer_store_b128(o, rp, u * 1024 + lane * 16, 0, 16);
        }
      }
    }
#pragma unroll
    for (int u = 0; u < 7; ++u) { asm volatile("" : "+v"(acc[u][0])); asm volatile("" : "+v"(acc[u][1])); }
    PAD_MFMA_IN();
    head(T0);
    WAITL0(); BAR();
#pragma unroll 1
    for (int s = 0; s < P_ND - 1; s += 2) {
      lptr sn = lds + slot * P_STAGE; slot = (slot == 4) ? 0 : slot + 1;
      rest(T0, T1, sn, 1); head(T1);
      WAITL0(); BAR();
      if (s == 0 && w != r) {
        WAITV(0);
        const unsigned tag = get_tag(lds);
        if (lane == 0) st_flag(ws + WS_FY + ((tile * 4 + r) * 4 + w) * 64, tag);
      }
      sn = lds + slot * P_STAGE; slot = (slot == 4) ? 0 : slot + 1;
      rest(T1, T0, sn, 1); head(T0);
      WAITL0(); if (s + 2 < P_ND - 1) BAR();
    }
    rest(T0, T1, lds, 2);
    PAD_MFMA_OUT();
    STAMP(19); STAMPR(29);
    if (w != r) {
      const rsrc_t rp = mk_rsrc(ws + WS_PD + (size_t)((tile * 4 + r) * 4 + w) * 7168, 7168);
#pragma unroll
      for (int u = 0; u < 7; ++u) {
        u32x4 o = {pk_bf16(acc[u][0][0], acc[u][0][1]), pk_bf16(acc[u][0][2], acc[u][0][3]),
                   pk_bf16(acc[u][1][0], acc[u][1][1]), pk_bf16(acc[u][1][2], acc[u][1][3])};
#pragma unroll
        for (int k = 0; k < 4; ++k) o[k] = o[k] < 0xfffffffeu ? o[k] : 0xfffffffeu;
        __builtin_amdgcn_raw_buffer_store_b128(o, rp, u * 1024 + lane * 16, 0, 16);
      }
    } else {
#pragma unroll
      for (int uu = 0; uu < 7; ++uu) {
        *(LAS f32x4*)(lds + (uu * 4 + 0) * 1024 + lane * 16) = accY[uu][0];
        *(LAS f32x4*)(lds + (uu * 4 + 1) * 1024 + lane * 16) = accY[uu][1];
        *(LAS f32x4*)(lds + (uu * 4 + 2) * 1024 + lane * 16) = acc[uu][0];
        *(LAS f32x4*)(lds + (uu * 4 + 3) * 1024 + lane * 16) = acc[uu][1];
      }
      WAITL0();
    }
    BAR();
  }
  if (wid >= 4) {
    __builtin_amdgcn_s_setprio(0);
    const int hw = wid - 4;
    const int nu = hw < 3 ? 2 : 1;
    const unsigned tag = get_tag(lds);
    const int tok0 = 128 * m + 32 * r + 8 * (i16 >> 2) + (i16 & 3);
    const rsrc_t ry = mk_rsrc(ws + WS_PY, 64 * 16 * 7168), rd = mk_rsrc(ws + WS_PD, 64 * 16 * 7168);
    u32x2 x0[2], x1[2];
    u32x4 fy[2][3], fd[2][3];
#pragma unroll
    for (int k = 0; k < 2; ++k) {
      const int u = 2 * hw + (k < nu ? k : 0);
      const int ecol = 112 * n + 16 * u + 4 * g;
      x0[k] = *(const u32x2*)(a.x + (size_t)tok0 * D + ecol); x1[k] = *(const u32x2*)(a.x + (size_t)(tok0 + 4) * D + ecol);
    }
    {
      const int rr = (r + 1 + (lane % 3)) & 3;
      poll_all(ws + WS_FY + ((tile * 4 + rr) * 4 + r) * 64, lane < 3, tag);
    }
#pragma unroll
    for (int k = 0; k < 2; ++k)
#pragma unroll
      for (int i = 0; i < 3; ++i)
        fy[k][i] = __builtin_amdgcn_raw_buffer_load_b128(ry, ((tile * 4 + ((r + 1 + i) & 3)) * 4 + r) * 7168 + (2 * hw + (k < nu ? k : 0)) * 1024 + lane * 16, 0, 16);
    STAMP(20);
    if (MODE == 0 && blockIdx.x == 0 && hw == 3) {
#pragma unroll 1
      for (int i = 0; i < 4; ++i) poll_all(ws + WS_ARRIVE + (64 * i + lane) * 64, true, tag);
      if (lane == 0) st_flag(ws + WS_NONCE, tag);
    }
    for (unsigned sp = 0; sp < (SPIN_MAX >> 3); ++sp) {
      const unsigned v = lane < 3 ? __builtin_amdgcn_raw_buffer_load_b32(rd, ((tile * 4 + ((r + 1 + lane) & 3)) * 4 + r) * 7168 + 6 * 1024 + 63 * 16 + 12, 0, 16) : 0u;
      if (__all(v != 0xffffffffu)) break;
      __builtin_amdgcn_s_sleep(1);
    }
    for (unsigned sp = 0; sp < (SPIN_MAX >> 3); ++sp) {
      bool ok = true;
#pragma unroll
      for (int k = 0; k < 2; ++k)
#pragma unroll
        for (int i = 0; i < 3; ++i) {
          fd[k][i] = __builtin_amdgcn_raw_buffer_load_b128(rd, ((tile * 4 + ((r + 1 + i) & 3)) * 4 + r) * 7168 + (2 * hw + (k < nu ? k : 0)) * 1024 + lane * 16, 0, 16);
        }
#pragma unroll
      for (int k = 0; k < 2; ++k)
#pragma unroll
        for (int i = 0; i < 3; ++i)
          ok = ok && fd[k][i][0] != 0xffffffffu && fd[k][i][1] != 0xffffffffu && fd[k][i][2] != 0xffffffffu && fd[k][i][3] != 0xffffffffu;
      if (__all(ok)) break;
      __builtin_amdgcn_s_sleep(2);
    }
    STAMP(21); STAMPR(29);
    BAR();
#pragma unroll
    for (int k = 0; k < 2; ++k) {
      if (k < nu) {
        const int u = 2 * hw + k;
        const int ecol = 112 * n + 16 * u + 4 * g;
        f32x4 y0 = *(LAS f32x4*)(lds + (u * 4 + 0) * 1024 + lane * 16), y1 = *(LAS f32x4*)(lds + (u * 4 + 1) * 1024 + lane * 16);
        f32x4 d0 = *(LAS f32x4*)(lds + (u * 4 + 2) * 1024 + lane * 16), d1 = *(LAS f32x4*)(lds + (u * 4 + 3) * 1024 + lane * 16);
#pragma unroll
        for (int i = 0; i < 3; ++i) {
          y0 += f32x4{bf_lo(fy[k][i][0]), bf_hi(fy[k][i][0]), bf_lo(fy[k][i][1]), bf_hi(fy[k][i][1])};
          y1 += f32x4{bf_lo(fy[k][i][2]), bf_hi(fy[k][i][2]), bf_lo(fy[k][i][3]), bf_hi(fy[k][i][3])};
          d0 += f32x4{bf_lo(fd[k][i][0]), bf_hi(fd[k][i][0]), bf_lo(fd[k][i][1]), bf_hi(fd[k][i][1])};
          d1 += f32x4{bf_lo(fd[k][i][2]), bf_hi(fd[k][i][2]), bf_lo(fd[k][i][3]), bf_hi(fd[k][i][3])};
        }
        const u32x2 o0 = {pk_bf16(bf_lo(x0[k][0]) + y0[0] * silu_f(d0[0]), bf_hi(x0[k][0]) + y0[1] * silu_f(d0[1])),
                          pk_bf16(bf_lo(x0[k][1]) + y0[2] * silu_f(d0[2]), bf_hi(x0[k][1]) + y0[3] * silu_f(d0[3]))};
        const u32x2 o1 = {pk_bf16(bf_lo(x1[k][0]) + y1[0] * silu_f(d1[0]), bf_hi(x1[k][0]) + y1[1] * silu_f(d1[1])),
                          pk_bf16(bf_lo(x1[k][1]) + y1[2] * silu_f(d1[2]), bf_hi(x1[k][1]) + y1[3] * silu_f(d1[3]))};
        *(u32x2*)(a.out + (size_t)tok0 * D + ecol) = o0;
        *(u32x2*)(a.out + (size_t)(tok0 + 4) * D + ecol) = o1;
      }
    }
  }
  WAITV(0);
  while (__builtin_amdgcn_s_memtime() - t0_ < 60000ull) { asm volatile("v_nop\n\tv_nop\n\tv_nop\n\tv_nop"); }
}

#define SPLIT 0
extern "C" void kernel_launch(void* const* d_in, const int* in_sizes, int n_in, void* d_out, int out_size, void* d_ws, size_t ws_size,
                              hipStream_t stream) {
  Args a;
  a.x = (const u16*)d_in[0]; a.Cr = (const u16*)d_in[1]; a.Ci = (const u16*)d_in[2]; a.Dr = (const u16*)d_in[3]; a.Di = (const u16*)d_in[4];
  a.Wg = (const u16*)d_in[6]; a.Wu = (const u16*)d_in[7]; a.Wd = (const u16*)d_in[8];
  a.ws = (char*)d_ws; a.out = (u16*)d_out;
  (void)hipFuncSetAttribute((const void*)fused_kernel<0>, hipFuncAttributeMaxDynamicSharedMemorySize, L_TOTAL);
  fused_kernel<0><<<256, 512, L_TOTAL, stream>>>(a);
}
```

```cpp
#include <hip/hip_runtime.h>
#include <hip/hip_bf16.h>
#include <stdint.h>

typedef unsigned short u16;
typedef __bf16 bf16x8 __attribute__((ext_vector_type(8)));
typedef short s16x8 __attribute__((ext_vector_type(8)));
typedef float f32x4 __attribute__((ext_vector_type(4)));
typedef float f32x2 __attribute__((ext_vector_type(2)));
typedef unsigned u32x4 __attribute__((ext_vector_type(4)));
typedef unsigned u32x2 __attribute__((ext_vector_type(2)));
#define LAS __attribute__((address_space(3)))
#define GAS __attribute__((address_space(1)))
typedef LAS char* lptr;
typedef __amdgpu_buffer_rsrc_t rsrc_t;

constexpr int S = 2048, D = 448, F = 1792;

constexpr size_t WS_NONCE  = 0;
constexpr size_t WS_ARRIVE = 4096;
constexpr size_t WS_FAB    = 32768;
constexpr size_t WS_FH     = 49152;
constexpr size_t WS_FWD    = 65536;
constexpr size_t WS_FY     = 81920;
constexpr size_t WS_FD     = 147456;
constexpr size_t WS_ST     = 212992;
constexpr size_t WS_CW     = 229376;
constexpr size_t WS_CD     = 286720;
constexpr size_t WS_AB     = 327680;
constexpr size_t WS_H      = WS_AB + (size_t)2 * 28 * 64 * 1024;
constexpr size_t WS_WDT    = WS_H + (size_t)128 * 56 * 1024;
constexpr size_t WS_PY     = WS_WDT + (size_t)28 * 56 * 1024;
constexpr size_t WS_PD     = WS_PY + (size_t)64 * 16 * 7 * 1024;

#define STAMP(i)
#define STAMPR(i)
#define SBAR() __builtin_amdgcn_sched_barrier(0)
#define BAR() __builtin_amdgcn_s_barrier()
#define WAITV(n) asm volatile("s_waitcnt vmcnt(%0)" ::"n"(n) : "memory")
#define WAITL0() __builtin_amdgcn_s_waitcnt(0xC07F)
#define CODE_PF 49152
#define DFTPF 0
#define PF0 1
#define STAUX 16
#define SPIN_MAX (1u << 18)

__device__ __forceinline__ rsrc_t mk_rsrc(const void* p, unsigned bytes) {
  return __builtin_amdgcn_make_buffer_rsrc((void*)p, 0, bytes, 0x00020000);
}
__device__ __forceinline__ u32x4 mk_srd(const void* p, unsigned bytes) {
  const unsigned long long a = (unsigned long long)p;
  return u32x4{(unsigned)a, (unsigned)(a >> 32) & 0xffffu, bytes, 0x00020000u};
}
__device__ __forceinline__ unsigned lds_addr(lptr p) { return (unsigned)(size_t)p; }
__device__ __forceinline__ void dma16(const u32x4& srd, unsigned dst, int voff, int soff) {
  asm volatile("s_mov_b32 m0, %0\n\ts_nop 0\n\tbuffer_load_dwordx4 %1, %2, %3 offen lds" ::"s"(dst), "v"(voff), "s"(srd), "s"(soff) : "memory");
}
__device__ __forceinline__ void dma16c(const u32x4& srd, unsigned dst, int voff, int soff) {
  asm volatile("s_mov_b32 m0, %0\n\ts_nop 0\n\tbuffer_load_dwordx4 %1, %2, %3 offen sc1 lds" ::"s"(dst), "v"(voff), "s"(srd), "s"(soff) : "memory");
}
__device__ __forceinline__ void pf4c(const char* p, unsigned dst) {
  asm volatile("s_mov_b32 m0, %0\n\ts_nop 0\n\tglobal_load_lds_dword %1, off sc1" ::"s"(dst), "v"(p) : "memory");
}
__device__ __forceinline__ void pf4(const char* p, unsigned dst) {
  asm volatile("s_mov_b32 m0, %0\n\ts_nop 0\n\tglobal_load_lds_dword %1, off" ::"s"(dst), "v"(p) : "memory");
}
__device__ __forceinline__ bf16x8 ldsr128(lptr p) { return *(const LAS bf16x8*)p; }
__device__ __forceinline__ bf16x8 ldstr(lptr p0, lptr p1) {
  typedef short v4i16_t __attribute__((ext_vector_type(4)));
  v4i16_t lo = __builtin_amdgcn_ds_read_tr16_b64_v4i16((LAS v4i16_t*)p0);
  v4i16_t hi = __builtin_amdgcn_ds_read_tr16_b64_v4i16((LAS v4i16_t*)p1);
  s16x8 v = __builtin_shufflevector(lo, hi, 0, 1, 2, 3, 4, 5, 6, 7);
  return __builtin_bit_cast(bf16x8, v);
}
__device__ __forceinline__ f32x4 mfma(bf16x8 a, bf16x8 b, f32x4 c) { return __builtin_amdgcn_mfma_f32_16x16x32_bf16(a, b, c, 0, 0, 0); }
__device__ __forceinline__ void mfma_ip(f32x4& c, const bf16x8& a, const bf16x8& b) {
  asm volatile("v_mfma_f32_16x16x32_bf16 %0, %1, %2, %0" : "+v"(c) : "v"(a), "v"(b));
}
#define FILL() asm volatile("s_nop 0")
#define PAD_MFMA_OUT() asm volatile("s_nop 7\n\ts_nop 3")
#define PAD_MFMA_IN() asm volatile("s_nop 3")
__device__ __forceinline__ unsigned pk_bf16(float lo, float hi) {
  typedef __bf16 bf16x2 __attribute__((ext_vector_type(2)));
  f32x2 v = {lo, hi};
  bf16x2 b = __builtin_convertvector(v, bf16x2);
  return __builtin_bit_cast(unsigned, b);
}
__device__ __forceinline__ float bf_lo(unsigned v) { return __uint_as_float(v << 16); }
__device__ __forceinline__ float bf_hi(unsigned v) { return __uint_as_float(v & 0xffff0000u); }
__device__ __forceinline__ float rbf(float f) { return bf_lo(pk_bf16(f, 0.f)); }
__device__ __forceinline__ float silu_f(float x) {
  float e = __builtin_amdgcn_exp2f(x * -1.4426950409f);
  return x * __builtin_amdgcn_rcpf(1.0f + e);
}
template <int N>
__device__ __forceinline__ void wave_sums(float (&v)[N]) {
#define DPP_STEP(ctrl, rmask) _Pragma("unroll") for (int i = 0; i < N; ++i) \
    v[i] += __builtin_bit_cast(float, __builtin_amdgcn_update_dpp(0, __builtin_bit_cast(int, v[i]), ctrl, rmask, 0xf, true));
  DPP_STEP(0x111, 0xf)
  DPP_STEP(0x112, 0xf)
  DPP_STEP(0x114, 0xf)
  DPP_STEP(0x118, 0xf)
  DPP_STEP(0x142, 0xa)
  DPP_STEP(0x143, 0xc)
#undef DPP_STEP
#pragma unroll
  for (int i = 0; i < N; ++i) v[i] = __builtin_bit_cast(float, __builtin_amdgcn_readlane(__builtin_bit_cast(int, v[i]), 63));
}
__device__ __forceinline__ unsigned ld_flag(const void* p) { return __hip_atomic_load((const GAS unsigned*)p, __ATOMIC_RELAXED, __HIP_MEMORY_SCOPE_AGENT); }
__device__ __forceinline__ void st_flag(void* p, unsigned v) { __hip_atomic_store((GAS unsigned*)p, v, __ATOMIC_RELAXED, __HIP_MEMORY_SCOPE_AGENT); }
__device__ __forceinline__ unsigned get_tag(lptr lds);
__device__ __forceinline__ void poll_all(const char* p, bool active, unsigned tag) {
  for (unsigned sp = 0; sp < SPIN_MAX; ++sp) {
    const unsigned v = active ? ld_flag(p) : tag;
    if (__all(v == tag)) break;
    __builtin_amdgcn_s_sleep(2);
  }
}
__device__ __forceinline__ void lds_spin(lptr p) {
  for (unsigned sp = 0; sp < SPIN_MAX * 4; ++sp) {
    if (*(volatile LAS unsigned*)p != 0u) break;
    __builtin_amdgcn_s_sleep(2);
  }
}

constexpr int K1_STAGE = 53248, K1_NST = 7, K1_WO = 16384;
constexpr int L_MISC = 3 * K1_STAGE;
constexpr int L_STATS = L_MISC;
constexpr int L_CS = L_MISC + 1024;
constexpr int L_WORDS = L_CS + 18 * 64;
constexpr int L_DUMMY = L_WORDS + 128;
constexpr int L_TOTAL = L_DUMMY + 512;
#define YREP 1
constexpr int P_STAGE = 30720, P_NY = 16 * YREP, P_ND = 7, P_NST = P_NY + P_ND;
static_assert(L_TOTAL <= 163840 && 5 * P_STAGE <= L_MISC, "LDS");

__device__ __forceinline__ unsigned get_tag(lptr lds) { return *(volatile LAS unsigned*)(lds + L_WORDS + 16); }

struct Args {
  const u16 *x, *Cr, *Ci, *Dr, *Di, *Wg, *Wu, *Wd;
  char* ws;
  u16* out;
};

__device__ __forceinline__ void launder(Args& a, int& xcd, int& idx) {
  asm volatile("" : "+s"(a.x), "+s"(a.Cr), "+s"(a.Ci), "+s"(a.Dr), "+s"(a.Di));
  asm volatile("" : "+s"(a.Wg), "+s"(a.Wu), "+s"(a.Wd), "+s"(a.ws), "+s"(a.out));
  asm volatile("" : "+s"(xcd), "+s"(idx));
}

struct RowJobs {
  u32x4 v[4];
  float f[2][8], sm[4];
  __device__ __forceinline__ void load(const Args& a, int nq, int idx, int mt, int grp, int h, int lane) {
    const int l56 = lane < 56 ? lane : 55;
    const u16* sp = a.x + (size_t)(mt * 128 + 8 * grp + 2 * h) * D + l56 * 8;
    const u16* dp = (nq >= 2 ? a.Di : a.Dr) + (size_t)(224 * (nq & 1) + 7 * idx + h) * D + l56 * 8;
    const u16* dq = dp + (h < 3 ? 4 * D : 0);
    asm volatile("global_load_dwordx4 %0, %4, off\n\tglobal_load_dwordx4 %1, %4, off offset:896\n\tglobal_load_dwordx4 %2, %5, off\n\tglobal_load_dwordx4 %3, %6, off"
                 : "=&v"(v[0]), "=&v"(v[1]), "=&v"(v[2]), "=&v"(v[3]) : "v"(sp), "v"(dp), "v"(dq) : "memory");
  }
  __device__ __forceinline__ void landed() { asm volatile("" : "+v"(v[0]), "+v"(v[1]), "+v"(v[2]), "+v"(v[3])); }
  __device__ __forceinline__ void sums(int lane) {
#pragma unroll
    for (int r = 0; r < 4; ++r) {
      float t[8];
#pragma unroll
      for (int i = 0; i < 4; ++i) { t[2 * i] = bf_lo(v[r][i]); t[2 * i + 1] = bf_hi(v[r][i]); }
      const float s = ((t[0] + t[1]) + (t[2] + t[3])) + ((t[4] + t[5]) + (t[6] + t[7]));
      sm[r] = lane < 56 ? s : 0.f;
      if (r < 2) {
#pragma unroll
        for (int i = 0; i < 8; ++i) f[r][i] = t[i];
      }
    }
    wave_sums<4>(sm);
  }
  __device__ __forceinline__ void finish(char* ws, unsigned tag, int nq, int idx, int mt, int grp, int h, int lane) {
    float q[2], mean[2];
#pragma unroll
    for (int r = 0; r < 2; ++r) {
      mean[r] = sm[r] * (1.0f / D);
      float t0 = 0.f, t1 = 0.f;
#pragma unroll
      for (int i = 0; i < 4; ++i) {
        const float d0 = f[r][2 * i] - mean[r], d1 = f[r][2 * i + 1] - mean[r];
        t0 = __builtin_fmaf(d0, d0, t0); t1 = __builtin_fmaf(d1, d1, t1);
      }
      q[r] = lane < 56 ? t0 + t1 : 0.f;
    }
    wave_sums<2>(q);
    const int r = lane & 1;
    const unsigned pay = pk_bf16(r ? mean[1] : mean[0], rbf(__builtin_amdgcn_sqrtf(rbf(rbf((r ? q[1] : q[0]) * (1.0f / D)) + 1e-5f))));
    if (lane < 2 || lane == 2 || (lane == 3 && h < 3)) {
      const unsigned v_ = lane < 2 ? pay : __float_as_uint(lane == 2 ? sm[2] : sm[3]);
      const size_t off = lane < 2 ? WS_ST + (size_t)(mt * 128 + 8 * grp + 2 * h + lane) * 8 : WS_CD + (size_t)(224 * nq + 7 * idx + h + 4 * (lane - 2)) * 8;
      __hip_atomic_store((GAS unsigned long long*)(ws + off), ((unsigned long long)tag << 32) | v_, __ATOMIC_RELAXED, __HIP_MEMORY_SCOPE_AGENT);
    }
  }
};

__device__ __forceinline__ void h_batch(const f32x4 (&g)[2][2], const f32x4 (&u)[2][2], const f32x4 (&cg)[2], const f32x4 (&cu)[2], const f32x2 (&st)[2],
                                        rsrc_t rh, int off0) {
  f32x2 gg[8], uu[8], z[8];
#pragma unroll
  for (int i = 0; i < 8; ++i) {
    const int t = i >> 2, j = (i >> 1) & 1, hh = i & 1;
    gg[i] = (f32x2{g[j][t][2 * hh], g[j][t][2 * hh + 1]} - f32x2{cg[j][2 * hh], cg[j][2 * hh + 1]} * st[t].x) * st[t].y;
  }
#pragma unroll
  for (int i = 0; i < 8; ++i) z[i] = gg[i] * ((gg[i] * gg[i]) * -0.1029432397f + -2.302208198f);
#pragma unroll
  for (int i = 0; i < 8; ++i) { z[i].x = __builtin_amdgcn_exp2f(z[i].x); z[i].y = __builtin_amdgcn_exp2f(z[i].y); }
#pragma unroll
  for (int i = 0; i < 8; ++i) {
    const int t = i >> 2, j = (i >> 1) & 1, hh = i & 1;
    uu[i] = ((f32x2{u[j][t][2 * hh], u[j][t][2 * hh + 1]} - f32x2{cu[j][2 * hh], cu[j][2 * hh + 1]} * st[t].x) * st[t].y) * gg[i];
    z[i] = z[i] + 1.0f;
  }
#pragma unroll
  for (int i = 0; i < 8; ++i) { z[i].x = __builtin_amdgcn_rcpf(z[i].x); z[i].y = __builtin_amdgcn_rcpf(z[i].y); }
#pragma unroll
  for (int i = 0; i < 8; ++i) uu[i] = uu[i] * z[i];
#pragma unroll
  for (int tl = 0; tl < 2; ++tl) {
    const u32x4 o = {pk_bf16(uu[4 * tl].x, uu[4 * tl].y), pk_bf16(uu[4 * tl + 1].x, uu[4 * tl + 1].y),
                     pk_bf16(uu[4 * tl + 2].x, uu[4 * tl + 2].y), pk_bf16(uu[4 * tl + 3].x, uu[4 * tl + 3].y)};
    __builtin_amdgcn_raw_buffer_store_b128(o, rh, off0 + tl * 56 * 1024, 0, STAUX);
  }
}
constexpr int L_HD = 61440;

template <int MODE, int NG, int NA>
__device__ __forceinline__ void p1_consumer(lptr lds, Args a, int xcd, int idx, int mt, int grp, int f0, int a0, int wm, int wn, int lane) {
  launder(a, xcd, idx);
  asm volatile("" : "+s"(mt), "+s"(grp), "+s"(f0), "+s"(a0));
  char* ws = a.ws;
  const char* cp = (const char*)((xcd >> 2) ? a.Ci : a.Cr) + (size_t)(1024 * (xcd & 1) + 32 * idx + 8 * (2 * wn + wm) + (lane >> 5)) * 4096 + 2048 * ((xcd >> 1) & 1) + (lane & 31) * 64;
  unsigned nonce = 0;
  if (MODE == 0 && wm == 0 && wn == 0) asm volatile("global_load_dword %0, %1, off sc1" : "+v"(nonce) : "v"(ws + WS_NONCE) : "memory");
  constexpr int NBK = 2 * NG, NCQ = NBK / 4, WSZ = NBK * 8 * 256, KSO = 4 * NCQ * 1024;
  constexpr int GO = K1_WO, UO = K1_WO + WSZ, DO = K1_WO + 2 * WSZ;
  const int i16 = lane & 15, g = lane >> 4, q = i16 >> 2, p = i16 & 3;
  const int rowx = 64 * wm + 8 * (i16 >> 2) + (i16 & 3);
  const int vx0 = rowx * 128 + ((g) ^ (i16 & 7)) * 16;
  const int vx1 = rowx * 128 + ((g + 4) ^ (i16 & 7)) * 16;
  const int fw = ((q >> 1) & 1) | ((g & 1) << 1);
  int vw[NG];
#pragma unroll
  for (int j = 0; j < NG; ++j) { const int nb = NG * wn + j; vw[j] = g * NCQ * 1024 + (nb >> 2) * 1024 + q * 128 + (((nb & 3) ^ fw) * 32) + p * 8; }
  const int vd0 = DO + (16 * NA * wn + i16) * 128 + ((g) ^ (i16 & 7)) * 16;
  const int vd1 = DO + (16 * NA * wn + i16) * 128 + ((g + 4) ^ (i16 & 7)) * 16;

  f32x4 G[NG][4], U[NG][4], AB[NA][4];
#pragma unroll
  for (int j = 0; j < NG; ++j)
#pragma unroll
    for (int t = 0; t < 4; ++t) { G[j][t] = f32x4{0, 0, 0, 0}; U[j][t] = f32x4{0, 0, 0, 0}; }
#pragma unroll
  for (int j = 0; j < NA; ++j)
#pragma unroll
    for (int t = 0; t < 4; ++t) AB[j][t] = f32x4{0, 0, 0, 0};

  bf16x8 X0[4], X1[4], WG[NG], WU[NG], WD[NA];
  auto ldx = [&](bf16x8 (&X)[4], lptr st, int ks) {
#pragma unroll
    for (int t = 0; t < 4; ++t) X[t] = ldsr128(st + (ks ? vx1 : vx0) + (32 * (t >> 1) + 4 * (t & 1)) * 128);
  };
  auto ldg = [&](int j, lptr st, int ks) { lptr b = st + GO + ks * KSO + vw[j]; WG[j] = ldstr(b, b + 512); };
  auto ldu = [&](int j, lptr st, int ks) { lptr b = st + UO + ks * KSO + vw[j]; WU[j] = ldstr(b, b + 512); };
  auto ldd = [&](int j, lptr st, int ks) { WD[j] = ldsr128(st + (ks ? vd1 : vd0) + j * 16 * 128); };
  auto grp4 = [&](f32x4 (&C)[4], const bf16x8& W, bf16x8 (&X)[4], bool wfirst, auto mid) {
    if (wfirst) { mfma_ip(C[0], W, X[0]); mfma_ip(C[1], W, X[1]); } else { mfma_ip(C[0], X[0], W); mfma_ip(C[1], X[1], W); }
    SBAR(); mid(); SBAR();
    if (wfirst) { mfma_ip(C[2], W, X[2]); mfma_ip(C[3], W, X[3]); } else { mfma_ip(C[2], X[2], W); mfma_ip(C[3], X[3], W); }
    SBAR();
  };
  auto fill = [&]() { FILL(); };
  auto ldx1 = [&](bf16x8 (&X)[4], lptr st, int ks, int t) { X[t] = ldsr128(st + (ks ? vx1 : vx0) + (32 * (t >> 1) + 4 * (t & 1)) * 128); };
  auto blockA = [&](lptr st) {
#pragma unroll
    for (int j = 0; j < NG; ++j) {
      grp4(G[j], WG[j], X0, true, [&]() { if (2 * j < 4) ldx1(X1, st, 1, 2 * j); else FILL(); }); ldg(j, st, 1); SBAR();
      grp4(U[j], WU[j], X0, true, [&]() { if (2 * j + 1 < 4) ldx1(X1, st, 1, 2 * j + 1); else FILL(); }); ldu(j, st, 1); SBAR();
    }
#pragma unroll
    for (int j = 0; j < NA; ++j) { grp4(AB[j], WD[j], X0, false, fill); ldd(j, st, 1); SBAR(); }
  };
  auto blockB_head = [&]() { grp4(G[0], WG[0], X1, true, fill); FILL(); grp4(U[0], WU[0], X1, true, fill); };
  auto blockB_rest = [&](lptr sn, bool reload) {
    int k = 0;
    auto mid = [&]() {
      if (!reload) FILL();
      else if (k == 0) ldg(0, sn, 0);
      else if (k == 1) ldu(0, sn, 0);
      else if (k < 6) ldx1(X0, sn, 0, k - 2);
      else FILL();
      ++k;
    };
#pragma unroll
    for (int j = 1; j < NG; ++j) {
      grp4(G[j], WG[j], X1, true, mid); if (reload) { ldg(j, sn, 0); SBAR(); } else FILL();
      grp4(U[j], WU[j], X1, true, mid); if (reload) { ldu(j, sn, 0); SBAR(); } else FILL();
    }
#pragma unroll
    for (int j = 0; j < NA; ++j) { grp4(AB[j], WD[j], X1, false, mid); if (reload) { ldd(j, sn, 0); SBAR(); } else FILL(); }
  };

#pragma unroll
  for (int j = 0; j < NG; ++j)
#pragma unroll
    for (int t = 0; t < 4; ++t) { asm volatile("" : "+v"(G[j][t])); asm volatile("" : "+v"(U[j][t])); }
#pragma unroll
  for (int j = 0; j < NA; ++j)
#pragma unroll
    for (int t = 0; t < 4; ++t) asm volatile("" : "+v"(AB[j][t]));
  PAD_MFMA_IN();
  STAMP(1);
  BAR();
  STAMP(2);
  ldg(0, lds, 0); ldu(0, lds, 0); ldx(X0, lds, 0);
#pragma unroll
  for (int j = 1; j < NG; ++j) { ldg(j, lds, 0); ldu(j, lds, 0); }
#pragma unroll
  for (int j = 0; j < NA; ++j) ldd(j, lds, 0);
  SBAR();
  int slot = 0;
#pragma unroll 1
  for (int s = 0; s < K1_NST; ++s) {
    blockA(lds + slot * K1_STAGE);
    if (s >= PF0 && s < PF0 + 4) { pf4(cp, lds_addr(lds) + L_DUMMY); cp += 2 * 4096; }
    blockB_head();
    if (s == 0 && wm == 0 && wn == 0) {
      if (PF0 == 0) { WAITV(1); } else { WAITV(0); }
      asm volatile("" : "+v"(nonce));
      if (lane == 0) *(volatile LAS unsigned*)(lds + L_WORDS + 16) = (MODE == 0) ? nonce * 0x9E3779B1u + 0x7F4A7C15u : 0x51u;
    }
    WAITL0();
    STAMP(19 + s);
    BAR();
    STAMP(7 + s);
    slot = (slot == 2) ? 0 : slot + 1;
    blockB_rest(lds + slot * K1_STAGE, true);
  }
  PAD_MFMA_OUT();
  STAMP(3);
  const lptr cs = lds + L_CS;
  const lptr stats = lds + L_STATS;
  {
    const rsrc_t rab = mk_rsrc(ws + WS_AB, 2 * 28 * 64 * 1024);
#pragma unroll
    for (int pi = 0; pi < 2; ++pi) {
      const lptr sp = stats + (64 * wm + 32 * pi + 8 * g) * 8;
      const f32x4 s0 = *(LAS f32x4*)(sp), s1 = *(LAS f32x4*)(sp + 16), s2 = *(LAS f32x4*)(sp + 32), s3 = *(LAS f32x4*)(sp + 48);
#pragma unroll
      for (int j = 0; j < NA; ++j) {
        const float c = *(LAS float*)(cs + (2 * NBK + NA * wn + j) * 64 + i16 * 4);
        const f32x4 a_ = AB[j][2 * pi], b = AB[j][2 * pi + 1];
        const int ks = mt * 4 + wm * 2 + pi;
        u32x4 o;
        o[0] = pk_bf16((a_[0] - s0[0] * c) * s0[1], (a_[1] - s0[2] * c) * s0[3]);
        o[1] = pk_bf16((a_[2] - s1[0] * c) * s1[1], (a_[3] - s1[2] * c) * s1[3]);
        o[2] = pk_bf16((b[0] - s2[0] * c) * s2[1], (b[1] - s2[2] * c) * s2[3]);
        o[3] = pk_bf16((b[2] - s3[0] * c) * s3[1], (b[3] - s3[2] * c) * s3[3]);
        __builtin_amdgcn_raw_buffer_store_b128(o, rab, lane * 16 + ((a0 + NA * wn + j) * 64 + ks) * 1024, 0, STAUX);
      }
    }
  }
  {
    lptr hd = lds + L_HD + (2 * wn + wm) * (NG * 4096) + lane * 16;
#pragma unroll
    for (int j = 0; j < NG; ++j)
#pragma unroll
      for (int tl = 0; tl < 2; ++tl) {
        *(LAS f32x4*)(hd + ((j * 2 + 0) * 2 + tl) * 1024) = G[j][2 + tl];
        *(LAS f32x4*)(hd + ((j * 2 + 1) * 2 + tl) * 1024) = U[j][2 + tl];
      }
  }
  WAITV(0);
  if (lane == 0) {
    const unsigned n = __hip_atomic_fetch_add((LAS unsigned*)(lds + L_WORDS), 1u, __ATOMIC_RELAXED, __HIP_MEMORY_SCOPE_WORKGROUP);
    if (n == 3u) st_flag(ws + WS_FAB + (mt * 16 + grp) * 64, get_tag(lds));
  }
  STAMP(5);
  WAITL0(); BAR();
  STAMP(14);
  const rsrc_t rh = mk_rsrc(ws + WS_H, 128 * 56 * 1024);
  const int tt0 = mt * 8 + wm * 4;
  f32x2 st[2];
#pragma unroll
  for (int t = 0; t < 2; ++t) st[t] = *(LAS f32x2*)(stats + (64 * wm + 8 * (i16 >> 2) + 4 * t + (i16 & 3)) * 8);
#pragma unroll
  for (int pp = 0; pp < NG / 2; ++pp) {
    const int ks = ((f0 >> 4) + NG * wn + 2 * pp) >> 1;
    f32x4 cg[2], cu[2], gq[2][2], uq[2][2];
#pragma unroll
    for (int j = 0; j < 2; ++j) {
      cg[j] = *(LAS f32x4*)(cs + (NG * wn + 2 * pp + j) * 64 + g * 16);
      cu[j] = *(LAS f32x4*)(cs + (NBK + NG * wn + 2 * pp + j) * 64 + g * 16);
      gq[j][0] = G[2 * pp + j][0]; gq[j][1] = G[2 * pp + j][1]; uq[j][0] = U[2 * pp + j][0]; uq[j][1] = U[2 * pp + j][1];
    }
    h_batch(gq, uq, cg, cu, st, rh, lane * 16 + (tt0 * 56 + ks) * 1024);
  }
  STAMP(4);
}

template <int NG>
__device__ __forceinline__ void p1_helper_epilogue(lptr lds, char* ws, int mt, int f0, int h, int lane) {
  constexpr int NBK = 2 * NG;
  const int wm = h & 1, wn = h >> 1, i16 = lane & 15, g = lane >> 4;
  const lptr cs = lds + L_CS, stats = lds + L_STATS;
  const rsrc_t rh = mk_rsrc(ws + WS_H, 128 * 56 * 1024);
  const int tt0 = mt * 8 + wm * 4 + 2;
  lptr hd = lds + L_HD + h * (NG * 4096) + lane * 16;
  f32x2 st[2];
#pragma unroll
  for (int t = 0; t < 2; ++t) st[t] = *(LAS f32x2*)(stats + (64 * wm + 32 + 8 * (i16 >> 2) + 4 * t + (i16 & 3)) * 8);
#pragma unroll
  for (int pp = 0; pp < NG / 2; ++pp) {
    const int ks = ((f0 >> 4) + NG * wn + 2 * pp) >> 1;
    f32x4 cg[2], cu[2], gq[2][2], uq[2][2];
#pragma unroll
    for (int j = 0; j < 2; ++j) {
      cg[j] = *(LAS f32x4*)(cs + (NG * wn + 2 * pp + j) * 64 + g * 16);
      cu[j] = *(LAS f32x4*)(cs + (NBK + NG * wn + 2 * pp + j) * 64 + g * 16);
#pragma unroll
      for (int tl = 0; tl < 2; ++tl) {
        gq[j][tl] = *(LAS f32x4*)(hd + (((2 * pp + j) * 2 + 0) * 2 + tl) * 1024);
        uq[j][tl] = *(LAS f32x4*)(hd + (((2 * pp + j) * 2 + 1) * 2 + tl) * 1024);
      }
    }
    h_batch(gq, uq, cg, cu, st, rh, lane * 16 + (tt0 * 56 + ks) * 1024);
  }
}

template <int MODE, bool TB>
__device__ __forceinline__ void p1_helper(lptr lds, Args a, int xcd, int idx, int mt, int grp, int f0, int a0, int h, int lane) {
  constexpr int NBK = TB ? 4 : 8, WSZ = NBK * 8 * 256;
  constexpr int NWP = TB ? 4 : 8, NDP = TB ? 4 : 1, PER = 4 + NWP + NDP;
  const int mh = xcd & 1, nq = xcd >> 1;
  const int up = h >> 1;
  const u32x4 rx = mk_srd(a.x, S * D * 2);
  const u32x4 rw = mk_srd(up ? a.Wu : a.Wg, D * F * 2);
  const u32x4 rd = mk_srd(a0 >= 28 ? a.Di : a.Dr, D * D * 2);
  const int e0 = (a0 >= 28 ? a0 - 28 : a0) * 16;
  const int r8 = lane >> 3, c8 = lane & 7;
  int vx[2];
  vx[0] = r8 * 896 + (c8 ^ (r8 & 3)) * 16;
  vx[1] = r8 * 896 + (c8 ^ ((r8 & 3) | 4)) * 16;
  const int vd = r8 * 896 + (c8 ^ r8) * 16;
  int vw[2];
  {
    const int f0_ = (r8 >> 1) & 1;
    vw[0] = r8 * (F * 2) + f0 * 2 + ((((c8 >> 1) ^ f0_) * 2) + (c8 & 1)) * 16;
    vw[1] = r8 * (F * 2) + f0 * 2 + ((((c8 >> 1) ^ (f0_ | 2)) * 2) + (c8 & 1)) * 16;
  }
  const unsigned l0 = lds_addr(lds);
  const unsigned dummy = l0 + L_DUMMY + (h & 1) * 256;
  const int sxb = (mt * 128 + 32 * h) * 896, swb = 32 * (h & 1) * (F * 2), sdb = (e0 + 8 * NDP * h) * 896;
  const unsigned lxb = l0 + 4 * h * 1024, lwb = l0 + K1_WO + up * WSZ + NWP * (h & 1) * 1024, ldb = l0 + K1_WO + 2 * WSZ + NDP * h * 1024;
  auto piece = [&](int s, int slot, int i) {
    const unsigned so = slot * K1_STAGE;
    if (i < 4) dma16(rx, lxb + so + i * 1024, vx[i & 1], sxb + s * 128 + i * 8 * 896);
    else if (i < 4 + NWP) {
      const int j = i - 4, kb = TB ? j : j >> 1, cq = TB ? 0 : j & 1;
      dma16(rw, lwb + so + j * 1024, vw[kb & 1], swb + s * 64 * (F * 2) + kb * 8 * (F * 2) + cq * 128);
    } else dma16(rd, ldb + so + (i - 4 - NWP) * 1024, vd, sdb + s * 128 + (i - 4 - NWP) * 8 * 896);
  };
  auto issue_a = [&](int s, int slot) {
#pragma unroll
    for (int i = 0; i < 8; ++i) piece(s, slot, i);
  };
  auto issue_b = [&](int s, int slot) {
#pragma unroll
    for (int i = 8; i < PER; ++i) piece(s, slot, i);
  };
  const int ln2 = lane >> 1, hb = (lane & 1) * 64;
  const char* pfx = (const char*)a.x + (size_t)(1024 * mh + 32 * idx + ln2) * 896 + hb;
  STAMP(1);
  issue_a(0, 0); issue_b(0, 0);
  STAMP(5);
  launder(a, xcd, idx);
  char* ws = a.ws;
  const bool wjob = idx < 28;
  const int wj_mat = idx >= 14, wj_cb = (idx - 14 * wj_mat) >> 1, wj_kh = idx & 1;
  u32x4 wx[7];
  {
    const u32x4 rj = mk_srd(wj_mat ? a.Wu : a.Wg, D * F * 2);
    const int vj = r8 * (F * 2) + c8 * 16 + (448 * nq + 64 * (wjob ? wj_cb : 0)) * 2;
    const int sj = (224 * (wjob ? wj_kh : 0) + 56 * h) * (F * 2);
    asm volatile("buffer_load_dwordx4 %0, %7, %8, %9 offen\n\tbuffer_load_dwordx4 %1, %7, %8, %10 offen\n\tbuffer_load_dwordx4 %2, %7, %8, %11 offen\n\t"
                 "buffer_load_dwordx4 %3, %7, %8, %12 offen\n\tbuffer_load_dwordx4 %4, %7, %8, %13 offen\n\tbuffer_load_dwordx4 %5, %7, %8, %14 offen\n\t"
                 "buffer_load_dwordx4 %6, %7, %8, %15 offen"
                 : "=&v"(wx[0]), "=&v"(wx[1]), "=&v"(wx[2]), "=&v"(wx[3]), "=&v"(wx[4]), "=&v"(wx[5]), "=&v"(wx[6])
                 : "v"(vj), "s"(rj), "s"(sj), "s"(sj + 8 * F * 2), "s"(sj + 16 * F * 2), "s"(sj + 24 * F * 2), "s"(sj + 32 * F * 2), "s"(sj + 40 * F * 2), "s"(sj + 48 * F * 2)
                 : "memory");
  }
  RowJobs rj_;
  rj_.load(a, nq, idx, mt, grp, h, lane);
  pf4(pfx + (1 + h) * 128, dummy);
  if (h == 3 && lane < 4) ((LAS unsigned*)(lds + L_WORDS))[lane] = 0u;
  WAITL0(); WAITV(12);
  STAMP(7); BAR();
  STAMP(2);
#pragma unroll
  for (int i = 0; i < PER; ++i) piece(1, 1, i);
  issue_a(2, 2);
  if (h < 2) pf4(pfx + (5 + h) * 128, dummy);
  WAITL0();
  if (h < 2) { WAITV(9); } else { WAITV(8); }
  asm volatile("" : "+v"(wx[0]), "+v"(wx[1]), "+v"(wx[2]), "+v"(wx[3]), "+v"(wx[4]), "+v"(wx[5]), "+v"(wx[6]));
  rj_.landed();
  BAR();
  const unsigned tag1 = get_tag(lds);
  float cw[8];
#pragma unroll
  for (int k = 0; k < 8; ++k) cw[k] = 0.f;
  constexpr bool TBc = TB;
  unsigned long long g0 = 0, g1 = 0, g2 = 0, g3 = 0;
  const bool wdjob = blockIdx.x < 196;
  const int wd_kp = blockIdx.x % 28, wd_cg = blockIdx.x / 28;
#pragma unroll
  for (int s = 1; s < K1_NST; ++s) {
    const int s1_ = (s + 1) % 3, s2_ = (s + 2) % 3;
    if (s == 1) {
#pragma unroll
      for (int i = 0; i < PER; ++i) {
        if (i < PER - 8) piece(2, s1_, 8 + i); else piece(3, s2_, i - (PER - 8));
        SBAR();
        if (i < 7) {
#pragma unroll
          for (int k = 0; k < 4; ++k) { cw[2 * k] += bf_lo(wx[i][k]); cw[2 * k + 1] += bf_hi(wx[i][k]); }
        }
        SBAR();
      }
    } else {
      if (s + 1 < K1_NST) issue_b(s + 1, s1_);

      if (s + 2 < K1_NST) issue_a(s + 2, s2_);
    }
    if (s == 1) {
      pf4((const char*)a.Wd + (size_t)(64 * wd_kp + 16 * h + ((lane >> 1) & 15)) * 896 + (wdjob ? wd_cg : 0) * 128 + (lane & 1) * 64, dummy);
#pragma unroll
      for (int k = 0; k < 8; ++k) {
        cw[k] += __builtin_bit_cast(float, __builtin_amdgcn_update_dpp(0, __builtin_bit_cast(int, cw[k]), 0x128, 0xf, 0xf, true));
        const auto p16 = __builtin_amdgcn_permlane16_swap(__float_as_uint(cw[k]), __float_as_uint(cw[k]), false, false);
        cw[k] = __uint_as_float(p16[0]) + __uint_as_float(p16[1]);
        const auto p32 = __builtin_amdgcn_permlane32_swap(__float_as_uint(cw[k]), __float_as_uint(cw[k]), false, false);
        cw[k] = __uint_as_float(p32[0]) + __uint_as_float(p32[1]);
      }
      if (lane < 8) {
        *(LAS f32x4*)(lds + L_CS + h * 256 + lane * 32) = f32x4{cw[0], cw[1], cw[2], cw[3]};
        *(LAS f32x4*)(lds + L_CS + h * 256 + lane * 32 + 16) = f32x4{cw[4], cw[5], cw[6], cw[7]};
      }
    }
    if (s == 2) rj_.sums(lane);
    if (s == 3) rj_.finish(ws, tag1, nq, idx, mt, grp, h, lane);
    if (s == 2 && h == 0 && wjob) {
      const float t = (*(LAS float*)(lds + L_CS + lane * 4) + *(LAS float*)(lds + L_CS + 256 + lane * 4)) +
                      (*(LAS float*)(lds + L_CS + 512 + lane * 4) + *(LAS float*)(lds + L_CS + 768 + lane * 4));
      __hip_atomic_store((GAS unsigned long long*)(ws + WS_CW + (size_t)((wj_mat * 2 + wj_kh) * F + 448 * nq + 64 * wj_cb + lane) * 8),
                         ((unsigned long long)tag1 << 32) | __float_as_uint(t), __ATOMIC_RELAXED, __HIP_MEMORY_SCOPE_AGENT);
    }
    if (s == 4 && h == 0 && lane == 0) st_flag(ws + WS_ARRIVE + blockIdx.x * 64, tag1);
    if (s == 5) {
      if (wdjob) {
        const u32x4 rwd = mk_srd(a.Wd, F * D * 2);
        const int rs = (lane & 15) >> 1, half = lane & 1, b = lane >> 4;
        const int vq = rs * 896 + (64 * wd_cg + 16 * b + 8 * half) * 2;
#pragma unroll
        for (int i = 0; i < 2; ++i) dma16(rwd, l0 + s2_ * K1_STAGE + (2 * h + i) * 1024, vq, (64 * wd_kp + 8 * (2 * h + i)) * 896);
      }
    }
    const int gc = 64 * h + lane;
    constexpr int NWC = TBc ? 128 : 256, CPM = NWC / 2;
    const bool isw = gc < NWC;
    const char* p_st = ws + WS_ST + (size_t)(mt * 128 + 32 * h + (lane & 31)) * 8;
    const char* p_c0 = ws + (isw ? WS_CW + (size_t)((gc / CPM) * 2 * F + f0 + gc % CPM) * 8 : WS_CD + (size_t)(a0 * 16 + (gc - NWC)) * 8);
    const char* p_c1 = isw ? p_c0 + (size_t)F * 8 : p_c0;
    const char* p_d = ws + WS_CD + (size_t)(a0 * 16 + (lane & 31)) * 8;
    if (s == 5)
      asm volatile("global_load_dwordx2 %0, %4, off sc1\n\tglobal_load_dwordx2 %1, %5, off sc1\n\tglobal_load_dwordx2 %2, %6, off sc1\n\tglobal_load_dwordx2 %3, %7, off sc1"
                   : "=&v"(g0), "=&v"(g1), "=&v"(g2), "=&v"(g3) : "v"(p_st), "v"(p_c0), "v"(p_c1), "v"(p_d) : "memory");
    if (s == 6) {
      WAITV(DFTPF == 2 ? 2 : 0);
      asm volatile("" : "+v"(g0), "+v"(g1), "+v"(g2), "+v"(g3));
      auto ld_gran = [&](const char* p) { return __hip_atomic_load((const GAS unsigned long long*)p, __ATOMIC_RELAXED, __HIP_MEMORY_SCOPE_AGENT); };
      for (unsigned sp = 0; sp < SPIN_MAX; ++sp) {
        if (__all((unsigned)(g0 >> 32) == tag1 && (unsigned)(g1 >> 32) == tag1 && (unsigned)(g2 >> 32) == tag1 && (unsigned)(g3 >> 32) == tag1)) break;
        __builtin_amdgcn_s_sleep(2);
        g0 = ld_gran(p_st); g1 = ld_gran(p_c0); g2 = ld_gran(p_c1); g3 = ld_gran(p_d);
      }
      if (lane < 32) *(LAS f32x2*)(lds + L_STATS + (32 * h + lane) * 8) = f32x2{bf_lo((unsigned)g0), 1.0f / bf_hi((unsigned)g0)};
      *(LAS float*)(lds + L_CS + gc * 4) = isw ? __uint_as_float((unsigned)g1) + __uint_as_float((unsigned)g2) : __uint_as_float((unsigned)g1);
      if (!TBc && h == 0 && lane < 32) *(LAS float*)(lds + L_CS + (256 + lane) * 4) = __uint_as_float((unsigned)g3);
    }
    WAITL0();
    if (s == 1) { WAITV(9); } else if (s + 2 < K1_NST) { WAITV(8); } else if (s == 5) { if (wdjob) { WAITV(6 + (DFTPF == 2 ? 2 : 0)); } else { WAITV(4 + (DFTPF == 2 ? 2 : 0)); } }
    STAMP(7 + s);
    BAR();
  }
  __builtin_amdgcn_s_setprio(0);
  if (h == 3 && wdjob) {
    const int i16 = lane & 15, g = lane >> 4, q = i16 >> 2, p = i16 & 3;
    const rsrc_t rt = mk_rsrc(ws + WS_WDT, 28 * 56 * 1024);
    lptr b0 = lds + 1 * K1_STAGE + (g >> 1) * 1024 + ((g & 1) * 4 + q) * 32 + p * 8;
#pragma unroll
    for (int kk = 0; kk < 2; ++kk)
#pragma unroll
      for (int u = 0; u < 4; ++u) {
        const bf16x8 f = ldstr(b0 + kk * 4096 + u * 256, b0 + kk * 4096 + u * 256 + 2048);
        __builtin_amdgcn_raw_buffer_store_b128(__builtin_bit_cast(u32x4, f), rt, lane * 16 + ((4 * wd_cg + u) * 56 + 2 * wd_kp + kk) * 1024, 0, STAUX);
      }
    WAITL0();
  }
  BAR();
  p1_helper_epilogue<TB ? 2 : 4>(lds, ws, mt, f0, h, lane);
}

template <int MODE, int NB, int JO>
__device__ __forceinline__ void p2_loader(lptr lds, const Args& a, int xcd, int idx, int r, int m, int n, int lw, int lane) {
  char* ws = a.ws;
  constexpr int PER = 4 + NB;
  const u32x4 rc = mk_srd((r >> 1) ? a.Ci : a.Cr, S * S * 2);
  const u32x4 rb = mk_srd(ws + WS_AB, 2 * 28 * 64 * 1024);
  const u32x4 rh = mk_srd(ws + WS_H, 128 * 56 * 1024);
  const u32x4 rw = mk_srd(ws + WS_WDT, 28 * 56 * 1024);
  const int r8 = lane >> 3, c8 = lane & 7;
  int va[2];
  va[0] = r8 * 4096 + (c8 ^ (r8 & 3)) * 16;
  va[1] = r8 * 4096 + (c8 ^ ((r8 & 3) | 4)) * 16;
  const int vl = lane * 16;
  const int k0 = 1024 * (r & 1), p = r >> 1;
  const int j0 = (lw < 2) ? 4 * lw : 8 + 3 * (lw - 2);
  const unsigned l0 = lds_addr(lds);
  const unsigned dummy = l0 + L_DUMMY + (lw & 1) * 256;
  const int sab = (128 * m + 32 * lw) * 4096 + k0 * 2;
  const int shb = ((8 * m + 2 * lw) * 56 + 14 * r) * 1024;
  const unsigned lab = l0 + 4 * lw * 1024, lbb = l0 + 16384 + j0 * 1024;
  const int sbb = ((p * 28 + 7 * n + (j0 >> 1)) * 64 + (k0 >> 5)) * 1024, swb = ((7 * n + (j0 >> 1)) * 56 + 14 * r) * 1024;
  auto issueAy = [&](int s, int slot) {
    const unsigned so = slot * P_STAGE; const int b = sab + 128 * (s & 15);
#pragma unroll
    for (int i = 0; i < 4; ++i) dma16(rc, lab + so + i * 1024, va[i & 1], b + i * 8 * 4096);
  };
  auto issueAd = [&](int sd, int slot) {
    const unsigned so = slot * P_STAGE; const int b = shb + 2048 * sd;
#pragma unroll
    for (int i = 0; i < 4; ++i) dma16c(rh, lab + so + i * 1024, vl, b + (i >> 1) * 56 * 1024 + (i & 1) * 1024);
  };
  auto issueBy = [&](int s, int slot) {
    const unsigned so = slot * P_STAGE; const int b = sbb + 2048 * (s & 15);
#pragma unroll
    for (int j = 0; j < NB; ++j) dma16c(rb, lbb + so + j * 1024, vl, b + ((j + JO) >> 1) * 65536 + ((j + JO) & 1) * 1024);
  };
  auto issueBd = [&](int sd, int slot) {
    const unsigned so = slot * P_STAGE; const int b = swb + 2048 * sd;
#pragma unroll
    for (int j = 0; j < NB; ++j) dma16c(rw, lbb + so + j * 1024, vl, b + ((j + JO) >> 1) * 57344 + ((j + JO) & 1) * 1024);
  };
  __builtin_amdgcn_s_setprio(0);
  const unsigned tag = get_tag(lds);
  if (MODE != 2 && lw == 3 && blockIdx.x < 196) {
    WAITV(0);
    if (lane == 0) st_flag(ws + WS_FWD + blockIdx.x * 64, tag);
  }
  issueAy(0, 0); issueAy(1, 1);
  if (lw == 3) {
    const int mtp = 8 * (r & 1) + (lane & 7), j = (lane >> 3) & 3;
    const bool act = lane < 32 && ((n & 1) == 0 || j == 3);
    poll_all(ws + WS_FAB + (mtp * 16 + (2 * p + (n >> 1)) * 4 + j) * 64, act, tag);
    if (lane == 0) *(volatile LAS unsigned*)(lds + L_WORDS + 8) = 1u;
  } else lds_spin(lds + L_WORDS + 8);
  STAMP(16);
  issueBy(0, 0);
  {
    const int l = (lane >> 1) < 28 ? (lane >> 1) : 27;
    const int sl = l >= 14, li = 14 * idx + l - 14 * sl;
    const char* bp = ws + WS_AB + ((size_t)(p * 28 + (li >> 4)) * 64 + 32 * (r & 1) + 2 * (4 * lw + sl) + ((li >> 3) & 1)) * 1024 + (li & 7) * 128 + (lane & 1) * 64;
    pf4c(bp, dummy); pf4c(bp + 4 * 1024, dummy);
  }
  issueBy(1, 1);
  __builtin_amdgcn_s_setprio(3);
  WAITV(NB + 2); BAR();
  if (MODE == 0 && lane == 0) {
    const unsigned cnt = __hip_atomic_fetch_add((LAS unsigned*)(lds + L_WORDS + 4), 1u, __ATOMIC_RELAXED, __HIP_MEMORY_SCOPE_WORKGROUP);
    if (cnt == 7u) st_flag(ws + WS_FH + (((xcd & 1) * 8 + (idx & 7)) * 16 + (xcd >> 1) * 4 + (idx >> 3)) * 64, tag);
  }
  issueAy(2, 2); issueBy(2, 2);
  WAITV(PER); BAR();
  issueAy(3, 3); issueBy(3, 3); issueAy(4, 4); issueBy(4, 4);
  WAITV(2 * PER); BAR();
  int slot = 0;
  auto ystep = [&](int s) { issueAy(s + 3, slot); issueBy(s + 3, slot); slot = (slot == 4) ? 0 : slot + 1; WAITV(2 * PER); BAR(); };
  auto dstep = [&](int sd) { issueAd(sd, slot); issueBd(sd, slot); slot = (slot == 4) ? 0 : slot + 1; WAITV(2 * PER); BAR(); };
#pragma unroll 1
  for (int s = 2; s < P_NY - 9; ++s) ystep(s);
  const int cg0 = (112 * n) >> 6, cg1 = (112 * n + 111) >> 6, jj = lane - 4;
  const bool fact = lane < 4 || (jj < 21 && cg0 + jj / 7 <= cg1);
  const char* fp = lane < 4 ? ws + WS_FH + (m * 16 + 4 * r + lane) * 64 : ws + WS_FWD + ((fact ? cg0 + jj / 7 : cg0) * 28 + 7 * r + (fact ? jj % 7 : 0)) * 64;
  unsigned fval = 0;
  if (lw == 3) asm volatile("global_load_dword %0, %1, off sc1" : "+v"(fval) : "v"(fp) : "memory");
#pragma unroll 1
  for (int s = P_NY - 9; s < P_NY - 6; ++s) ystep(s);
  if (lw == 3) {
    asm volatile("" : "+v"(fval));
    if (!__all(!fact || fval == tag)) poll_all(fp, fact, tag);
    if (lane == 0) *(volatile LAS unsigned*)(lds + L_WORDS + 12) = 1u;
  }
#pragma unroll 1
  for (int s = P_NY - 6; s < P_NY - 3; ++s) ystep(s);
  if (lw != 3) lds_spin(lds + L_WORDS + 12);
#pragma unroll 1
  for (int sd = 0; sd < P_ND; ++sd) dstep(sd);
  WAITV(PER); BAR();
  WAITV(0); BAR();
}

template <int MODE>
__global__ __launch_bounds__(512, 2) void fused_kernel(Args a) {
  extern __shared__ __attribute__((aligned(1024))) char smem[];
  lptr lds = (lptr)smem;
  char* ws = a.ws;
  const int tid = threadIdx.x, lane = tid & 63;
  const int wid = __builtin_amdgcn_readfirstlane(tid >> 6);
  int xcd = blockIdx.x & 7, idx = blockIdx.x >> 3;
  STAMP(0); STAMPR(30);
  unsigned long long n_clk_;
  { const unsigned long long r0_ = __builtin_amdgcn_s_memrealtime(), c0_ = __builtin_amdgcn_s_memtime();
    while (__builtin_amdgcn_s_memrealtime() - r0_ < 1000ull) { asm volatile("v_nop\n\tv_nop"); }
    n_clk_ = __builtin_amdgcn_s_memtime() - c0_; }
  if (wid == 3) {
    unsigned long long pc;
    asm volatile("s_getpc_b64 %0" : "=s"(pc));
    const unsigned ln = 13 * idx + ((lane >> 1) < 13 ? (lane >> 1) : 12);
    pf4((const char*)(pc & ~127ull) + (ln * 128 < CODE_PF ? ln * 128 : 0) + (lane & 1) * 64, lds_addr(lds) + L_DUMMY);
  }
  if (wid >= 4) __builtin_amdgcn_s_setprio(3);

  if (MODE != 2) {
    const int nq = xcd >> 1;
    const int mt = (xcd & 1) * 8 + (idx & 7), j = idx >> 3;
    const bool tb = j == 3;
    const int grp = nq * 4 + j;
    const int f0 = 448 * nq + 128 * j, a0 = 14 * nq + 2 * j;
    if (wid < 4) {
      if (tb) p1_consumer<MODE, 2, 4>(lds, a, xcd, idx, mt, grp, f0, a0, wid & 1, wid >> 1, lane);
      else p1_consumer<MODE, 4, 1>(lds, a, xcd, idx, mt, grp, f0, a0, wid & 1, wid >> 1, lane);
    } else {
      const int h = wid - 4;
      if (tb) p1_helper<MODE, true>(lds, a, xcd, idx, mt, grp, f0, a0, h, lane);
      else p1_helper<MODE, false>(lds, a, xcd, idx, mt, grp, f0, a0, h, lane);
    }
  }
  if (MODE == 1) return;
  if (MODE == 2) { if (tid < 4) ((LAS unsigned*)(lds + L_WORDS))[tid] = 0u; if (tid == 4) ((LAS unsigned*)(lds + L_WORDS))[4] = 0x51u; __syncthreads(); }

  launder(a, xcd, idx);
  ws = a.ws;
  const int p1_mt = (xcd & 1) * 8 + (idx & 7), p1_grp = (xcd >> 1) * 4 + (idx >> 3);
  const int r = xcd >> 1, m = 8 * (xcd & 1) + (idx & 7), n = idx >> 3;
  const int tile = m * 4 + n;
  const int i16 = lane & 15, g = lane >> 4;
  f32x4 accY[7][2], acc[7][2];
  if (wid == 7) {
    p2_loader<MODE, 3, 1>(lds, a, xcd, idx, r, m, n, 3, lane);
  } else if (wid == 6) {
    p2_loader<MODE, 3, 0>(lds, a, xcd, idx, r, m, n, 2, lane);
  } else if (wid >= 4) {
    p2_loader<MODE, 4, 0>(lds, a, xcd, idx, r, m, n, wid - 4, lane);
  } else {
    const int w = wid;
    const int rowt = 32 * w + 8 * (i16 >> 2) + (i16 & 3);
    const int vt0 = rowt * 128 + ((g) ^ (i16 & 7)) * 16, vt1 = rowt * 128 + ((g + 4) ^ (i16 & 7)) * 16;
    const int ve = 16384 + lane * 16;
    const int vh = (4 * w) * 1024 + lane * 16;
    bf16x8 T0[4], T1[4], E[14];
    auto ldTy = [&](bf16x8 (&T)[4], lptr st) {
      T[0] = ldsr128(st + vt0); T[1] = ldsr128(st + vt0 + 512); T[2] = ldsr128(st + vt1); T[3] = ldsr128(st + vt1 + 512);
    };
    auto ldTd = [&](bf16x8 (&T)[4], lptr st) {
      T[0] = ldsr128(st + vh); T[1] = ldsr128(st + vh + 2048); T[2] = ldsr128(st + vh + 1024); T[3] = ldsr128(st + vh + 3072);
    };
    auto ldE = [&](int j, lptr st) { E[j] = ldsr128(st + ve + ((j % 7) * 2 + j / 7) * 1024); };
    auto mm = [&](int j, bf16x8 (&T)[4]) {
      mfma_ip(acc[j % 7][0], E[j], T[(j / 7) * 2]);
      mfma_ip(acc[j % 7][1], E[j], T[(j / 7) * 2 + 1]);
    };
#define GHEAD 3
    constexpr int GH = GHEAD;
    auto ldT1 = [&](bf16x8 (&T)[4], lptr st, int i, int NT) {
      if (NT == 0) T[i] = ldsr128(st + (i >= 2 ? vt1 : vt0) + (i & 1) * 512);
      else T[i] = ldsr128(st + vh + (i & 1) * 2048 + (i >> 1) * 1024);
    };
    auto rest = [&](bf16x8 (&Tc)[4], bf16x8 (&Tn)[4], lptr sn, int NT) {
#pragma unroll
      for (int j = GH; j < 14; ++j) {
        const int k = j - GH;
        mfma_ip(acc[j % 7][0], E[j], Tc[(j / 7) * 2]); SBAR();
        if (NT != 2 && k < 4 + GH) { if (k < 4) ldT1(Tn, sn, k, NT); else ldE(k - 4, sn); SBAR(); }
        mfma_ip(acc[j % 7][1], E[j], Tc[(j / 7) * 2 + 1]); SBAR();
        if (NT != 2) ldE(j, sn); else FILL();
        SBAR();
      }
    };
    auto head = [&](bf16x8 (&Tc)[4]) {
#pragma unroll
      for (int j = 0; j < GH; ++j) { mm(j, Tc); FILL(); }
      SBAR();
    };
#pragma unroll
    for (int u = 0; u < 7; ++u) { acc[u][0] = f32x4{0, 0, 0, 0}; acc[u][1] = f32x4{0, 0, 0, 0}; }
#pragma unroll
    for (int u = 0; u < 7; ++u) { asm volatile("" : "+v"(acc[u][0])); asm volatile("" : "+v"(acc[u][1])); }
    PAD_MFMA_IN();
    if (w != r) {
      const rsrc_t rp = mk_rsrc(ws + WS_PD + (size_t)((tile * 4 + r) * 4 + w) * 7168, 7168);
      const u32x4 sent = {0xffffffffu, 0xffffffffu, 0xffffffffu, 0xffffffffu};
#pragma unroll
      for (int u = 0; u < 7; ++u) {
        __builtin_amdgcn_raw_buffer_store_b128(sent, rp, u * 1024 + lane * 16, 0, 16);
      }
    }
    STAMP(16); STAMPR(28);
    BAR();
    STAMP(17); STAMPR(27);
    ldTy(T0, lds);
#pragma unroll
    for (int j = 0; j < 14; ++j) ldE(j, lds);
    SBAR();
    head(T0);
    WAITL0(); BAR();
    int slot = 1;
#pragma unroll 1
    for (int s = 0; s < P_NY - 2; s += 2) {
      lptr sn = lds + slot * P_STAGE; slot = (slot == 4) ? 0 : slot + 1;
      rest(T0, T1, sn, 0); head(T1);
      WAITL0(); BAR();
      sn = lds + slot * P_STAGE; slot = (slot == 4) ? 0 : slot + 1;
      rest(T1, T0, sn, 0); head(T0);
      WAITL0(); BAR();
      if (YREP > 1 && s == 14) { STAMP(23); }
      if (MODE == 0 && s == 0) {
        WAITV(0);
        if (lane == 0) {
          const unsigned cnt = __hip_atomic_fetch_add((LAS unsigned*)(lds + L_WORDS + 4), 1u, __ATOMIC_RELAXED, __HIP_MEMORY_SCOPE_WORKGROUP);
          if (cnt == 7u) st_flag(ws + WS_FH + (p1_mt * 16 + p1_grp) * 64, get_tag(lds));
        }
      }
    }
    {
      lptr sn = lds + slot * P_STAGE; slot = (slot == 4) ? 0 : slot + 1;
      rest(T0, T1, sn, 0); head(T1);
      WAITL0(); BAR();
      sn = lds + slot * P_STAGE; slot = (slot == 4) ? 0 : slot + 1;
      rest(T1, T0, sn, 1);
    }
    PAD_MFMA_OUT();
    STAMP(18);
    {
      const float sg = (r >> 1) ? -1.0f : 1.0f;
#pragma unroll
      for (int u = 0; u < 7; ++u) { accY[u][0] = acc[u][0] * sg; accY[u][1] = acc[u][1] * sg; acc[u][0] = f32x4{0, 0, 0, 0}; acc[u][1] = f32x4{0, 0, 0, 0}; }
      if (w != r) {
        const rsrc_t rp = mk_rsrc(ws + WS_PY + (size_t)((tile * 4 + r) * 4 + w) * 7168, 7168);
#pragma unroll
        for (int u = 0; u < 7; ++u) {
          const u32x4 o = {pk_bf16(accY[u][0][0], accY[u][0][1]), pk_bf16(accY[u][0][2], accY[u][0][3]),
                           pk_bf16(accY[u][1][0], accY[u][1][1]), pk_bf16(accY[u][1][2], accY[u][1][3])};
          __builtin_amdgcn_raw_buffer_store_b128(o, rp, u * 1024 + lane * 16, 0, 16);
        }
      }
    }
#pragma unroll
    for (int u = 0; u < 7; ++u) { asm volatile("" : "+v"(acc[u][0])); asm volatile("" : "+v"(acc[u][1])); }
    PAD_MFMA_IN();
    head(T0);
    WAITL0(); BAR();
#pragma unroll 1
    for (int s = 0; s < P_ND - 1; s += 2) {
      lptr sn = lds + slot * P_STAGE; slot = (slot == 4) ? 0 : slot + 1;
      rest(T0, T1, sn, 1); head(T1);
      WAITL0(); BAR();
      if (s == 0 && w != r) {
        WAITV(0);
        const unsigned tag = get_tag(lds);
        if (lane == 0) st_flag(ws + WS_FY + ((tile * 4 + r) * 4 + w) * 64, tag);
      }
      sn = lds + slot * P_STAGE; slot = (slot == 4) ? 0 : slot + 1;
      rest(T1, T0, sn, 1); head(T0);
      WAITL0(); if (s + 2 < P_ND - 1) BAR();
    }
    rest(T0, T1, lds, 2);
    PAD_MFMA_OUT();
    STAMP(19); STAMPR(29);
    if (w != r) {
      const rsrc_t rp = mk_rsrc(ws + WS_PD + (size_t)((tile * 4 + r) * 4 + w) * 7168, 7168);
#pragma unroll
      for (int u = 0; u < 7; ++u) {
        u32x4 o = {pk_bf16(acc[u][0][0], acc[u][0][1]), pk_bf16(acc[u][0][2], acc[u][0][3]),
                   pk_bf16(acc[u][1][0], acc[u][1][1]), pk_bf16(acc[u][1][2], acc[u][1][3])};
#pragma unroll
        for (int k = 0; k < 4; ++k) o[k] = o[k] < 0xfffffffeu ? o[k] : 0xfffffffeu;
        __builtin_amdgcn_raw_buffer_store_b128(o, rp, u * 1024 + lane * 16, 0, 16);
      }
    } else {
#pragma unroll
      for (int uu = 0; uu < 7; ++uu) {
        *(LAS f32x4*)(lds + (uu * 4 + 0) * 1024 + lane * 16) = accY[uu][0];
        *(LAS f32x4*)(lds + (uu * 4 + 1) * 1024 + lane * 16) = accY[uu][1];
        *(LAS f32x4*)(lds + (uu * 4 + 2) * 1024 + lane * 16) = acc[uu][0];
        *(LAS f32x4*)(lds + (uu * 4 + 3) * 1024 + lane * 16) = acc[uu][1];
      }
      WAITL0();
    }
    BAR();
  }
  if (wid >= 4) {
    __builtin_amdgcn_s_setprio(0);
    const int hw = wid - 4;
    const int nu = hw < 3 ? 2 : 1;
    const unsigned tag = get_tag(lds);
    const int tok0 = 128 * m + 32 * r + 8 * (i16 >> 2) + (i16 & 3);
    const rsrc_t ry = mk_rsrc(ws + WS_PY, 64 * 16 * 7168), rd = mk_rsrc(ws + WS_PD, 64 * 16 * 7168);
    u32x2 x0[2], x1[2];
    u32x4 fy[2][3], fd[2][3];
#pragma unroll
    for (int k = 0; k < 2; ++k) {
      const int u = 2 * hw + (k < nu ? k : 0);
      const int ecol = 112 * n + 16 * u + 4 * g;
      x0[k] = *(const u32x2*)(a.x + (size_t)tok0 * D + ecol); x1[k] = *(const u32x2*)(a.x + (size_t)(tok0 + 4) * D + ecol);
    }
    {
      const int rr = (r + 1 + (lane % 3)) & 3;
      poll_all(ws + WS_FY + ((tile * 4 + rr) * 4 + r) * 64, lane < 3, tag);
    }
#pragma unroll
    for (int k = 0; k < 2; ++k)
#pragma unroll
      for (int i = 0; i < 3; ++i)
        fy[k][i] = __builtin_amdgcn_raw_buffer_load_b128(ry, ((tile * 4 + ((r + 1 + i) & 3)) * 4 + r) * 7168 + (2 * hw + (k < nu ? k : 0)) * 1024 + lane * 16, 0, 16);
    STAMP(20);
    if (MODE == 0 && blockIdx.x == 0 && hw == 3) {
#pragma unroll 1
      for (int i = 0; i < 4; ++i) poll_all(ws + WS_ARRIVE + (64 * i + lane) * 64, true, tag);
      if (lane == 0) st_flag(ws + WS_NONCE, tag);
    }
    for (unsigned sp = 0; sp < (SPIN_MAX >> 3); ++sp) {
      const unsigned v = lane < 3 ? __builtin_amdgcn_raw_buffer_load_b32(rd, ((tile * 4 + ((r + 1 + lane) & 3)) * 4 + r) * 7168 + 6 * 1024 + 63 * 16 + 12, 0, 16) : 0u;
      if (__all(v != 0xffffffffu)) break;
      __builtin_amdgcn_s_sleep(1);
    }
    for (unsigned sp = 0; sp < (SPIN_MAX >> 3); ++sp) {
      bool ok = true;
#pragma unroll
      for (int k = 0; k < 2; ++k)
#pragma unroll
        for (int i = 0; i < 3; ++i) {
          fd[k][i] = __builtin_amdgcn_raw_buffer_load_b128(rd, ((tile * 4 + ((r + 1 + i) & 3)) * 4 + r) * 7168 + (2 * hw + (k < nu ? k : 0)) * 1024 + lane * 16, 0, 16);
        }
#pragma unroll
      for (int k = 0; k < 2; ++k)
#pragma unroll
        for (int i = 0; i < 3; ++i)
          ok = ok && fd[k][i][0] != 0xffffffffu && fd[k][i][1] != 0xffffffffu && fd[k][i][2] != 0xffffffffu && fd[k][i][3] != 0xffffffffu;
      if (__all(ok)) break;
      __builtin_amdgcn_s_sleep(2);
    }
    STAMP(21); STAMPR(29);
    BAR();
#pragma unroll
    for (int k = 0; k < 2; ++k) {
      if (k < nu) {
        const int u = 2 * hw + k;
        const int ecol = 112 * n + 16 * u + 4 * g;
        f32x4 y0 = *(LAS f32x4*)(lds + (u * 4 + 0) * 1024 + lane * 16), y1 = *(LAS f32x4*)(lds + (u * 4 + 1) * 1024 + lane * 16);
        f32x4 d0 = *(LAS f32x4*)(lds + (u * 4 + 2) * 1024 + lane * 16), d1 = *(LAS f32x4*)(lds + (u * 4 + 3) * 1024 + lane * 16);
#pragma unroll
        for (int i = 0; i < 3; ++i) {
          y0 += f32x4{bf_lo(fy[k][i][0]), bf_hi(fy[k][i][0]), bf_lo(fy[k][i][1]), bf_hi(fy[k][i][1])};
          y1 += f32x4{bf_lo(fy[k][i][2]), bf_hi(fy[k][i][2]), bf_lo(fy[k][i][3]), bf_hi(fy[k][i][3])};
          d0 += f32x4{bf_lo(fd[k][i][0]), bf_hi(fd[k][i][0]), bf_lo(fd[k][i][1]), bf_hi(fd[k][i][1])};
          d1 += f32x4{bf_lo(fd[k][i][2]), bf_hi(fd[k][i][2]), bf_lo(fd[k][i][3]), bf_hi(fd[k][i][3])};
        }
        const u32x2 o0 = {pk_bf16(bf_lo(x0[k][0]) + y0[0] * silu_f(d0[0]), bf_hi(x0[k][0]) + y0[1] * silu_f(d0[1])),
                          pk_bf16(bf_lo(x0[k][1]) + y0[2] * silu_f(d0[2]), bf_hi(x0[k][1]) + y0[3] * silu_f(d0[3]))};
        const u32x2 o1 = {pk_bf16(bf_lo(x1[k][0]) + y1[0] * silu_f(d1[0]), bf_hi(x1[k][0]) + y1[1] * silu_f(d1[1])),
                          pk_bf16(bf_lo(x1[k][1]) + y1[2] * silu_f(d1[2]), bf_hi(x1[k][1]) + y1[3] * silu_f(d1[3]))};
        *(u32x2*)(a.out + (size_t)tok0 * D + ecol) = o0;
        *(u32x2*)(a.out + (size_t)(tok0 + 4) * D + ecol) = o1;
      }
    }
  }
  WAITV(0);
  { const unsigned long long r1_ = __builtin_amdgcn_s_memrealtime(), e_ = 0ull; while (__builtin_amdgcn_s_memrealtime() - r1_ < e_) { asm volatile("v_nop\n\tv_nop"); } }
}

#define SPLIT 0
extern "C" void kernel_launch(void* const* d_in, const int* in_sizes, int n_in, void* d_out, int out_size, void* d_ws, size_t ws_size,
                              hipStream_t stream) {
  Args a;
  a.x = (const u16*)d_in[0]; a.Cr = (const u16*)d_in[1]; a.Ci = (const u16*)d_in[2]; a.Dr = (const u16*)d_in[3]; a.Di = (const u16*)d_in[4];
  a.Wg = (const u16*)d_in[6]; a.Wu = (const u16*)d_in[7]; a.Wd = (const u16*)d_in[8];
  a.ws = (char*)d_ws; a.out = (u16*)d_out;
  (void)hipFuncSetAttribute((const void*)fused_kernel<0>, hipFuncAttributeMaxDynamicSharedMemorySize, L_TOTAL);
  fused_kernel<0><<<256, 512, L_TOTAL, stream>>>(a);
}
```

```cpp
#include <hip/hip_runtime.h>
#include <hip/hip_bf16.h>
#include <stdint.h>

typedef unsigned short u16;
typedef __bf16 bf16x8 __attribute__((ext_vector_type(8)));
typedef short s16x8 __attribute__((ext_vector_type(8)));
typedef float f32x4 __attribute__((ext_vector_type(4)));
typedef float f32x2 __attribute__((ext_vector_type(2)));
typedef unsigned u32x4 __attribute__((ext_vector_type(4)));
typedef unsigned u32x2 __attribute__((ext_vector_type(2)));
#define LAS __attribute__((address_space(3)))
#define GAS __attribute__((address_space(1)))
typedef LAS char* lptr;
typedef __amdgpu_buffer_rsrc_t rsrc_t;

constexpr int S = 2048, D = 448, F = 1792;

constexpr size_t WS_NONCE  = 0;
constexpr size_t WS_ARRIVE = 4096;
constexpr size_t WS_FAB    = 32768;
constexpr size_t WS_FH     = 49152;
constexpr size_t WS_FWD    = 65536;
constexpr size_t WS_FY     = 81920;
constexpr size_t WS_FD     = 147456;
constexpr size_t WS_ST     = 212992;
constexpr size_t WS_CW     = 229376;
constexpr size_t WS_CD     = 286720;
constexpr size_t WS_AB     = 327680;
constexpr size_t WS_H      = WS_AB + (size_t)2 * 28 * 64 * 1024;
constexpr size_t WS_WDT    = WS_H + (size_t)128 * 56 * 1024;
constexpr size_t WS_PY     = WS_WDT + (size_t)28 * 56 * 1024;
constexpr size_t WS_PD     = WS_PY + (size_t)64 * 16 * 7 * 1024;

#define STAMP(i)
#define STAMPR(i)
#define SBAR() __builtin_amdgcn_sched_barrier(0)
#define BAR() __builtin_amdgcn_s_barrier()
#define WAITV(n) asm volatile("s_waitcnt vmcnt(%0)" ::"n"(n) : "memory")
#define WAITL0() __builtin_amdgcn_s_waitcnt(0xC07F)
#define CODE_PF 49152
#define DFTPF 0
#define PF0 1
#define STAUX 16
#define SPIN_MAX (1u << 18)

__device__ __forceinline__ rsrc_t mk_rsrc(const void* p, unsigned bytes) {
  return __builtin_amdgcn_make_buffer_rsrc((void*)p, 0, bytes, 0x00020000);
}
__device__ __forceinline__ u32x4 mk_srd(const void* p, unsigned bytes) {
  const unsigned long long a = (unsigned long long)p;
  return u32x4{(unsigned)a, (unsigned)(a >> 32) & 0xffffu, bytes, 0x00020000u};
}
__device__ __forceinline__ unsigned lds_addr(lptr p) { return (unsigned)(size_t)p; }
__device__ __forceinline__ void dma16(const u32x4& srd, unsigned dst, int voff, int soff) {
  asm volatile("s_mov_b32 m0, %0\n\ts_nop 0\n\tbuffer_load_dwordx4 %1, %2, %3 offen lds" ::"s"(dst), "v"(voff), "s"(srd), "s"(soff) : "memory");
}
__device__ __forceinline__ void dma16c(const u32x4& srd, unsigned dst, int voff, int soff) {
  asm volatile("s_mov_b32 m0, %0\n\ts_nop 0\n\tbuffer_load_dwordx4 %1, %2, %3 offen sc1 lds" ::"s"(dst), "v"(voff), "s"(srd), "s"(soff) : "memory");
}
__device__ __forceinline__ void pf4c(const char* p, unsigned dst) {
  asm volatile("s_mov_b32 m0, %0\n\ts_nop 0\n\tglobal_load_lds_dword %1, off sc1" ::"s"(dst), "v"(p) : "memory");
}
__device__ __forceinline__ void pf4(const char* p, unsigned dst) {
  asm volatile("s_mov_b32 m0, %0\n\ts_nop 0\n\tglobal_load_lds_dword %1, off" ::"s"(dst), "v"(p) : "memory");
}
__device__ __forceinline__ bf16x8 ldsr128(lptr p) { return *(const LAS bf16x8*)p; }
__device__ __forceinline__ bf16x8 ldstr(lptr p0, lptr p1) {
  typedef short v4i16_t __attribute__((ext_vector_type(4)));
  v4i16_t lo = __builtin_amdgcn_ds_read_tr16_b64_v4i16((LAS v4i16_t*)p0);
  v4i16_t hi = __builtin_amdgcn_ds_read_tr16_b64_v4i16((LAS v4i16_t*)p1);
  s16x8 v = __builtin_shufflevector(lo, hi, 0, 1, 2, 3, 4, 5, 6, 7);
  return __builtin_bit_cast(bf16x8, v);
}
__device__ __forceinline__ f32x4 mfma(bf16x8 a, bf16x8 b, f32x4 c) { return __builtin_amdgcn_mfma_f32_16x16x32_bf16(a, b, c, 0, 0, 0); }
__device__ __forceinline__ void mfma_ip(f32x4& c, const bf16x8& a, const bf16x8& b) {
  asm volatile("v_mfma_f32_16x16x32_bf16 %0, %1, %2, %0" : "+v"(c) : "v"(a), "v"(b));
}
#define FILL() asm volatile("s_nop 0")
#define PAD_MFMA_OUT() asm volatile("s_nop 7\n\ts_nop 3")
#define PAD_MFMA_IN() asm volatile("s_nop 3")
__device__ __forceinline__ unsigned pk_bf16(float lo, float hi) {
  typedef __bf16 bf16x2 __attribute__((ext_vector_type(2)));
  f32x2 v = {lo, hi};
  bf16x2 b = __builtin_convertvector(v, bf16x2);
  return __builtin_bit_cast(unsigned, b);
}
__device__ __forceinline__ float bf_lo(unsigned v) { return __uint_as_float(v << 16); }
__device__ __forceinline__ float bf_hi(unsigned v) { return __uint_as_float(v & 0xffff0000u); }
__device__ __forceinline__ float rbf(float f) { return bf_lo(pk_bf16(f, 0.f)); }
__device__ __forceinline__ float silu_f(float x) {
  float e = __builtin_amdgcn_exp2f(x * -1.4426950409f);
  return x * __builtin_amdgcn_rcpf(1.0f + e);
}
template <int N>
__device__ __forceinline__ void wave_sums(float (&v)[N]) {
#define DPP_STEP(ctrl, rmask) _Pragma("unroll") for (int i = 0; i < N; ++i) \
    v[i] += __builtin_bit_cast(float, __builtin_amdgcn_update_dpp(0, __builtin_bit_cast(int, v[i]), ctrl, rmask, 0xf, true));
  DPP_STEP(0x111, 0xf)
  DPP_STEP(0x112, 0xf)
  DPP_STEP(0x114, 0xf)
  DPP_STEP(0x118, 0xf)
  DPP_STEP(0x142, 0xa)
  DPP_STEP(0x143, 0xc)
#undef DPP_STEP
#pragma unroll
  for (int i = 0; i < N; ++i) v[i] = __builtin_bit_cast(float, __builtin_amdgcn_readlane(__builtin_bit_cast(int, v[i]), 63));
}
__device__ __forceinline__ unsigned ld_flag(const void* p) { return __hip_atomic_load((const GAS unsigned*)p, __ATOMIC_RELAXED, __HIP_MEMORY_SCOPE_AGENT); }
__device__ __forceinline__ void st_flag(void* p, unsigned v) { __hip_atomic_store((GAS unsigned*)p, v, __ATOMIC_RELAXED, __HIP_MEMORY_SCOPE_AGENT); }
__device__ __forceinline__ unsigned get_tag(lptr lds);
__device__ __forceinline__ void poll_all(const char* p, bool active, unsigned tag) {
  for (unsigned sp = 0; sp < SPIN_MAX; ++sp) {
    const unsigned v = active ? ld_flag(p) : tag;
    if (__all(v == tag)) break;
    __builtin_amdgcn_s_sleep(2);
  }
}
__device__ __forceinline__ void lds_spin(lptr p) {
  for (unsigned sp = 0; sp < SPIN_MAX * 4; ++sp) {
    if (*(volatile LAS unsigned*)p != 0u) break;
    __builtin_amdgcn_s_sleep(2);
  }
}

constexpr int K1_STAGE = 53248, K1_NST = 7, K1_WO = 16384;
constexpr int L_MISC = 3 * K1_STAGE;
constexpr int L_STATS = L_MISC;
constexpr int L_CS = L_MISC + 1024;
constexpr int L_WORDS = L_CS + 18 * 64;
constexpr int L_DUMMY = L_WORDS + 128;
constexpr int L_TOTAL = L_DUMMY + 512;
#define YREP 1
constexpr int P_STAGE = 30720, P_NY = 16 * YREP, P_ND = 7, P_NST = P_NY + P_ND;
static_assert(L_TOTAL <= 163840 && 5 * P_STAGE <= L_MISC, "LDS");

__device__ __forceinline__ unsigned get_tag(lptr lds) { return *(volatile LAS unsigned*)(lds + L_WORDS + 16); }

struct Args {
  const u16 *x, *Cr, *Ci, *Dr, *Di, *Wg, *Wu, *Wd;
  char* ws;
  u16* out;
};

__device__ __forceinline__ void launder(Args& a, int& xcd, int& idx) {
  asm volatile("" : "+s"(a.x), "+s"(a.Cr), "+s"(a.Ci), "+s"(a.Dr), "+s"(a.Di));
  asm volatile("" : "+s"(a.Wg), "+s"(a.Wu), "+s"(a.Wd), "+s"(a.ws), "+s"(a.out));
  asm volatile("" : "+s"(xcd), "+s"(idx));
}

struct RowJobs {
  u32x4 v[4];
  float f[2][8], sm[4];
  __device__ __forceinline__ void load(const Args& a, int nq, int idx, int mt, int grp, int h, int lane) {
    const int l56 = lane < 56 ? lane : 55;
    const u16* sp = a.x + (size_t)(mt * 128 + 8 * grp + 2 * h) * D + l56 * 8;
    const u16* dp = (nq >= 2 ? a.Di : a.Dr) + (size_t)(224 * (nq & 1) + 7 * idx + h) * D + l56 * 8;
    const u16* dq = dp + (h < 3 ? 4 * D : 0);
    asm volatile("global_load_dwordx4 %0, %4, off\n\tglobal_load_dwordx4 %1, %4, off offset:896\n\tglobal_load_dwordx4 %2, %5, off\n\tglobal_load_dwordx4 %3, %6, off"
                 : "=&v"(v[0]), "=&v"(v[1]), "=&v"(v[2]), "=&v"(v[3]) : "v"(sp), "v"(dp), "v"(dq) : "memory");
  }
  __device__ __forceinline__ void landed() { asm volatile("" : "+v"(v[0]), "+v"(v[1]), "+v"(v[2]), "+v"(v[3])); }
  __device__ __forceinline__ void sums(int lane) {
#pragma unroll
    for (int r = 0; r < 4; ++r) {
      float t[8];
#pragma unroll
      for (int i = 0; i < 4; ++i) { t[2 * i] = bf_lo(v[r][i]); t[2 * i + 1] = bf_hi(v[r][i]); }
      const float s = ((t[0] + t[1]) + (t[2] + t[3])) + ((t[4] + t[5]) + (t[6] + t[7]));
      sm[r] = lane < 56 ? s : 0.f;
      if (r < 2) {
#pragma unroll
        for (int i = 0; i < 8; ++i) f[r][i] = t[i];
      }
    }
    wave_sums<4>(sm);
  }
  __device__ __forceinline__ void finish(char* ws, unsigned tag, int nq, int idx, int mt, int grp, int h, int lane) {
    float q[2], mean[2];
#pragma unroll
    for (int r = 0; r < 2; ++r) {
      mean[r] = sm[r] * (1.0f / D);
      float t0 = 0.f, t1 = 0.f;
#pragma unroll
      for (int i = 0; i < 4; ++i) {
        const float d0 = f[r][2 * i] - mean[r], d1 = f[r][2 * i + 1] - mean[r];
        t0 = __builtin_fmaf(d0, d0, t0); t1 = __builtin_fmaf(d1, d1, t1);
      }
      q[r] = lane < 56 ? t0 + t1 : 0.f;
    }
    wave_sums<2>(q);
    const int r = lane & 1;
    const unsigned pay = pk_bf16(r ? mean[1] : mean[0], rbf(__builtin_amdgcn_sqrtf(rbf(rbf((r ? q[1] : q[0]) * (1.0f / D)) + 1e-5f))));
    if (lane < 2 || lane == 2 || (lane == 3 && h < 3)) {
      const unsigned v_ = lane < 2 ? pay : __float_as_uint(lane == 2 ? sm[2] : sm[3]);
      const size_t off = lane < 2 ? WS_ST + (size_t)(mt * 128 + 8 * grp + 2 * h + lane) * 8 : WS_CD + (size_t)(224 * nq + 7 * idx + h + 4 * (lane - 2)) * 8;
      __hip_atomic_store((GAS unsigned long long*)(ws + off), ((unsigned long long)tag << 32) | v_, __ATOMIC_RELAXED, __HIP_MEMORY_SCOPE_AGENT);
    }
  }
};

__device__ __forceinline__ void h_batch(const f32x4 (&g)[2][2], const f32x4 (&u)[2][2], const f32x4 (&cg)[2], const f32x4 (&cu)[2], const f32x2 (&st)[2],
                                        rsrc_t rh, int off0) {
  f32x2 gg[8], uu[8], z[8];
#pragma unroll
  for (int i = 0; i < 8; ++i) {
    const int t = i >> 2, j = (i >> 1) & 1, hh = i & 1;
    gg[i] = (f32x2{g[j][t][2 * hh], g[j][t][2 * hh + 1]} - f32x2{cg[j][2 * hh], cg[j][2 * hh + 1]} * st[t].x) * st[t].y;
  }
#pragma unroll
  for (int i = 0; i < 8; ++i) z[i] = gg[i] * ((gg[i] * gg[i]) * -0.1029432397f + -2.302208198f);
#pragma unroll
  for (int i = 0; i < 8; ++i) { z[i].x = __builtin_amdgcn_exp2f(z[i].x); z[i].y = __builtin_amdgcn_exp2f(z[i].y); }
#pragma unroll
  for (int i = 0; i < 8; ++i) {
    const int t = i >> 2, j = (i >> 1) & 1, hh = i & 1;
    uu[i] = ((f32x2{u[j][t][2 * hh], u[j][t][2 * hh + 1]} - f32x2{cu[j][2 * hh], cu[j][2 * hh + 1]} * st[t].x) * st[t].y) * gg[i];
    z[i] = z[i] + 1.0f;
  }
#pragma unroll
  for (int i = 0; i < 8; ++i) { z[i].x = __builtin_amdgcn_rcpf(z[i].x); z[i].y = __builtin_amdgcn_rcpf(z[i].y); }
#pragma unroll
  for (int i = 0; i < 8; ++i) uu[i] = uu[i] * z[i];
#pragma unroll
  for (int tl = 0; tl < 2; ++tl) {
    const u32x4 o = {pk_bf16(uu[4 * tl].x, uu[4 * tl].y), pk_bf16(uu[4 * tl + 1].x, uu[4 * tl + 1].y),
                     pk_bf16(uu[4 * tl + 2].x, uu[4 * tl + 2].y), pk_bf16(uu[4 * tl + 3].x, uu[4 * tl + 3].y)};
    __builtin_amdgcn_raw_buffer_store_b128(o, rh, off0 + tl * 56 * 1024, 0, STAUX);
  }
}
constexpr int L_HD = 61440;

template <int MODE, int NG, int NA>
__device__ __forceinline__ void p1_consumer(lptr lds, Args a, int xcd, int idx, int mt, int grp, int f0, int a0, int wm, int wn, int lane) {
  launder(a, xcd, idx);
  asm volatile("" : "+s"(mt), "+s"(grp), "+s"(f0), "+s"(a0));
  char* ws = a.ws;
  const char* cp = (const char*)((xcd >> 2) ? a.Ci : a.Cr) + (size_t)(1024 * (xcd & 1) + 32 * idx + 8 * (2 * wn + wm) + (lane >> 5)) * 4096 + 2048 * ((xcd >> 1) & 1) + (lane & 31) * 64;
  unsigned nonce = 0;
  if (MODE == 0 && wm == 0 && wn == 0) asm volatile("global_load_dword %0, %1, off sc1" : "+v"(nonce) : "v"(ws + WS_NONCE) : "memory");
  constexpr int NBK = 2 * NG, NCQ = NBK / 4, WSZ = NBK * 8 * 256, KSO = 4 * NCQ * 1024;
  constexpr int GO = K1_WO, UO = K1_WO + WSZ, DO = K1_WO + 2 * WSZ;
  const int i16 = lane & 15, g = lane >> 4, q = i16 >> 2, p = i16 & 3;
  const int rowx = 64 * wm + 8 * (i16 >> 2) + (i16 & 3);
  const int vx0 = rowx * 128 + ((g) ^ (i16 & 7)) * 16;
  const int vx1 = rowx * 128 + ((g + 4) ^ (i16 & 7)) * 16;
  const int fw = ((q >> 1) & 1) | ((g & 1) << 1);
  int vw[NG];
#pragma unroll
  for (int j = 0; j < NG; ++j) { const int nb = NG * wn + j; vw[j] = g * NCQ * 1024 + (nb >> 2) * 1024 + q * 128 + (((nb & 3) ^ fw) * 32) + p * 8; }
  const int vd0 = DO + (16 * NA * wn + i16) * 128 + ((g) ^ (i16 & 7)) * 16;
  const int vd1 = DO + (16 * NA * wn + i16) * 128 + ((g + 4) ^ (i16 & 7)) * 16;

  f32x4 G[NG][4], U[NG][4], AB[NA][4];
#pragma unroll
  for (int j = 0; j < NG; ++j)
#pragma unroll
    for (int t = 0; t < 4; ++t) { G[j][t] = f32x4{0, 0, 0, 0}; U[j][t] = f32x4{0, 0, 0, 0}; }
#pragma unroll
  for (int j = 0; j < NA; ++j)
#pragma unroll
    for (int t = 0; t < 4; ++t) AB[j][t] = f32x4{0, 0, 0, 0};

  bf16x8 X0[4], X1[4], WG[NG], WU[NG], WD[NA];
  auto ldx = [&](bf16x8 (&X)[4], lptr st, int ks) {
#pragma unroll
    for (int t = 0; t < 4; ++t) X[t] = ldsr128(st + (ks ? vx1 : vx0) + (32 * (t >> 1) + 4 * (t & 1)) * 128);
  };
  auto ldg = [&](int j, lptr st, int ks) { lptr b = st + GO + ks * KSO + vw[j]; WG[j] = ldstr(b, b + 512); };
  auto ldu = [&](int j, lptr st, int ks) { lptr b = st + UO + ks * KSO + vw[j]; WU[j] = ldstr(b, b + 512); };
  auto ldd = [&](int j, lptr st, int ks) { WD[j] = ldsr128(st + (ks ? vd1 : vd0) + j * 16 * 128); };
  auto grp4 = [&](f32x4 (&C)[4], const bf16x8& W, bf16x8 (&X)[4], bool wfirst, auto mid) {
    if (wfirst) { mfma_ip(C[0], W, X[0]); mfma_ip(C[1], W, X[1]); } else { mfma_ip(C[0], X[0], W); mfma_ip(C[1], X[1], W); }
    SBAR(); mid(); SBAR();
    if (wfirst) { mfma_ip(C[2], W, X[2]); mfma_ip(C[3], W, X[3]); } else { mfma_ip(C[2], X[2], W); mfma_ip(C[3], X[3], W); }
    SBAR();
  };
  auto fill = [&]() { FILL(); };
  auto ldx1 = [&](bf16x8 (&X)[4], lptr st, int ks, int t) { X[t] = ldsr128(st + (ks ? vx1 : vx0) + (32 * (t >> 1) + 4 * (t & 1)) * 128); };
  auto blockA = [&](lptr st) {
#pragma unroll
    for (int j = 0; j < NG; ++j) {
      grp4(G[j], WG[j], X0, true, [&]() { if (2 * j < 4) ldx1(X1, st, 1, 2 * j); else FILL(); }); ldg(j, st, 1); SBAR();
      grp4(U[j], WU[j], X0, true, [&]() { if (2 * j + 1 < 4) ldx1(X1, st, 1, 2 * j + 1); else FILL(); }); ldu(j, st, 1); SBAR();
    }
#pragma unroll
    for (int j = 0; j < NA; ++j) { grp4(AB[j], WD[j], X0, false, fill); ldd(j, st, 1); SBAR(); }
  };
  auto blockB_head = [&]() { grp4(G[0], WG[0], X1, true, fill); FILL(); grp4(U[0], WU[0], X1, true, fill); };
  auto blockB_rest = [&](lptr sn, bool reload) {
    int k = 0;
    auto mid = [&]() {
      if (!reload) FILL();
      else if (k == 0) ldg(0, sn, 0);
      else if (k == 1) ldu(0, sn, 0);
      else if (k < 6) ldx1(X0, sn, 0, k - 2);
      else FILL();
      ++k;
    };
#pragma unroll
    for (int j = 1; j < NG; ++j) {
      grp4(G[j], WG[j], X1, true, mid); if (reload) { ldg(j, sn, 0); SBAR(); } else FILL();
      grp4(U[j], WU[j], X1, true, mid); if (reload) { ldu(j, sn, 0); SBAR(); } else FILL();
    }
#pragma unroll
    for (int j = 0; j < NA; ++j) { grp4(AB[j], WD[j], X1, false, mid); if (reload) { ldd(j, sn, 0); SBAR(); } else FILL(); }
  };

#pragma unroll
  for (int j = 0; j < NG; ++j)
#pragma unroll
    for (int t = 0; t < 4; ++t) { asm volatile("" : "+v"(G[j][t])); asm volatile("" : "+v"(U[j][t])); }
#pragma unroll
  for (int j = 0; j < NA; ++j)
#pragma unroll
    for (int t = 0; t < 4; ++t) asm volatile("" : "+v"(AB[j][t]));
  PAD_MFMA_IN();
  STAMP(1);
  BAR();
  STAMP(2);
  ldg(0, lds, 0); ldu(0, lds, 0); ldx(X0, lds, 0);
#pragma unroll
  for (int j = 1; j < NG; ++j) { ldg(j, lds, 0); ldu(j, lds, 0); }
#pragma unroll
  for (int j = 0; j < NA; ++j) ldd(j, lds, 0);
  SBAR();
  int slot = 0;
#pragma unroll 1
  for (int s = 0; s < K1_NST; ++s) {
    blockA(lds + slot * K1_STAGE);
    if (s >= PF0 && s < PF0 + 4) { pf4(cp, lds_addr(lds) + L_DUMMY); cp += 2 * 4096; }
    blockB_head();
    if (s == 0 && wm == 0 && wn == 0) {
      if (PF0 == 0) { WAITV(1); } else { WAITV(0); }
      asm volatile("" : "+v"(nonce));
      if (lane == 0) *(volatile LAS unsigned*)(lds + L_WORDS + 16) = (MODE == 0) ? nonce * 0x9E3779B1u + 0x7F4A7C15u : 0x51u;
    }
    WAITL0();
    STAMP(19 + s);
    BAR();
    STAMP(7 + s);
    slot = (slot == 2) ? 0 : slot + 1;
    blockB_rest(lds + slot * K1_STAGE, true);
  }
  PAD_MFMA_OUT();
  STAMP(3);
  const lptr cs = lds + L_CS;
  const lptr stats = lds + L_STATS;
  {
    const rsrc_t rab = mk_rsrc(ws + WS_AB, 2 * 28 * 64 * 1024);
#pragma unroll
    for (int pi = 0; pi < 2; ++pi) {
      const lptr sp = stats + (64 * wm + 32 * pi + 8 * g) * 8;
      const f32x4 s0 = *(LAS f32x4*)(sp), s1 = *(LAS f32x4*)(sp + 16), s2 = *(LAS f32x4*)(sp + 32), s3 = *(LAS f32x4*)(sp + 48);
#pragma unroll
      for (int j = 0; j < NA; ++j) {
        const float c = *(LAS float*)(cs + (2 * NBK + NA * wn + j) * 64 + i16 * 4);
        const f32x4 a_ = AB[j][2 * pi], b = AB[j][2 * pi + 1];
        const int ks = mt * 4 + wm * 2 + pi;
        u32x4 o;
        o[0] = pk_bf16((a_[0] - s0[0] * c) * s0[1], (a_[1] - s0[2] * c) * s0[3]);
        o[1] = pk_bf16((a_[2] - s1[0] * c) * s1[1], (a_[3] - s1[2] * c) * s1[3]);
        o[2] = pk_bf16((b[0] - s2[0] * c) * s2[1], (b[1] - s2[2] * c) * s2[3]);
        o[3] = pk_bf16((b[2] - s3[0] * c) * s3[1], (b[3] - s3[2] * c) * s3[3]);
        __builtin_amdgcn_raw_buffer_store_b128(o, rab, lane * 16 + ((a0 + NA * wn + j) * 64 + ks) * 1024, 0, STAUX);
      }
    }
  }
  {
    lptr hd = lds + L_HD + (2 * wn + wm) * (NG * 4096) + lane * 16;
#pragma unroll
    for (int j = 0; j < NG; ++j)
#pragma unroll
      for (int tl = 0; tl < 2; ++tl) {
        *(LAS f32x4*)(hd + ((j * 2 + 0) * 2 + tl) * 1024) = G[j][2 + tl];
        *(LAS f32x4*)(hd + ((j * 2 + 1) * 2 + tl) * 1024) = U[j][2 + tl];
      }
  }
  WAITV(0);
  if (lane == 0) {
    const unsigned n = __hip_atomic_fetch_add((LAS unsigned*)(lds + L_WORDS), 1u, __ATOMIC_RELAXED, __HIP_MEMORY_SCOPE_WORKGROUP);
    if (n == 3u) st_flag(ws + WS_FAB + (mt * 16 + grp) * 64, get_tag(lds));
  }
  STAMP(5);
  WAITL0(); BAR();
  STAMP(14);
  const rsrc_t rh = mk_rsrc(ws + WS_H, 128 * 56 * 1024);
  const int tt0 = mt * 8 + wm * 4;
  f32x2 st[2];
#pragma unroll
  for (int t = 0; t < 2; ++t) st[t] = *(LAS f32x2*)(stats + (64 * wm + 8 * (i16 >> 2) + 4 * t + (i16 & 3)) * 8);
#pragma unroll
  for (int pp = 0; pp < NG / 2; ++pp) {
    const int ks = ((f0 >> 4) + NG * wn + 2 * pp) >> 1;
    f32x4 cg[2], cu[2], gq[2][2], uq[2][2];
#pragma unroll
    for (int j = 0; j < 2; ++j) {
      cg[j] = *(LAS f32x4*)(cs + (NG * wn + 2 * pp + j) * 64 + g * 16);
      cu[j] = *(LAS f32x4*)(cs + (NBK + NG * wn + 2 * pp + j) * 64 + g * 16);
      gq[j][0] = G[2 * pp + j][0]; gq[j][1] = G[2 * pp + j][1]; uq[j][0] = U[2 * pp + j][0]; uq[j][1] = U[2 * pp + j][1];
    }
    h_batch(gq, uq, cg, cu, st, rh, lane * 16 + (tt0 * 56 + ks) * 1024);
  }
  STAMP(4);
}

template <int NG>
__device__ __forceinline__ void p1_helper_epilogue(lptr lds, char* ws, int mt, int f0, int h, int lane) {
  constexpr int NBK = 2 * NG;
  const int wm = h & 1, wn = h >> 1, i16 = lane & 15, g = lane >> 4;
  const lptr cs = lds + L_CS, stats = lds + L_STATS;
  const rsrc_t rh = mk_rsrc(ws + WS_H, 128 * 56 * 1024);
  const int tt0 = mt * 8 + wm * 4 + 2;
  lptr hd = lds + L_HD + h * (NG * 4096) + lane * 16;
  f32x2 st[2];
#pragma unroll
  for (int t = 0; t < 2; ++t) st[t] = *(LAS f32x2*)(stats + (64 * wm + 32 + 8 * (i16 >> 2) + 4 * t + (i16 & 3)) * 8);
#pragma unroll
  for (int pp = 0; pp < NG / 2; ++pp) {
    const int ks = ((f0 >> 4) + NG * wn + 2 * pp) >> 1;
    f32x4 cg[2], cu[2], gq[2][2], uq[2][2];
#pragma unroll
    for (int j = 0; j < 2; ++j) {
      cg[j] = *(LAS f32x4*)(cs + (NG * wn + 2 * pp + j) * 64 + g * 16);
      cu[j] = *(LAS f32x4*)(cs + (NBK + NG * wn + 2 * pp + j) * 64 + g * 16);
#pragma unroll
      for (int tl = 0; tl < 2; ++tl) {
        gq[j][tl] = *(LAS f32x4*)(hd + (((2 * pp + j) * 2 + 0) * 2 + tl) * 1024);
        uq[j][tl] = *(LAS f32x4*)(hd + (((2 * pp + j) * 2 + 1) * 2 + tl) * 1024);
      }
    }
    h_batch(gq, uq, cg, cu, st, rh, lane * 16 + (tt0 * 56 + ks) * 1024);
  }
}

template <int MODE, bool TB>
__device__ __forceinline__ void p1_helper(lptr lds, Args a, int xcd, int idx, int mt, int grp, int f0, int a0, int h, int lane) {
  constexpr int NBK = TB ? 4 : 8, WSZ = NBK * 8 * 256;
  constexpr int NWP = TB ? 4 : 8, NDP = TB ? 4 : 1, PER = 4 + NWP + NDP;
  const int mh = xcd & 1, nq = xcd >> 1;
  const int up = h >> 1;
  const u32x4 rx = mk_srd(a.x, S * D * 2);
  const u32x4 rw = mk_srd(up ? a.Wu : a.Wg, D * F * 2);
  const u32x4 rd = mk_srd(a0 >= 28 ? a.Di : a.Dr, D * D * 2);
  const int e0 = (a0 >= 28 ? a0 - 28 : a0) * 16;
  const int r8 = lane >> 3, c8 = lane & 7;
  int vx[2];
  vx[0] = r8 * 896 + (c8 ^ (r8 & 3)) * 16;
  vx[1] = r8 * 896 + (c8 ^ ((r8 & 3) | 4)) * 16;
  const int vd = r8 * 896 + (c8 ^ r8) * 16;
  int vw[2];
  {
    const int f0_ = (r8 >> 1) & 1;
    vw[0] = r8 * (F * 2) + f0 * 2 + ((((c8 >> 1) ^ f0_) * 2) + (c8 & 1)) * 16;
    vw[1] = r8 * (F * 2) + f0 * 2 + ((((c8 >> 1) ^ (f0_ | 2)) * 2) + (c8 & 1)) * 16;
  }
  const unsigned l0 = lds_addr(lds);
  const unsigned dummy = l0 + L_DUMMY + (h & 1) * 256;
  const int sxb = (mt * 128 + 32 * h) * 896, swb = 32 * (h & 1) * (F * 2), sdb = (e0 + 8 * NDP * h) * 896;
  const unsigned lxb = l0 + 4 * h * 1024, lwb = l0 + K1_WO + up * WSZ + NWP * (h & 1) * 1024, ldb = l0 + K1_WO + 2 * WSZ + NDP * h * 1024;
  auto piece = [&](int s, int slot, int i) {
    const unsigned so = slot * K1_STAGE;
    if (i < 4) dma16(rx, lxb + so + i * 1024, vx[i & 1], sxb + s * 128 + i * 8 * 896);
    else if (i < 4 + NWP) {
      const int j = i - 4, kb = TB ? j : j >> 1, cq = TB ? 0 : j & 1;
      dma16(rw, lwb + so + j * 1024, vw[kb & 1], swb + s * 64 * (F * 2) + kb * 8 * (F * 2) + cq * 128);
    } else dma16(rd, ldb + so + (i - 4 - NWP) * 1024, vd, sdb + s * 128 + (i - 4 - NWP) * 8 * 896);
  };
  auto issue_a = [&](int s, int slot) {
#pragma unroll
    for (int i = 0; i < 8; ++i) piece(s, slot, i);
  };
  auto issue_b = [&](int s, int slot) {
#pragma unroll
    for (int i = 8; i < PER; ++i) piece(s, slot, i);
  };
  const int ln2 = lane >> 1, hb = (lane & 1) * 64;
  const char* pfx = (const char*)a.x + (size_t)(1024 * mh + 32 * idx + ln2) * 896 + hb;
  STAMP(1);
  issue_a(0, 0); issue_b(0, 0);
  STAMP(5);
  launder(a, xcd, idx);
  char* ws = a.ws;
  const bool wjob = idx < 28;
  const int wj_mat = idx >= 14, wj_cb = (idx - 14 * wj_mat) >> 1, wj_kh = idx & 1;
  u32x4 wx[7];
  {
    const u32x4 rj = mk_srd(wj_mat ? a.Wu : a.Wg, D * F * 2);
    const int vj = r8 * (F * 2) + c8 * 16 + (448 * nq + 64 * (wjob ? wj_cb : 0)) * 2;
    const int sj = (224 * (wjob ? wj_kh : 0) + 56 * h) * (F * 2);
    asm volatile("buffer_load_dwordx4 %0, %7, %8, %9 offen\n\tbuffer_load_dwordx4 %1, %7, %8, %10 offen\n\tbuffer_load_dwordx4 %2, %7, %8, %11 offen\n\t"
                 "buffer_load_dwordx4 %3, %7, %8, %12 offen\n\tbuffer_load_dwordx4 %4, %7, %8, %13 offen\n\tbuffer_load_dwordx4 %5, %7, %8, %14 offen\n\t"
                 "buffer_load_dwordx4 %6, %7, %8, %15 offen"
                 : "=&v"(wx[0]), "=&v"(wx[1]), "=&v"(wx[2]), "=&v"(wx[3]), "=&v"(wx[4]), "=&v"(wx[5]), "=&v"(wx[6])
                 : "v"(vj), "s"(rj), "s"(sj), "s"(sj + 8 * F * 2), "s"(sj + 16 * F * 2), "s"(sj + 24 * F * 2), "s"(sj + 32 * F * 2), "s"(sj + 40 * F * 2), "s"(sj + 48 * F * 2)
                 : "memory");
  }
  RowJobs rj_;
  rj_.load(a, nq, idx, mt, grp, h, lane);
  pf4(pfx + (1 + h) * 128, dummy);
  if (h == 3 && lane < 4) ((LAS unsigned*)(lds + L_WORDS))[lane] = 0u;
  WAITL0(); WAITV(12);
  STAMP(7); BAR();
  STAMP(2);
#pragma unroll
  for (int i = 0; i < PER; ++i) piece(1, 1, i);
  issue_a(2, 2);
  if (h < 2) pf4(pfx + (5 + h) * 128, dummy);
  WAITL0();
  if (h < 2) { WAITV(9); } else { WAITV(8); }
  asm volatile("" : "+v"(wx[0]), "+v"(wx[1]), "+v"(wx[2]), "+v"(wx[3]), "+v"(wx[4]), "+v"(wx[5]), "+v"(wx[6]));
  rj_.landed();
  BAR();
  const unsigned tag1 = get_tag(lds);
  float cw[8];
#pragma unroll
  for (int k = 0; k < 8; ++k) cw[k] = 0.f;
  constexpr bool TBc = TB;
  unsigned long long g0 = 0, g1 = 0, g2 = 0, g3 = 0;
  const bool wdjob = blockIdx.x < 196;
  const int wd_kp = blockIdx.x % 28, wd_cg = blockIdx.x / 28;
#pragma unroll
  for (int s = 1; s < K1_NST; ++s) {
    const int s1_ = (s + 1) % 3, s2_ = (s + 2) % 3;
    if (s == 1) {
#pragma unroll
      for (int i = 0; i < PER; ++i) {
        if (i < PER - 8) piece(2, s1_, 8 + i); else piece(3, s2_, i - (PER - 8));
        SBAR();
        if (i < 7) {
#pragma unroll
          for (int k = 0; k < 4; ++k) { cw[2 * k] += bf_lo(wx[i][k]); cw[2 * k + 1] += bf_hi(wx[i][k]); }
        }
        SBAR();
      }
    } else {
      if (s + 1 < K1_NST) issue_b(s + 1, s1_);

      if (s + 2 < K1_NST) issue_a(s + 2, s2_);
    }
    if (s == 1) {
      pf4((const char*)a.Wd + (size_t)(64 * wd_kp + 16 * h + ((lane >> 1) & 15)) * 896 + (wdjob ? wd_cg : 0) * 128 + (lane & 1) * 64, dummy);
#pragma unroll
      for (int k = 0; k < 8; ++k) {
        cw[k] += __builtin_bit_cast(float, __builtin_amdgcn_update_dpp(0, __builtin_bit_cast(int, cw[k]), 0x128, 0xf, 0xf, true));
        const auto p16 = __builtin_amdgcn_permlane16_swap(__float_as_uint(cw[k]), __float_as_uint(cw[k]), false, false);
        cw[k] = __uint_as_float(p16[0]) + __uint_as_float(p16[1]);
        const auto p32 = __builtin_amdgcn_permlane32_swap(__float_as_uint(cw[k]), __float_as_uint(cw[k]), false, false);
        cw[k] = __uint_as_float(p32[0]) + __uint_as_float(p32[1]);
      }
      if (lane < 8) {
        *(LAS f32x4*)(lds + L_CS + h * 256 + lane * 32) = f32x4{cw[0], cw[1], cw[2], cw[3]};
        *(LAS f32x4*)(lds + L_CS + h * 256 + lane * 32 + 16) = f32x4{cw[4], cw[5], cw[6], cw[7]};
      }
    }
    if (s == 2) rj_.sums(lane);
    if (s == 3) rj_.finish(ws, tag1, nq, idx, mt, grp, h, lane);
    if (s == 2 && h == 0 && wjob) {
      const float t = (*(LAS float*)(lds + L_CS + lane * 4) + *(LAS float*)(lds + L_CS + 256 + lane * 4)) +
                      (*(LAS float*)(lds + L_CS + 512 + lane * 4) + *(LAS float*)(lds + L_CS + 768 + lane * 4));
      __hip_atomic_store((GAS unsigned long long*)(ws + WS_CW + (size_t)((wj_mat * 2 + wj_kh) * F + 448 * nq + 64 * wj_cb + lane) * 8),
                         ((unsigned long long)tag1 << 32) | __float_as_uint(t), __ATOMIC_RELAXED, __HIP_MEMORY_SCOPE_AGENT);
    }
    if (s == 4 && h == 0 && lane == 0) st_flag(ws + WS_ARRIVE + blockIdx.x * 64, tag1);
    if (s == 5) {
      if (wdjob) {
        const u32x4 rwd = mk_srd(a.Wd, F * D * 2);
        const int rs = (lane & 15) >> 1, half = lane & 1, b = lane >> 4;
        const int vq = rs * 896 + (64 * wd_cg + 16 * b + 8 * half) * 2;
#pragma unroll
        for (int i = 0; i < 2; ++i) dma16(rwd, l0 + s2_ * K1_STAGE + (2 * h + i) * 1024, vq, (64 * wd_kp + 8 * (2 * h + i)) * 896);
      }
    }
    const int gc = 64 * h + lane;
    constexpr int NWC = TBc ? 128 : 256, CPM = NWC / 2;
    const bool isw = gc < NWC;
    const char* p_st = ws + WS_ST + (size_t)(mt * 128 + 32 * h + (lane & 31)) * 8;
    const char* p_c0 = ws + (isw ? WS_CW + (size_t)((gc / CPM) * 2 * F + f0 + gc % CPM) * 8 : WS_CD + (size_t)(a0 * 16 + (gc - NWC)) * 8);
    const char* p_c1 = isw ? p_c0 + (size_t)F * 8 : p_c0;
    const char* p_d = ws + WS_CD + (size_t)(a0 * 16 + (lane & 31)) * 8;
    if (s == 5)
      asm volatile("global_load_dwordx2 %0, %4, off sc1\n\tglobal_load_dwordx2 %1, %5, off sc1\n\tglobal_load_dwordx2 %2, %6, off sc1\n\tglobal_load_dwordx2 %3, %7, off sc1"
                   : "=&v"(g0), "=&v"(g1), "=&v"(g2), "=&v"(g3) : "v"(p_st), "v"(p_c0), "v"(p_c1), "v"(p_d) : "memory");
    if (s == 6) {
      WAITV(DFTPF == 2 ? 2 : 0);
      asm volatile("" : "+v"(g0), "+v"(g1), "+v"(g2), "+v"(g3));
      auto ld_gran = [&](const char* p) { return __hip_atomic_load((const GAS unsigned long long*)p, __ATOMIC_RELAXED, __HIP_MEMORY_SCOPE_AGENT); };
      for (unsigned sp = 0; sp < SPIN_MAX; ++sp) {
        if (__all((unsigned)(g0 >> 32) == tag1 && (unsigned)(g1 >> 32) == tag1 && (unsigned)(g2 >> 32) == tag1 && (unsigned)(g3 >> 32) == tag1)) break;
        __builtin_amdgcn_s_sleep(2);
        g0 = ld_gran(p_st); g1 = ld_gran(p_c0); g2 = ld_gran(p_c1); g3 = ld_gran(p_d);
      }
      if (lane < 32) *(LAS f32x2*)(lds + L_STATS + (32 * h + lane) * 8) = f32x2{bf_lo((unsigned)g0), 1.0f / bf_hi((unsigned)g0)};
      *(LAS float*)(lds + L_CS + gc * 4) = isw ? __uint_as_float((unsigned)g1) + __uint_as_float((unsigned)g2) : __uint_as_float((unsigned)g1);
      if (!TBc && h == 0 && lane < 32) *(LAS float*)(lds + L_CS + (256 + lane) * 4) = __uint_as_float((unsigned)g3);
    }
    WAITL0();
    if (s == 1) { WAITV(9); } else if (s + 2 < K1_NST) { WAITV(8); } else if (s == 5) { if (wdjob) { WAITV(6 + (DFTPF == 2 ? 2 : 0)); } else { WAITV(4 + (DFTPF == 2 ? 2 : 0)); } }
    STAMP(7 + s);
    BAR();
  }
  __builtin_amdgcn_s_setprio(0);
  if (h == 3 && wdjob) {
    const int i16 = lane & 15, g = lane >> 4, q = i16 >> 2, p = i16 & 3;
    const rsrc_t rt = mk_rsrc(ws + WS_WDT, 28 * 56 * 1024);
    lptr b0 = lds + 1 * K1_STAGE + (g >> 1) * 1024 + ((g & 1) * 4 + q) * 32 + p * 8;
#pragma unroll
    for (int kk = 0; kk < 2; ++kk)
#pragma unroll
      for (int u = 0; u < 4; ++u) {
        const bf16x8 f = ldstr(b0 + kk * 4096 + u * 256, b0 + kk * 4096 + u * 256 + 2048);
        __builtin_amdgcn_raw_buffer_store_b128(__builtin_bit_cast(u32x4, f), rt, lane * 16 + ((4 * wd_cg + u) * 56 + 2 * wd_kp + kk) * 1024, 0, STAUX);
      }
    WAITL0();
  }
  BAR();
  p1_helper_epilogue<TB ? 2 : 4>(lds, ws, mt, f0, h, lane);
}

template <int MODE, int NB, int JO>
__device__ __forceinline__ void p2_loader(lptr lds, const Args& a, int xcd, int idx, int r, int m, int n, int lw, int lane) {
  char* ws = a.ws;
  constexpr int PER = 4 + NB;
  const u32x4 rc = mk_srd((r >> 1) ? a.Ci : a.Cr, S * S * 2);
  const u32x4 rb = mk_srd(ws + WS_AB, 2 * 28 * 64 * 1024);
  const u32x4 rh = mk_srd(ws + WS_H, 128 * 56 * 1024);
  const u32x4 rw = mk_srd(ws + WS_WDT, 28 * 56 * 1024);
  const int r8 = lane >> 3, c8 = lane & 7;
  int va[2];
  va[0] = r8 * 4096 + (c8 ^ (r8 & 3)) * 16;
  va[1] = r8 * 4096 + (c8 ^ ((r8 & 3) | 4)) * 16;
  const int vl = lane * 16;
  const int k0 = 1024 * (r & 1), p = r >> 1;
  const int j0 = (lw < 2) ? 4 * lw : 8 + 3 * (lw - 2);
  const unsigned l0 = lds_addr(lds);
  const unsigned dummy = l0 + L_DUMMY + (lw & 1) * 256;
  const int sab = (128 * m + 32 * lw) * 4096 + k0 * 2;
  const int shb = ((8 * m + 2 * lw) * 56 + 14 * r) * 1024;
  const unsigned lab = l0 + 4 * lw * 1024, lbb = l0 + 16384 + j0 * 1024;
  const int sbb = ((p * 28 + 7 * n + (j0 >> 1)) * 64 + (k0 >> 5)) * 1024, swb = ((7 * n + (j0 >> 1)) * 56 + 14 * r) * 1024;
  auto issueAy = [&](int s, int slot) {
    const unsigned so = slot * P_STAGE; const int b = sab + 128 * (s & 15);
#pragma unroll
    for (int i = 0; i < 4; ++i) dma16(rc, lab + so + i * 1024, va[i & 1], b + i * 8 * 4096);
  };
  auto issueAd = [&](int sd, int slot) {
    const unsigned so = slot * P_STAGE; const int b = shb + 2048 * sd;
#pragma unroll
    for (int i = 0; i < 4; ++i) dma16c(rh, lab + so + i * 1024, vl, b + (i >> 1) * 56 * 1024 + (i & 1) * 1024);
  };
  auto issueBy = [&](int s, int slot) {
    const unsigned so = slot * P_STAGE; const int b = sbb + 2048 * (s & 15);
#pragma unroll
    for (int j = 0; j < NB; ++j) dma16c(rb, lbb + so + j * 1024, vl, b + ((j + JO) >> 1) * 65536 + ((j + JO) & 1) * 1024);
  };
  auto issueBd = [&](int sd, int slot) {
    const unsigned so = slot * P_STAGE; const int b = swb + 2048 * sd;
#pragma unroll
    for (int j = 0; j < NB; ++j) dma16c(rw, lbb + so + j * 1024, vl, b + ((j + JO) >> 1) * 57344 + ((j + JO) & 1) * 1024);
  };
  __builtin_amdgcn_s_setprio(0);
  const unsigned tag = get_tag(lds);
  if (MODE != 2 && lw == 3 && blockIdx.x < 196) {
    WAITV(0);
    if (lane == 0) st_flag(ws + WS_FWD + blockIdx.x * 64, tag);
  }
  issueAy(0, 0); issueAy(1, 1);
  if (lw == 3) {
    const int mtp = 8 * (r & 1) + (lane & 7), j = (lane >> 3) & 3;
    const bool act = lane < 32 && ((n & 1) == 0 || j == 3);
    poll_all(ws + WS_FAB + (mtp * 16 + (2 * p + (n >> 1)) * 4 + j) * 64, act, tag);
    if (lane == 0) *(volatile LAS unsigned*)(lds + L_WORDS + 8) = 1u;
  } else lds_spin(lds + L_WORDS + 8);
  STAMP(16);
  issueBy(0, 0);
  {
    const int l = (lane >> 1) < 28 ? (lane >> 1) : 27;
    const int sl = l >= 14, li = 14 * idx + l - 14 * sl;
    const char* bp = ws + WS_AB + ((size_t)(p * 28 + (li >> 4)) * 64 + 32 * (r & 1) + 2 * (4 * lw + sl) + ((li >> 3) & 1)) * 1024 + (li & 7) * 128 + (lane & 1) * 64;
    pf4c(bp, dummy); pf4c(bp + 4 * 1024, dummy);
  }
  issueBy(1, 1);
  __builtin_amdgcn_s_setprio(3);
  WAITV(NB + 2); BAR();
  if (MODE == 0 && lane == 0) {
    const unsigned cnt = __hip_atomic_fetch_add((LAS unsigned*)(lds + L_WORDS + 4), 1u, __ATOMIC_RELAXED, __HIP_MEMORY_SCOPE_WORKGROUP);
    if (cnt == 7u) st_flag(ws + WS_FH + (((xcd & 1) * 8 + (idx & 7)) * 16 + (xcd >> 1) * 4 + (idx >> 3)) * 64, tag);
  }
  issueAy(2, 2); issueBy(2, 2);
  WAITV(PER); BAR();
  issueAy(3, 3); issueBy(3, 3); issueAy(4, 4); issueBy(4, 4);
  WAITV(2 * PER); BAR();
  int slot = 0;
  auto ystep = [&](int s) { issueAy(s + 3, slot); issueBy(s + 3, slot); slot = (slot == 4) ? 0 : slot + 1; WAITV(2 * PER); BAR(); };
  auto dstep = [&](int sd) { issueAd(sd, slot); issueBd(sd, slot); slot = (slot == 4) ? 0 : slot + 1; WAITV(2 * PER); BAR(); };
#pragma unroll 1
  for (int s = 2; s < P_NY - 9; ++s) ystep(s);
  const int cg0 = (112 * n) >> 6, cg1 = (112 * n + 111) >> 6, jj = lane - 4;
  const bool fact = lane < 4 || (jj < 21 && cg0 + jj / 7 <= cg1);
  const char* fp = lane < 4 ? ws + WS_FH + (m * 16 + 4 * r + lane) * 64 : ws + WS_FWD + ((fact ? cg0 + jj / 7 : cg0) * 28 + 7 * r + (fact ? jj % 7 : 0)) * 64;
  unsigned fval = 0;
  if (lw == 3) asm volatile("global_load_dword %0, %1, off sc1" : "+v"(fval) : "v"(fp) : "memory");
#pragma unroll 1
  for (int s = P_NY - 9; s < P_NY - 6; ++s) ystep(s);
  if (lw == 3) {
    asm volatile("" : "+v"(fval));
    if (!__all(!fact || fval == tag)) poll_all(fp, fact, tag);
    if (lane == 0) *(volatile LAS unsigned*)(lds + L_WORDS + 12) = 1u;
  }
#pragma unroll 1
  for (int s = P_NY - 6; s < P_NY - 3; ++s) ystep(s);
  if (lw != 3) lds_spin(lds + L_WORDS + 12);
#pragma unroll 1
  for (int sd = 0; sd < P_ND; ++sd) dstep(sd);
  WAITV(PER); BAR();
  WAITV(0); BAR();
}

template <int MODE>
__global__ __launch_bounds__(512, 2) void fused_kernel(Args a) {
  extern __shared__ __attribute__((aligned(1024))) char smem[];
  lptr lds = (lptr)smem;
  char* ws = a.ws;
  const int tid = threadIdx.x, lane = tid & 63;
  const int wid = __builtin_amdgcn_readfirstlane(tid >> 6);
  int xcd = blockIdx.x & 7, idx = blockIdx.x >> 3;
  STAMP(0); STAMPR(30);
  if (wid == 3) {
    unsigned long long pc;
    asm volatile("s_getpc_b64 %0" : "=s"(pc));
    const unsigned ln = 13 * idx + ((lane >> 1) < 13 ? (lane >> 1) : 12);
    pf4((const char*)(pc & ~127ull) + (ln * 128 < CODE_PF ? ln * 128 : 0) + (lane & 1) * 64, lds_addr(lds) + L_DUMMY);
  }
  if (wid >= 4) __builtin_amdgcn_s_setprio(3);

  if (MODE != 2) {
    const int nq = xcd >> 1;
    const int mt = (xcd & 1) * 8 + (idx & 7), j = idx >> 3;
    const bool tb = j == 3;
    const int grp = nq * 4 + j;
    const int f0 = 448 * nq + 128 * j, a0 = 14 * nq + 2 * j;
    if (wid < 4) {
      if (tb) p1_consumer<MODE, 2, 4>(lds, a, xcd, idx, mt, grp, f0, a0, wid & 1, wid >> 1, lane);
      else p1_consumer<MODE, 4, 1>(lds, a, xcd, idx, mt, grp, f0, a0, wid & 1, wid >> 1, lane);
    } else {
      const int h = wid - 4;
      if (tb) p1_helper<MODE, true>(lds, a, xcd, idx, mt, grp, f0, a0, h, lane);
      else p1_helper<MODE, false>(lds, a, xcd, idx, mt, grp, f0, a0, h, lane);
    }
  }
  if (MODE == 1) return;
  if (MODE == 2) { if (tid < 4) ((LAS unsigned*)(lds + L_WORDS))[tid] = 0u; if (tid == 4) ((LAS unsigned*)(lds + L_WORDS))[4] = 0x51u; __syncthreads(); }

  launder(a, xcd, idx);
  ws = a.ws;
  const int p1_mt = (xcd & 1) * 8 + (idx & 7), p1_grp = (xcd >> 1) * 4 + (idx >> 3);
  const int r = xcd >> 1, m = 8 * (xcd & 1) + (idx & 7), n = idx >> 3;
  const int tile = m * 4 + n;
  const int i16 = lane & 15, g = lane >> 4;
  f32x4 accY[7][2], acc[7][2];
  if (wid == 7) {
    p2_loader<MODE, 3, 1>(lds, a, xcd, idx, r, m, n, 3, lane);
  } else if (wid == 6) {
    p2_loader<MODE, 3, 0>(lds, a, xcd, idx, r, m, n, 2, lane);
  } else if (wid >= 4) {
    p2_loader<MODE, 4, 0>(lds, a, xcd, idx, r, m, n, wid - 4, lane);
  } else {
    const int w = wid;
    const int rowt = 32 * w + 8 * (i16 >> 2) + (i16 & 3);
    const int vt0 = rowt * 128 + ((g) ^ (i16 & 7)) * 16, vt1 = rowt * 128 + ((g + 4) ^ (i16 & 7)) * 16;
    const int ve = 16384 + lane * 16;
    const int vh = (4 * w) * 1024 + lane * 16;
    bf16x8 T0[4], T1[4], E[14];
    auto ldTy = [&](bf16x8 (&T)[4], lptr st) {
      T[0] = ldsr128(st + vt0); T[1] = ldsr128(st + vt0 + 512); T[2] = ldsr128(st + vt1); T[3] = ldsr128(st + vt1 + 512);
    };
    auto ldTd = [&](bf16x8 (&T)[4], lptr st) {
      T[0] = ldsr128(st + vh); T[1] = ldsr128(st + vh + 2048); T[2] = ldsr128(st + vh + 1024); T[3] = ldsr128(st + vh + 3072);
    };
    auto ldE = [&](int j, lptr st) { E[j] = ldsr128(st + ve + ((j % 7) * 2 + j / 7) * 1024); };
    auto mm = [&](int j, bf16x8 (&T)[4]) {
      mfma_ip(acc[j % 7][0], E[j], T[(j / 7) * 2]);
      mfma_ip(acc[j % 7][1], E[j], T[(j / 7) * 2 + 1]);
    };
#define GHEAD 3
    constexpr int GH = GHEAD;
    auto ldT1 = [&](bf16x8 (&T)[4], lptr st, int i, int NT) {
      if (NT == 0) T[i] = ldsr128(st + (i >= 2 ? vt1 : vt0) + (i & 1) * 512);
      else T[i] = ldsr128(st + vh + (i & 1) * 2048 + (i >> 1) * 1024);
    };
    auto rest = [&](bf16x8 (&Tc)[4], bf16x8 (&Tn)[4], lptr sn, int NT) {
#pragma unroll
      for (int j = GH; j < 14; ++j) {
        const int k = j - GH;
        mfma_ip(acc[j % 7][0], E[j], Tc[(j / 7) * 2]); SBAR();
        if (NT != 2 && k < 4 + GH) { if (k < 4) ldT1(Tn, sn, k, NT); else ldE(k - 4, sn); SBAR(); }
        mfma_ip(acc[j % 7][1], E[j], Tc[(j / 7) * 2 + 1]); SBAR();
        if (NT != 2) ldE(j, sn); else FILL();
        SBAR();
      }
    };
    auto head = [&](bf16x8 (&Tc)[4]) {
#pragma unroll
      for (int j = 0; j < GH; ++j) { mm(j, Tc); FILL(); }
      SBAR();
    };
#pragma unroll
    for (int u = 0; u < 7; ++u) { acc[u][0] = f32x4{0, 0, 0, 0}; acc[u][1] = f32x4{0, 0, 0, 0}; }
#pragma unroll
    for (int u = 0; u < 7; ++u) { asm volatile("" : "+v"(acc[u][0])); asm volatile("" : "+v"(acc[u][1])); }
    PAD_MFMA_IN();
    if (w != r) {
      const rsrc_t rp = mk_rsrc(ws + WS_PD + (size_t)((tile * 4 + r) * 4 + w) * 7168, 7168);
      const u32x4 sent = {0xffffffffu, 0xffffffffu, 0xffffffffu, 0xffffffffu};
#pragma unroll
      for (int u = 0; u < 7; ++u) {
        __builtin_amdgcn_raw_buffer_store_b128(sent, rp, u * 1024 + lane * 16, 0, 16);
      }
    }
    STAMP(16); STAMPR(28);
    BAR();
    STAMP(17); STAMPR(27);
    ldTy(T0, lds);
#pragma unroll
    for (int j = 0; j < 14; ++j) ldE(j, lds);
    SBAR();
    head(T0);
    WAITL0(); BAR();
    int slot = 1;
#pragma unroll 1
    for (int s = 0; s < P_NY - 2; s += 2) {
      lptr sn = lds + slot * P_STAGE; slot = (slot == 4) ? 0 : slot + 1;
      rest(T0, T1, sn, 0); head(T1);
      WAITL0(); BAR();
      sn = lds + slot * P_STAGE; slot = (slot == 4) ? 0 : slot + 1;
      rest(T1, T0, sn, 0); head(T0);
      WAITL0(); BAR();
      if (YREP > 1 && s == 14) { STAMP(23); }
      if (MODE == 0 && s == 0) {
        WAITV(0);
        if (lane == 0) {
          const unsigned cnt = __hip_atomic_fetch_add((LAS unsigned*)(lds + L_WORDS + 4), 1u, __ATOMIC_RELAXED, __HIP_MEMORY_SCOPE_WORKGROUP);
          if (cnt == 7u) st_flag(ws + WS_FH + (p1_mt * 16 + p1_grp) * 64, get_tag(lds));
        }
      }
    }
    {
      lptr sn = lds + slot * P_STAGE; slot = (slot == 4) ? 0 : slot + 1;
      rest(T0, T1, sn, 0); head(T1);
      WAITL0(); BAR();
      sn = lds + slot * P_STAGE; slot = (slot == 4) ? 0 : slot + 1;
      rest(T1, T0, sn, 1);
    }
    PAD_MFMA_OUT();
    STAMP(18);
    {
      const float sg = (r >> 1) ? -1.0f : 1.0f;
#pragma unroll
      for (int u = 0; u < 7; ++u) { accY[u][0] = acc[u][0] * sg; accY[u][1] = acc[u][1] * sg; acc[u][0] = f32x4{0, 0, 0, 0}; acc[u][1] = f32x4{0, 0, 0, 0}; }
      if (w != r) {
        const rsrc_t rp = mk_rsrc(ws + WS_PY + (size_t)((tile * 4 + r) * 4 + w) * 7168, 7168);
#pragma unroll
        for (int u = 0; u < 7; ++u) {
          const u32x4 o = {pk_bf16(accY[u][0][0], accY[u][0][1]), pk_bf16(accY[u][0][2], accY[u][0][3]),
                           pk_bf16(accY[u][1][0], accY[u][1][1]), pk_bf16(accY[u][1][2], accY[u][1][3])};
          __builtin_amdgcn_raw_buffer_store_b128(o, rp, u * 1024 + lane * 16, 0, 16);
        }
      }
    }
#pragma unroll
    for (int u = 0; u < 7; ++u) { asm volatile("" : "+v"(acc[u][0])); asm volatile("" : "+v"(acc[u][1])); }
    PAD_MFMA_IN();
    head(T0);
    WAITL0(); BAR();
#pragma unroll 1
    for (int s = 0; s < P_ND - 1; s += 2) {
      lptr sn = lds + slot * P_STAGE; slot = (slot == 4) ? 0 : slot + 1;
      rest(T0, T1, sn, 1); head(T1);
      WAITL0(); BAR();
      if (s == 0 && w != r) {
        WAITV(0);
        const unsigned tag = get_tag(lds);
        if (lane == 0) st_flag(ws + WS_FY + ((tile * 4 + r) * 4 + w) * 64, tag);
      }
      sn = lds + slot * P_STAGE; slot = (slot == 4) ? 0 : slot + 1;
      rest(T1, T0, sn, 1); head(T0);
      WAITL0(); if (s + 2 < P_ND - 1) BAR();
    }
    rest(T0, T1, lds, 2);
    PAD_MFMA_OUT();
    STAMP(19); STAMPR(29);
    if (w != r) {
      const rsrc_t rp = mk_rsrc(ws + WS_PD + (size_t)((tile * 4 + r) * 4 + w) * 7168, 7168);
#pragma unroll
      for (int u = 0; u < 7; ++u) {
        u32x4 o = {pk_bf16(acc[u][0][0], acc[u][0][1]), pk_bf16(acc[u][0][2], acc[u][0][3]),
                   pk_bf16(acc[u][1][0], acc[u][1][1]), pk_bf16(acc[u][1][2], acc[u][1][3])};
#pragma unroll
        for (int k = 0; k < 4; ++k) o[k] = o[k] < 0xfffffffeu ? o[k] : 0xfffffffeu;
        __builtin_amdgcn_raw_buffer_store_b128(o, rp, u * 1024 + lane * 16, 0, 16);
      }
    } else {
#pragma unroll
      for (int uu = 0; uu < 7; ++uu) {
        *(LAS f32x4*)(lds + (uu * 4 + 0) * 1024 + lane * 16) = accY[uu][0];
        *(LAS f32x4*)(lds + (uu * 4 + 1) * 1024 + lane * 16) = accY[uu][1];
        *(LAS f32x4*)(lds + (uu * 4 + 2) * 1024 + lane * 16) = acc[uu][0];
        *(LAS f32x4*)(lds + (uu * 4 + 3) * 1024 + lane * 16) = acc[uu][1];
      }
      WAITL0();
    }
    BAR();
  }
  if (wid >= 4) {
    __builtin_amdgcn_s_setprio(0);
    const int hw = wid - 4;
    const int nu = hw < 3 ? 2 : 1;
    const unsigned tag = get_tag(lds);
    const int tok0 = 128 * m + 32 * r + 8 * (i16 >> 2) + (i16 & 3);
    const rsrc_t ry = mk_rsrc(ws + WS_PY, 64 * 16 * 7168), rd = mk_rsrc(ws + WS_PD, 64 * 16 * 7168);
    u32x2 x0[2], x1[2];
    u32x4 fy[2][3], fd[2][3];
#pragma unroll
    for (int k = 0; k < 2; ++k) {
      const int u = 2 * hw + (k < nu ? k : 0);
      const int ecol = 112 * n + 16 * u + 4 * g;
      x0[k] = *(const u32x2*)(a.x + (size_t)tok0 * D + ecol); x1[k] = *(const u32x2*)(a.x + (size_t)(tok0 + 4) * D + ecol);
    }
    {
      const int rr = (r + 1 + (lane % 3)) & 3;
      poll_all(ws + WS_FY + ((tile * 4 + rr) * 4 + r) * 64, lane < 3, tag);
    }
#pragma unroll
    for (int k = 0; k < 2; ++k)
#pragma unroll
      for (int i = 0; i < 3; ++i)
        fy[k][i] = __builtin_amdgcn_raw_buffer_load_b128(ry, ((tile * 4 + ((r + 1 + i) & 3)) * 4 + r) * 7168 + (2 * hw + (k < nu ? k : 0)) * 1024 + lane * 16, 0, 16);
    STAMP(20);
    if (MODE == 0 && blockIdx.x == 0 && hw == 3) {
#pragma unroll 1
      for (int i = 0; i < 4; ++i) poll_all(ws + WS_ARRIVE + (64 * i + lane) * 64, true, tag);
      if (lane == 0) st_flag(ws + WS_NONCE, tag);
    }
    for (unsigned sp = 0; sp < (SPIN_MAX >> 3); ++sp) {
      const unsigned v = lane < 3 ? __builtin_amdgcn_raw_buffer_load_b32(rd, ((tile * 4 + ((r + 1 + lane) & 3)) * 4 + r) * 7168 + 6 * 1024 + 63 * 16 + 12, 0, 16) : 0u;
      if (__all(v != 0xffffffffu)) break;
      __builtin_amdgcn_s_sleep(1);
    }
    for (unsigned sp = 0; sp < (SPIN_MAX >> 3); ++sp) {
      bool ok = true;
#pragma unroll
      for (int k = 0; k < 2; ++k)
#pragma unroll
        for (int i = 0; i < 3; ++i) {
          fd[k][i] = __builtin_amdgcn_raw_buffer_load_b128(rd, ((tile * 4 + ((r + 1 + i) & 3)) * 4 + r) * 7168 + (2 * hw + (k < nu ? k : 0)) * 1024 + lane * 16, 0, 16);
        }
#pragma unroll
      for (int k = 0; k < 2; ++k)
#pragma unroll
        for (int i = 0; i < 3; ++i)
          ok = ok && fd[k][i][0] != 0xffffffffu && fd[k][i][1] != 0xffffffffu && fd[k][i][2] != 0xffffffffu && fd[k][i][3] != 0xffffffffu;
      if (__all(ok)) break;
      __builtin_amdgcn_s_sleep(2);
    }
    STAMP(21); STAMPR(29);
    BAR();
#pragma unroll
    for (int k = 0; k < 2; ++k) {
      if (k < nu) {
        const int u = 2 * hw + k;
        const int ecol = 112 * n + 16 * u + 4 * g;
        f32x4 y0 = *(LAS f32x4*)(lds + (u * 4 + 0) * 1024 + lane * 16), y1 = *(LAS f32x4*)(lds + (u * 4 + 1) * 1024 + lane * 16);
        f32x4 d0 = *(LAS f32x4*)(lds + (u * 4 + 2) * 1024 + lane * 16), d1 = *(LAS f32x4*)(lds + (u * 4 + 3) * 1024 + lane * 16);
#pragma unroll
        for (int i = 0; i < 3; ++i) {
          y0 += f32x4{bf_lo(fy[k][i][0]), bf_hi(fy[k][i][0]), bf_lo(fy[k][i][1]), bf_hi(fy[k][i][1])};
          y1 += f32x4{bf_lo(fy[k][i][2]), bf_hi(fy[k][i][2]), bf_lo(fy[k][i][3]), bf_hi(fy[k][i][3])};
          d0 += f32x4{bf_lo(fd[k][i][0]), bf_hi(fd[k][i][0]), bf_lo(fd[k][i][1]), bf_hi(fd[k][i][1])};
          d1 += f32x4{bf_lo(fd[k][i][2]), bf_hi(fd[k][i][2]), bf_lo(fd[k][i][3]), bf_hi(fd[k][i][3])};
        }
        const u32x2 o0 = {pk_bf16(bf_lo(x0[k][0]) + y0[0] * silu_f(d0[0]), bf_hi(x0[k][0]) + y0[1] * silu_f(d0[1])),
                          pk_bf16(bf_lo(x0[k][1]) + y0[2] * silu_f(d0[2]), bf_hi(x0[k][1]) + y0[3] * silu_f(d0[3]))};
        const u32x2 o1 = {pk_bf16(bf_lo(x1[k][0]) + y1[0] * silu_f(d1[0]), bf_hi(x1[k][0]) + y1[1] * silu_f(d1[1])),
                          pk_bf16(bf_lo(x1[k][1]) + y1[2] * silu_f(d1[2]), bf_hi(x1[k][1]) + y1[3] * silu_f(d1[3]))};
        *(u32x2*)(a.out + (size_t)tok0 * D + ecol) = o0;
        *(u32x2*)(a.out + (size_t)(tok0 + 4) * D + ecol) = o1;
      }
    }
  }
}

#define SPLIT 0
extern "C" void kernel_launch(void* const* d_in, const int* in_sizes, int n_in, void* d_out, int out_size, void* d_ws, size_t ws_size,
                              hipStream_t stream) {
  Args a;
  a.x = (const u16*)d_in[0]; a.Cr = (const u16*)d_in[1]; a.Ci = (const u16*)d_in[2]; a.Dr = (const u16*)d_in[3]; a.Di = (const u16*)d_in[4];
  a.Wg = (const u16*)d_in[6]; a.Wu = (const u16*)d_in[7]; a.Wd = (const u16*)d_in[8];
  a.ws = (char*)d_ws; a.out = (u16*)d_out;
  (void)hipFuncSetAttribute((const void*)fused_kernel<0>, hipFuncAttributeMaxDynamicSharedMemorySize, L_TOTAL);
  fused_kernel<0><<<256, 512, L_TOTAL, stream>>>(a);
}
```

```cpp
#include <hip/hip_runtime.h>
#include <hip/hip_bf16.h>
#include <stdint.h>

typedef unsigned short u16;
typedef __bf16 bf16x8 __attribute__((ext_vector_type(8)));
typedef short s16x8 __attribute__((ext_vector_type(8)));
typedef float f32x4 __attribute__((ext_vector_type(4)));
typedef float f32x2 __attribute__((ext_vector_type(2)));
typedef unsigned u32x4 __attribute__((ext_vector_type(4)));
typedef unsigned u32x2 __attribute__((ext_vector_type(2)));
#define LAS __attribute__((address_space(3)))
#define GAS __attribute__((address_space(1)))
typedef LAS char* lptr;
typedef __amdgpu_buffer_rsrc_t rsrc_t;

constexpr int S = 2048, D = 448, F = 1792;

constexpr size_t WS_NONCE  = 0;
constexpr size_t WS_ARRIVE = 4096;
constexpr size_t WS_FAB    = 32768;
constexpr size_t WS_FH     = 49152;
constexpr size_t WS_FWD    = 65536;
constexpr size_t WS_FY     = 81920;
constexpr size_t WS_FD     = 147456;
constexpr size_t WS_ST     = 212992;
constexpr size_t WS_CW     = 229376;
constexpr size_t WS_CD     = 286720;
constexpr size_t WS_AB     = 327680;
constexpr size_t WS_H      = WS_AB + (size_t)2 * 28 * 64 * 1024;
constexpr size_t WS_WDT    = WS_H + (size_t)128 * 56 * 1024;
constexpr size_t WS_PY     = WS_WDT + (size_t)28 * 56 * 1024;
constexpr size_t WS_PD     = WS_PY + (size_t)64 * 16 * 7 * 1024;

#define STAMP(i)
#define STAMPR(i)
#define SBAR() __builtin_amdgcn_sched_barrier(0)
#define BAR() __builtin_amdgcn_s_barrier()
#define WAITV(n) asm volatile("s_waitcnt vmcnt(%0)" ::"n"(n) : "memory")
#define WAITL0() __builtin_amdgcn_s_waitcnt(0xC07F)
#define CODE_PF 49152
#define DFTPF 0
#define OUTAUX 16
#define PF0 1
#define STAUX 16
#define SPIN_MAX (1u << 18)

__device__ __forceinline__ rsrc_t mk_rsrc(const void* p, unsigned bytes) {
  return __builtin_amdgcn_make_buffer_rsrc((void*)p, 0, bytes, 0x00020000);
}
__device__ __forceinline__ u32x4 mk_srd(const void* p, unsigned bytes) {
  const unsigned long long a = (unsigned long long)p;
  return u32x4{(unsigned)a, (unsigned)(a >> 32) & 0xffffu, bytes, 0x00020000u};
}
__device__ __forceinline__ unsigned lds_addr(lptr p) { return (unsigned)(size_t)p; }
__device__ __forceinline__ void dma16(const u32x4& srd, unsigned dst, int voff, int soff) {
  asm volatile("s_mov_b32 m0, %0\n\ts_nop 0\n\tbuffer_load_dwordx4 %1, %2, %3 offen lds" ::"s"(dst), "v"(voff), "s"(srd), "s"(soff) : "memory");
}
__device__ __forceinline__ void dma16c(const u32x4& srd, unsigned dst, int voff, int soff) {
  asm volatile("s_mov_b32 m0, %0\n\ts_nop 0\n\tbuffer_load_dwordx4 %1, %2, %3 offen sc1 lds" ::"s"(dst), "v"(voff), "s"(srd), "s"(soff) : "memory");
}
__device__ __forceinline__ void pf4c(const char* p, unsigned dst) {
  asm volatile("s_mov_b32 m0, %0\n\ts_nop 0\n\tglobal_load_lds_dword %1, off sc1" ::"s"(dst), "v"(p) : "memory");
}
__device__ __forceinline__ void pf4(const char* p, unsigned dst) {
  asm volatile("s_mov_b32 m0, %0\n\ts_nop 0\n\tglobal_load_lds_dword %1, off" ::"s"(dst), "v"(p) : "memory");
}
__device__ __forceinline__ bf16x8 ldsr128(lptr p) { return *(const LAS bf16x8*)p; }
__device__ __forceinline__ bf16x8 ldstr(lptr p0, lptr p1) {
  typedef short v4i16_t __attribute__((ext_vector_type(4)));
  v4i16_t lo = __builtin_amdgcn_ds_read_tr16_b64_v4i16((LAS v4i16_t*)p0);
  v4i16_t hi = __builtin_amdgcn_ds_read_tr16_b64_v4i16((LAS v4i16_t*)p1);
  s16x8 v = __builtin_shufflevector(lo, hi, 0, 1, 2, 3, 4, 5, 6, 7);
  return __builtin_bit_cast(bf16x8, v);
}
__device__ __forceinline__ f32x4 mfma(bf16x8 a, bf16x8 b, f32x4 c) { return __builtin_amdgcn_mfma_f32_16x16x32_bf16(a, b, c, 0, 0, 0); }
__device__ __forceinline__ void mfma_ip(f32x4& c, const bf16x8& a, const bf16x8& b) {
  asm volatile("v_mfma_f32_16x16x32_bf16 %0, %1, %2, %0" : "+v"(c) : "v"(a), "v"(b));
}
#define FILL() asm volatile("s_nop 0")
#define PAD_MFMA_OUT() asm volatile("s_nop 7\n\ts_nop 3")
#define PAD_MFMA_IN() asm volatile("s_nop 3")
__device__ __forceinline__ unsigned pk_bf16(float lo, float hi) {
  typedef __bf16 bf16x2 __attribute__((ext_vector_type(2)));
  f32x2 v = {lo, hi};
  bf16x2 b = __builtin_convertvector(v, bf16x2);
  return __builtin_bit_cast(unsigned, b);
}
__device__ __forceinline__ float bf_lo(unsigned v) { return __uint_as_float(v << 16); }
__device__ __forceinline__ float bf_hi(unsigned v) { return __uint_as_float(v & 0xffff0000u); }
__device__ __forceinline__ float rbf(float f) { return bf_lo(pk_bf16(f, 0.f)); }
__device__ __forceinline__ float silu_f(float x) {
  float e = __builtin_amdgcn_exp2f(x * -1.4426950409f);
  return x * __builtin_amdgcn_rcpf(1.0f + e);
}
template <int N>
__device__ __forceinline__ void wave_sums(float (&v)[N]) {
#define DPP_STEP(ctrl, rmask) _Pragma("unroll") for (int i = 0; i < N; ++i) \
    v[i] += __builtin_bit_cast(float, __builtin_amdgcn_update_dpp(0, __builtin_bit_cast(int, v[i]), ctrl, rmask, 0xf, true));
  DPP_STEP(0x111, 0xf)
  DPP_STEP(0x112, 0xf)
  DPP_STEP(0x114, 0xf)
  DPP_STEP(0x118, 0xf)
  DPP_STEP(0x142, 0xa)
  DPP_STEP(0x143, 0xc)
#undef DPP_STEP
#pragma unroll
  for (int i = 0; i < N; ++i) v[i] = __builtin_bit_cast(float, __builtin_amdgcn_readlane(__builtin_bit_cast(int, v[i]), 63));
}
__device__ __forceinline__ unsigned ld_flag(const void* p) { return __hip_atomic_load((const GAS unsigned*)p, __ATOMIC_RELAXED, __HIP_MEMORY_SCOPE_AGENT); }
__device__ __forceinline__ void st_flag(void* p, unsigned v) { __hip_atomic_store((GAS unsigned*)p, v, __ATOMIC_RELAXED, __HIP_MEMORY_SCOPE_AGENT); }
__device__ __forceinline__ unsigned get_tag(lptr lds);
__device__ __forceinline__ void poll_all(const char* p, bool active, unsigned tag) {
  for (unsigned sp = 0; sp < SPIN_MAX; ++sp) {
    const unsigned v = active ? ld_flag(p) : tag;
    if (__all(v == tag)) break;
    __builtin_amdgcn_s_sleep(2);
  }
}
__device__ __forceinline__ void lds_spin(lptr p) {
  for (unsigned sp = 0; sp < SPIN_MAX * 4; ++sp) {
    if (*(volatile LAS unsigned*)p != 0u) break;
    __builtin_amdgcn_s_sleep(2);
  }
}

constexpr int K1_STAGE = 53248, K1_NST = 7, K1_WO = 16384;
constexpr int L_MISC = 3 * K1_STAGE;
constexpr int L_STATS = L_MISC;
constexpr int L_CS = L_MISC + 1024;
constexpr int L_WORDS = L_CS + 18 * 64;
constexpr int L_DUMMY = L_WORDS + 128;
constexpr int L_TOTAL = L_DUMMY + 512;
#define YREP 1
constexpr int P_STAGE = 30720, P_NY = 16 * YREP, P_ND = 7, P_NST = P_NY + P_ND;
static_assert(L_TOTAL <= 163840 && 5 * P_STAGE <= L_MISC, "LDS");

__device__ __forceinline__ unsigned get_tag(lptr lds) { return *(volatile LAS unsigned*)(lds + L_WORDS + 16); }

struct Args {
  const u16 *x, *Cr, *Ci, *Dr, *Di, *Wg, *Wu, *Wd;
  char* ws;
  u16* out;
};

__device__ __forceinline__ void launder(Args& a, int& xcd, int& idx) {
  asm volatile("" : "+s"(a.x), "+s"(a.Cr), "+s"(a.Ci), "+s"(a.Dr), "+s"(a.Di));
  asm volatile("" : "+s"(a.Wg), "+s"(a.Wu), "+s"(a.Wd), "+s"(a.ws), "+s"(a.out));
  asm volatile("" : "+s"(xcd), "+s"(idx));
}

struct RowJobs {
  u32x4 v[4];
  float f[2][8], sm[4];
  __device__ __forceinline__ void load(const Args& a, int nq, int idx, int mt, int grp, int h, int lane) {
    const int l56 = lane < 56 ? lane : 55;
    const u16* sp = a.x + (size_t)(mt * 128 + 8 * grp + 2 * h) * D + l56 * 8;
    const u16* dp = (nq >= 2 ? a.Di : a.Dr) + (size_t)(224 * (nq & 1) + 7 * idx + h) * D + l56 * 8;
    const u16* dq = dp + (h < 3 ? 4 * D : 0);
    asm volatile("global_load_dwordx4 %0, %4, off\n\tglobal_load_dwordx4 %1, %4, off offset:896\n\tglobal_load_dwordx4 %2, %5, off\n\tglobal_load_dwordx4 %3, %6, off"
                 : "=&v"(v[0]), "=&v"(v[1]), "=&v"(v[2]), "=&v"(v[3]) : "v"(sp), "v"(dp), "v"(dq) : "memory");
  }
  __device__ __forceinline__ void landed() { asm volatile("" : "+v"(v[0]), "+v"(v[1]), "+v"(v[2]), "+v"(v[3])); }
  __device__ __forceinline__ void sums(int lane) {
#pragma unroll
    for (int r = 0; r < 4; ++r) {
      float t[8];
#pragma unroll
      for (int i = 0; i < 4; ++i) { t[2 * i] = bf_lo(v[r][i]); t[2 * i + 1] = bf_hi(v[r][i]); }
      const float s = ((t[0] + t[1]) + (t[2] + t[3])) + ((t[4] + t[5]) + (t[6] + t[7]));
      sm[r] = lane < 56 ? s : 0.f;
      if (r < 2) {
#pragma unroll
        for (int i = 0; i < 8; ++i) f[r][i] = t[i];
      }
    }
    wave_sums<4>(sm);
  }
  __device__ __forceinline__ void finish(char* ws, unsigned tag, int nq, int idx, int mt, int grp, int h, int lane) {
    float q[2], mean[2];
#pragma unroll
    for (int r = 0; r < 2; ++r) {
      mean[r] = sm[r] * (1.0f / D);
      float t0 = 0.f, t1 = 0.f;
#pragma unroll
      for (int i = 0; i < 4; ++i) {
        const float d0 = f[r][2 * i] - mean[r], d1 = f[r][2 * i + 1] - mean[r];
        t0 = __builtin_fmaf(d0, d0, t0); t1 = __builtin_fmaf(d1, d1, t1);
      }
      q[r] = lane < 56 ? t0 + t1 : 0.f;
    }
    wave_sums<2>(q);
    const int r = lane & 1;
    const unsigned pay = pk_bf16(r ? mean[1] : mean[0], rbf(__builtin_amdgcn_sqrtf(rbf(rbf((r ? q[1] : q[0]) * (1.0f / D)) + 1e-5f))));
    if (lane < 2 || lane == 2 || (lane == 3 && h < 3)) {
      const unsigned v_ = lane < 2 ? pay : __float_as_uint(lane == 2 ? sm[2] : sm[3]);
      const size_t off = lane < 2 ? WS_ST + (size_t)(mt * 128 + 8 * grp + 2 * h + lane) * 8 : WS_CD + (size_t)(224 * nq + 7 * idx + h + 4 * (lane - 2)) * 8;
      __hip_atomic_store((GAS unsigned long long*)(ws + off), ((unsigned long long)tag << 32) | v_, __ATOMIC_RELAXED, __HIP_MEMORY_SCOPE_AGENT);
    }
  }
};

__device__ __forceinline__ void h_batch(const f32x4 (&g)[2][2], const f32x4 (&u)[2][2], const f32x4 (&cg)[2], const f32x4 (&cu)[2], const f32x2 (&st)[2],
                                        rsrc_t rh, int off0) {
  f32x2 gg[8], uu[8], z[8];
#pragma unroll
  for (int i = 0; i < 8; ++i) {
    const int t = i >> 2, j = (i >> 1) & 1, hh = i & 1;
    gg[i] = (f32x2{g[j][t][2 * hh], g[j][t][2 * hh + 1]} - f32x2{cg[j][2 * hh], cg[j][2 * hh + 1]} * st[t].x) * st[t].y;
  }
#pragma unroll
  for (int i = 0; i < 8; ++i) z[i] = gg[i] * ((gg[i] * gg[i]) * -0.1029432397f + -2.302208198f);
#pragma unroll
  for (int i = 0; i < 8; ++i) { z[i].x = __builtin_amdgcn_exp2f(z[i].x); z[i].y = __builtin_amdgcn_exp2f(z[i].y); }
#pragma unroll
  for (int i = 0; i < 8; ++i) {
    const int t = i >> 2, j = (i >> 1) & 1, hh = i & 1;
    uu[i] = ((f32x2{u[j][t][2 * hh], u[j][t][2 * hh + 1]} - f32x2{cu[j][2 * hh], cu[j][2 * hh + 1]} * st[t].x) * st[t].y) * gg[i];
    z[i] = z[i] + 1.0f;
  }
#pragma unroll
  for (int i = 0; i < 8; ++i) { z[i].x = __builtin_amdgcn_rcpf(z[i].x); z[i].y = __builtin_amdgcn_rcpf(z[i].y); }
#pragma unroll
  for (int i = 0; i < 8; ++i) uu[i] = uu[i] * z[i];
#pragma unroll
  for (int tl = 0; tl < 2; ++tl) {
    const u32x4 o = {pk_bf16(uu[4 * tl].x, uu[4 * tl].y), pk_bf16(uu[4 * tl + 1].x, uu[4 * tl + 1].y),
                     pk_bf16(uu[4 * tl + 2].x, uu[4 * tl + 2].y), pk_bf16(uu[4 * tl + 3].x, uu[4 * tl + 3].y)};
    __builtin_amdgcn_raw_buffer_store_b128(o, rh, off0 + tl * 56 * 1024, 0, STAUX);
  }
}
constexpr int L_HD = 61440;

template <int MODE, int NG, int NA>
__device__ __forceinline__ void p1_consumer(lptr lds, Args a, int xcd, int idx, int mt, int grp, int f0, int a0, int wm, int wn, int lane) {
  launder(a, xcd, idx);
  asm volatile("" : "+s"(mt), "+s"(grp), "+s"(f0), "+s"(a0));
  char* ws = a.ws;
  const char* cp = (const char*)((xcd >> 2) ? a.Ci : a.Cr) + (size_t)(1024 * (xcd & 1) + 32 * idx + 8 * (2 * wn + wm) + (lane >> 5)) * 4096 + 2048 * ((xcd >> 1) & 1) + (lane & 31) * 64;
  unsigned nonce = 0;
  if (MODE == 0 && wm == 0 && wn == 0) asm volatile("global_load_dword %0, %1, off sc1" : "+v"(nonce) : "v"(ws + WS_NONCE) : "memory");
  constexpr int NBK = 2 * NG, NCQ = NBK / 4, WSZ = NBK * 8 * 256, KSO = 4 * NCQ * 1024;
  constexpr int GO = K1_WO, UO = K1_WO + WSZ, DO = K1_WO + 2 * WSZ;
  const int i16 = lane & 15, g = lane >> 4, q = i16 >> 2, p = i16 & 3;
  const int rowx = 64 * wm + 8 * (i16 >> 2) + (i16 & 3);
  const int vx0 = rowx * 128 + ((g) ^ (i16 & 7)) * 16;
  const int vx1 = rowx * 128 + ((g + 4) ^ (i16 & 7)) * 16;
  const int fw = ((q >> 1) & 1) | ((g & 1) << 1);
  int vw[NG];
#pragma unroll
  for (int j = 0; j < NG; ++j) { const int nb = NG * wn + j; vw[j] = g * NCQ * 1024 + (nb >> 2) * 1024 + q * 128 + (((nb & 3) ^ fw) * 32) + p * 8; }
  const int vd0 = DO + (16 * NA * wn + i16) * 128 + ((g) ^ (i16 & 7)) * 16;
  const int vd1 = DO + (16 * NA * wn + i16) * 128 + ((g + 4) ^ (i16 & 7)) * 16;

  f32x4 G[NG][4], U[NG][4], AB[NA][4];
#pragma unroll
  for (int j = 0; j < NG; ++j)
#pragma unroll
    for (int t = 0; t < 4; ++t) { G[j][t] = f32x4{0, 0, 0, 0}; U[j][t] = f32x4{0, 0, 0, 0}; }
#pragma unroll
  for (int j = 0; j < NA; ++j)
#pragma unroll
    for (int t = 0; t < 4; ++t) AB[j][t] = f32x4{0, 0, 0, 0};

  bf16x8 X0[4], X1[4], WG[NG], WU[NG], WD[NA];
  auto ldx = [&](bf16x8 (&X)[4], lptr st, int ks) {
#pragma unroll
    for (int t = 0; t < 4; ++t) X[t] = ldsr128(st + (ks ? vx1 : vx0) + (32 * (t >> 1) + 4 * (t & 1)) * 128);
  };
  auto ldg = [&](int j, lptr st, int ks) { lptr b = st + GO + ks * KSO + vw[j]; WG[j] = ldstr(b, b + 512); };
  auto ldu = [&](int j, lptr st, int ks) { lptr b = st + UO + ks * KSO + vw[j]; WU[j] = ldstr(b, b + 512); };
  auto ldd = [&](int j, lptr st, int ks) { WD[j] = ldsr128(st + (ks ? vd1 : vd0) + j * 16 * 128); };
  auto grp4 = [&](f32x4 (&C)[4], const bf16x8& W, bf16x8 (&X)[4], bool wfirst, auto mid) {
    if (wfirst) { mfma_ip(C[0], W, X[0]); mfma_ip(C[1], W, X[1]); } else { mfma_ip(C[0], X[0], W); mfma_ip(C[1], X[1], W); }
    SBAR(); mid(); SBAR();
    if (wfirst) { mfma_ip(C[2], W, X[2]); mfma_ip(C[3], W, X[3]); } else { mfma_ip(C[2], X[2], W); mfma_ip(C[3], X[3], W); }
    SBAR();
  };
  auto fill = [&]() { FILL(); };
  auto ldx1 = [&](bf16x8 (&X)[4], lptr st, int ks, int t) { X[t] = ldsr128(st + (ks ? vx1 : vx0) + (32 * (t >> 1) + 4 * (t & 1)) * 128); };
  auto blockA = [&](lptr st) {
#pragma unroll
    for (int j = 0; j < NG; ++j) {
      grp4(G[j], WG[j], X0, true, [&]() { if (2 * j < 4) ldx1(X1, st, 1, 2 * j); else FILL(); }); ldg(j, st, 1); SBAR();
      grp4(U[j], WU[j], X0, true, [&]() { if (2 * j + 1 < 4) ldx1(X1, st, 1, 2 * j + 1); else FILL(); }); ldu(j, st, 1); SBAR();
    }
#pragma unroll
    for (int j = 0; j < NA; ++j) { grp4(AB[j], WD[j], X0, false, fill); ldd(j, st, 1); SBAR(); }
  };
  auto blockB_head = [&]() { grp4(G[0], WG[0], X1, true, fill); FILL(); grp4(U[0], WU[0], X1, true, fill); };
  auto blockB_rest = [&](lptr sn, bool reload) {
    int k = 0;
    auto mid = [&]() {
      if (!reload) FILL();
      else if (k == 0) ldg(0, sn, 0);
      else if (k == 1) ldu(0, sn, 0);
      else if (k < 6) ldx1(X0, sn, 0, k - 2);
      else FILL();
      ++k;
    };
#pragma unroll
    for (int j = 1; j < NG; ++j) {
      grp4(G[j], WG[j], X1, true, mid); if (reload) { ldg(j, sn, 0); SBAR(); } else FILL();
      grp4(U[j], WU[j], X1, true, mid); if (reload) { ldu(j, sn, 0); SBAR(); } else FILL();
    }
#pragma unroll
    for (int j = 0; j < NA; ++j) { grp4(AB[j], WD[j], X1, false, mid); if (reload) { ldd(j, sn, 0); SBAR(); } else FILL(); }
  };

#pragma unroll
  for (int j = 0; j < NG; ++j)
#pragma unroll
    for (int t = 0; t < 4; ++t) { asm volatile("" : "+v"(G[j][t])); asm volatile("" : "+v"(U[j][t])); }
#pragma unroll
  for (int j = 0; j < NA; ++j)
#pragma unroll
    for (int t = 0; t < 4; ++t) asm volatile("" : "+v"(AB[j][t]));
  PAD_MFMA_IN();
  STAMP(1);
  BAR();
  STAMP(2);
  ldg(0, lds, 0); ldu(0, lds, 0); ldx(X0, lds, 0);
#pragma unroll
  for (int j = 1; j < NG; ++j) { ldg(j, lds, 0); ldu(j, lds, 0); }
#pragma unroll
  for (int j = 0; j < NA; ++j) ldd(j, lds, 0);
  SBAR();
  int slot = 0;
#pragma unroll 1
  for (int s = 0; s < K1_NST; ++s) {
    blockA(lds + slot * K1_STAGE);
    if (s >= PF0 && s < PF0 + 4) { pf4(cp, lds_addr(lds) + L_DUMMY); cp += 2 * 4096; }
    blockB_head();
    if (s == 0 && wm == 0 && wn == 0) {
      if (PF0 == 0) { WAITV(1); } else { WAITV(0); }
      asm volatile("" : "+v"(nonce));
      if (lane == 0) *(volatile LAS unsigned*)(lds + L_WORDS + 16) = (MODE == 0) ? nonce * 0x9E3779B1u + 0x7F4A7C15u : 0x51u;
    }
    WAITL0();
    STAMP(19 + s);
    BAR();
    STAMP(7 + s);
    slot = (slot == 2) ? 0 : slot + 1;
    blockB_rest(lds + slot * K1_STAGE, true);
  }
  PAD_MFMA_OUT();
  STAMP(3);
  const lptr cs = lds + L_CS;
  const lptr stats = lds + L_STATS;
  {
    const rsrc_t rab = mk_rsrc(ws + WS_AB, 2 * 28 * 64 * 1024);
#pragma unroll
    for (int pi = 0; pi < 2; ++pi) {
      const lptr sp = stats + (64 * wm + 32 * pi + 8 * g) * 8;
      const f32x4 s0 = *(LAS f32x4*)(sp), s1 = *(LAS f32x4*)(sp + 16), s2 = *(LAS f32x4*)(sp + 32), s3 = *(LAS f32x4*)(sp + 48);
#pragma unroll
      for (int j = 0; j < NA; ++j) {
        const float c = *(LAS float*)(cs + (2 * NBK + NA * wn + j) * 64 + i16 * 4);
        const f32x4 a_ = AB[j][2 * pi], b = AB[j][2 * pi + 1];
        const int ks = mt * 4 + wm * 2 + pi;
        u32x4 o;
        o[0] = pk_bf16((a_[0] - s0[0] * c) * s0[1], (a_[1] - s0[2] * c) * s0[3]);
        o[1] = pk_bf16((a_[2] - s1[0] * c) * s1[1], (a_[3] - s1[2] * c) * s1[3]);
        o[2] = pk_bf16((b[0] - s2[0] * c) * s2[1], (b[1] - s2[2] * c) * s2[3]);
        o[3] = pk_bf16((b[2] - s3[0] * c) * s3[1], (b[3] - s3[2] * c) * s3[3]);
        __builtin_amdgcn_raw_buffer_store_b128(o, rab, lane * 16 + ((a0 + NA * wn + j) * 64 + ks) * 1024, 0, STAUX);
      }
    }
  }
  {
    lptr hd = lds + L_HD + (2 * wn + wm) * (NG * 4096) + lane * 16;
#pragma unroll
    for (int j = 0; j < NG; ++j)
#pragma unroll
      for (int tl = 0; tl < 2; ++tl) {
        *(LAS f32x4*)(hd + ((j * 2 + 0) * 2 + tl) * 1024) = G[j][2 + tl];
        *(LAS f32x4*)(hd + ((j * 2 + 1) * 2 + tl) * 1024) = U[j][2 + tl];
      }
  }
  WAITV(0);
  if (lane == 0) {
    const unsigned n = __hip_atomic_fetch_add((LAS unsigned*)(lds + L_WORDS), 1u, __ATOMIC_RELAXED, __HIP_MEMORY_SCOPE_WORKGROUP);
    if (n == 3u) st_flag(ws + WS_FAB + (mt * 16 + grp) * 64, get_tag(lds));
  }
  STAMP(5);
  WAITL0(); BAR();
  STAMP(14);
  const rsrc_t rh = mk_rsrc(ws + WS_H, 128 * 56 * 1024);
  const int tt0 = mt * 8 + wm * 4;
  f32x2 st[2];
#pragma unroll
  for (int t = 0; t < 2; ++t) st[t] = *(LAS f32x2*)(stats + (64 * wm + 8 * (i16 >> 2) + 4 * t + (i16 & 3)) * 8);
#pragma unroll
  for (int pp = 0; pp < NG / 2; ++pp) {
    const int ks = ((f0 >> 4) + NG * wn + 2 * pp) >> 1;
    f32x4 cg[2], cu[2], gq[2][2], uq[2][2];
#pragma unroll
    for (int j = 0; j < 2; ++j) {
      cg[j] = *(LAS f32x4*)(cs + (NG * wn + 2 * pp + j) * 64 + g * 16);
      cu[j] = *(LAS f32x4*)(cs + (NBK + NG * wn + 2 * pp + j) * 64 + g * 16);
      gq[j][0] = G[2 * pp + j][0]; gq[j][1] = G[2 * pp + j][1]; uq[j][0] = U[2 * pp + j][0]; uq[j][1] = U[2 * pp + j][1];
    }
    h_batch(gq, uq, cg, cu, st, rh, lane * 16 + (tt0 * 56 + ks) * 1024);
  }
  STAMP(4);
}

template <int NG>
__device__ __forceinline__ void p1_helper_epilogue(lptr lds, char* ws, int mt, int f0, int h, int lane) {
  constexpr int NBK = 2 * NG;
  const int wm = h & 1, wn = h >> 1, i16 = lane & 15, g = lane >> 4;
  const lptr cs = lds + L_CS, stats = lds + L_STATS;
  const rsrc_t rh = mk_rsrc(ws + WS_H, 128 * 56 * 1024);
  const int tt0 = mt * 8 + wm * 4 + 2;
  lptr hd = lds + L_HD + h * (NG * 4096) + lane * 16;
  f32x2 st[2];
#pragma unroll
  for (int t = 0; t < 2; ++t) st[t] = *(LAS f32x2*)(stats + (64 * wm + 32 + 8 * (i16 >> 2) + 4 * t + (i16 & 3)) * 8);
#pragma unroll
  for (int pp = 0; pp < NG / 2; ++pp) {
    const int ks = ((f0 >> 4) + NG * wn + 2 * pp) >> 1;
    f32x4 cg[2], cu[2], gq[2][2], uq[2][2];
#pragma unroll
    for (int j = 0; j < 2; ++j) {
      cg[j] = *(LAS f32x4*)(cs + (NG * wn + 2 * pp + j) * 64 + g * 16);
      cu[j] = *(LAS f32x4*)(cs + (NBK + NG * wn + 2 * pp + j) * 64 + g * 16);
#pragma unroll
      for (int tl = 0; tl < 2; ++tl) {
        gq[j][tl] = *(LAS f32x4*)(hd + (((2 * pp + j) * 2 + 0) * 2 + tl) * 1024);
        uq[j][tl] = *(LAS f32x4*)(hd + (((2 * pp + j) * 2 + 1) * 2 + tl) * 1024);
      }
    }
    h_batch(gq, uq, cg, cu, st, rh, lane * 16 + (tt0 * 56 + ks) * 1024);
  }
}

template <int MODE, bool TB>
__device__ __forceinline__ void p1_helper(lptr lds, Args a, int xcd, int idx, int mt, int grp, int f0, int a0, int h, int lane) {
  constexpr int NBK = TB ? 4 : 8, WSZ = NBK * 8 * 256;
  constexpr int NWP = TB ? 4 : 8, NDP = TB ? 4 : 1, PER = 4 + NWP + NDP;
  const int mh = xcd & 1, nq = xcd >> 1;
  const int up = h >> 1;
  const u32x4 rx = mk_srd(a.x, S * D * 2);
  const u32x4 rw = mk_srd(up ? a.Wu : a.Wg, D * F * 2);
  const u32x4 rd = mk_srd(a0 >= 28 ? a.Di : a.Dr, D * D * 2);
  const int e0 = (a0 >= 28 ? a0 - 28 : a0) * 16;
  const int r8 = lane >> 3, c8 = lane & 7;
  int vx[2];
  vx[0] = r8 * 896 + (c8 ^ (r8 & 3)) * 16;
  vx[1] = r8 * 896 + (c8 ^ ((r8 & 3) | 4)) * 16;
  const int vd = r8 * 896 + (c8 ^ r8) * 16;
  int vw[2];
  {
    const int f0_ = (r8 >> 1) & 1;
    vw[0] = r8 * (F * 2) + f0 * 2 + ((((c8 >> 1) ^ f0_) * 2) + (c8 & 1)) * 16;
    vw[1] = r8 * (F * 2) + f0 * 2 + ((((c8 >> 1) ^ (f0_ | 2)) * 2) + (c8 & 1)) * 16;
  }
  const unsigned l0 = lds_addr(lds);
  const unsigned dummy = l0 + L_DUMMY + (h & 1) * 256;
  const int sxb = (mt * 128 + 32 * h) * 896, swb = 32 * (h & 1) * (F * 2), sdb = (e0 + 8 * NDP * h) * 896;
  const unsigned lxb = l0 + 4 * h * 1024, lwb = l0 + K1_WO + up * WSZ + NWP * (h & 1) * 1024, ldb = l0 + K1_WO + 2 * WSZ + NDP * h * 1024;
  auto piece = [&](int s, int slot, int i) {
    const unsigned so = slot * K1_STAGE;
    if (i < 4) dma16(rx, lxb + so + i * 1024, vx[i & 1], sxb + s * 128 + i * 8 * 896);
    else if (i < 4 + NWP) {
      const int j = i - 4, kb = TB ? j : j >> 1, cq = TB ? 0 : j & 1;
      dma16(rw, lwb + so + j * 1024, vw[kb & 1], swb + s * 64 * (F * 2) + kb * 8 * (F * 2) + cq * 128);
    } else dma16(rd, ldb + so + (i - 4 - NWP) * 1024, vd, sdb + s * 128 + (i - 4 - NWP) * 8 * 896);
  };
  auto issue_a = [&](int s, int slot) {
#pragma unroll
    for (int i = 0; i < 8; ++i) piece(s, slot, i);
  };
  auto issue_b = [&](int s, int slot) {
#pragma unroll
    for (int i = 8; i < PER; ++i) piece(s, slot, i);
  };
  const int ln2 = lane >> 1, hb = (lane & 1) * 64;
  const char* pfx = (const char*)a.x + (size_t)(1024 * mh + 32 * idx + ln2) * 896 + hb;
  STAMP(1);
  issue_a(0, 0); issue_b(0, 0);
  STAMP(5);
  launder(a, xcd, idx);
  char* ws = a.ws;
  const bool wjob = idx < 28;
  const int wj_mat = idx >= 14, wj_cb = (idx - 14 * wj_mat) >> 1, wj_kh = idx & 1;
  u32x4 wx[7];
  {
    const u32x4 rj = mk_srd(wj_mat ? a.Wu : a.Wg, D * F * 2);
    const int vj = r8 * (F * 2) + c8 * 16 + (448 * nq + 64 * (wjob ? wj_cb : 0)) * 2;
    const int sj = (224 * (wjob ? wj_kh : 0) + 56 * h) * (F * 2);
    asm volatile("buffer_load_dwordx4 %0, %7, %8, %9 offen\n\tbuffer_load_dwordx4 %1, %7, %8, %10 offen\n\tbuffer_load_dwordx4 %2, %7, %8, %11 offen\n\t"
                 "buffer_load_dwordx4 %3, %7, %8, %12 offen\n\tbuffer_load_dwordx4 %4, %7, %8, %13 offen\n\tbuffer_load_dwordx4 %5, %7, %8, %14 offen\n\t"
                 "buffer_load_dwordx4 %6, %7, %8, %15 offen"
                 : "=&v"(wx[0]), "=&v"(wx[1]), "=&v"(wx[2]), "=&v"(wx[3]), "=&v"(wx[4]), "=&v"(wx[5]), "=&v"(wx[6])
                 : "v"(vj), "s"(rj), "s"(sj), "s"(sj + 8 * F * 2), "s"(sj + 16 * F * 2), "s"(sj + 24 * F * 2), "s"(sj + 32 * F * 2), "s"(sj + 40 * F * 2), "s"(sj + 48 * F * 2)
                 : "memory");
  }
  RowJobs rj_;
  rj_.load(a, nq, idx, mt, grp, h, lane);
  pf4(pfx + (1 + h) * 128, dummy);
  if (h == 3 && lane < 4) ((LAS unsigned*)(lds + L_WORDS))[lane] = 0u;
  WAITL0(); WAITV(12);
  STAMP(7); BAR();
  STAMP(2);
#pragma unroll
  for (int i = 0; i < PER; ++i) piece(1, 1, i);
  issue_a(2, 2);
  if (h < 2) pf4(pfx + (5 + h) * 128, dummy);
  WAITL0();
  if (h < 2) { WAITV(9); } else { WAITV(8); }
  asm volatile("" : "+v"(wx[0]), "+v"(wx[1]), "+v"(wx[2]), "+v"(wx[3]), "+v"(wx[4]), "+v"(wx[5]), "+v"(wx[6]));
  rj_.landed();
  BAR();
  const unsigned tag1 = get_tag(lds);
  float cw[8];
#pragma unroll
  for (int k = 0; k < 8; ++k) cw[k] = 0.f;
  constexpr bool TBc = TB;
  unsigned long long g0 = 0, g1 = 0, g2 = 0, g3 = 0;
  const bool wdjob = blockIdx.x < 196;
  const int wd_kp = blockIdx.x % 28, wd_cg = blockIdx.x / 28;
#pragma unroll
  for (int s = 1; s < K1_NST; ++s) {
    const int s1_ = (s + 1) % 3, s2_ = (s + 2) % 3;
    if (s == 1) {
#pragma unroll
      for (int i = 0; i < PER; ++i) {
        if (i < PER - 8) piece(2, s1_, 8 + i); else piece(3, s2_, i - (PER - 8));
        SBAR();
        if (i < 7) {
#pragma unroll
          for (int k = 0; k < 4; ++k) { cw[2 * k] += bf_lo(wx[i][k]); cw[2 * k + 1] += bf_hi(wx[i][k]); }
        }
        SBAR();
      }
    } else {
      if (s + 1 < K1_NST) issue_b(s + 1, s1_);

      if (s + 2 < K1_NST) issue_a(s + 2, s2_);
    }
    if (s == 1) {
      pf4((const char*)a.Wd + (size_t)(64 * wd_kp + 16 * h + ((lane >> 1) & 15)) * 896 + (wdjob ? wd_cg : 0) * 128 + (lane & 1) * 64, dummy);
#pragma unroll
      for (int k = 0; k < 8; ++k) {
        cw[k] += __builtin_bit_cast(float, __builtin_amdgcn_update_dpp(0, __builtin_bit_cast(int, cw[k]), 0x128, 0xf, 0xf, true));
        const auto p16 = __builtin_amdgcn_permlane16_swap(__float_as_uint(cw[k]), __float_as_uint(cw[k]), false, false);
        cw[k] = __uint_as_float(p16[0]) + __uint_as_float(p16[1]);
        const auto p32 = __builtin_amdgcn_permlane32_swap(__float_as_uint(cw[k]), __float_as_uint(cw[k]), false, false);
        cw[k] = __uint_as_float(p32[0]) + __uint_as_float(p32[1]);
      }
      if (lane < 8) {
        *(LAS f32x4*)(lds + L_CS + h * 256 + lane * 32) = f32x4{cw[0], cw[1], cw[2], cw[3]};
        *(LAS f32x4*)(lds + L_CS + h * 256 + lane * 32 + 16) = f32x4{cw[4], cw[5], cw[6], cw[7]};
      }
    }
    if (s == 2) rj_.sums(lane);
    if (s == 3) rj_.finish(ws, tag1, nq, idx, mt, grp, h, lane);
    if (s == 2 && h == 0 && wjob) {
      const float t = (*(LAS float*)(lds + L_CS + lane * 4) + *(LAS float*)(lds + L_CS + 256 + lane * 4)) +
                      (*(LAS float*)(lds + L_CS + 512 + lane * 4) + *(LAS float*)(lds + L_CS + 768 + lane * 4));
      __hip_atomic_store((GAS unsigned long long*)(ws + WS_CW + (size_t)((wj_mat * 2 + wj_kh) * F + 448 * nq + 64 * wj_cb + lane) * 8),
                         ((unsigned long long)tag1 << 32) | __float_as_uint(t), __ATOMIC_RELAXED, __HIP_MEMORY_SCOPE_AGENT);
    }
    if (s == 4 && h == 0 && lane == 0) st_flag(ws + WS_ARRIVE + blockIdx.x * 64, tag1);
    if (s == 5) {
      if (wdjob) {
        const u32x4 rwd = mk_srd(a.Wd, F * D * 2);
        const int rs = (lane & 15) >> 1, half = lane & 1, b = lane >> 4;
        const int vq = rs * 896 + (64 * wd_cg + 16 * b + 8 * half) * 2;
#pragma unroll
        for (int i = 0; i < 2; ++i) dma16(rwd, l0 + s2_ * K1_STAGE + (2 * h + i) * 1024, vq, (64 * wd_kp + 8 * (2 * h + i)) * 896);
      }
    }
    const int gc = 64 * h + lane;
    constexpr int NWC = TBc ? 128 : 256, CPM = NWC / 2;
    const bool isw = gc < NWC;
    const char* p_st = ws + WS_ST + (size_t)(mt * 128 + 32 * h + (lane & 31)) * 8;
    const char* p_c0 = ws + (isw ? WS_CW + (size_t)((gc / CPM) * 2 * F + f0 + gc % CPM) * 8 : WS_CD + (size_t)(a0 * 16 + (gc - NWC)) * 8);
    const char* p_c1 = isw ? p_c0 + (size_t)F * 8 : p_c0;
    const char* p_d = ws + WS_CD + (size_t)(a0 * 16 + (lane & 31)) * 8;
    if (s == 5)
      asm volatile("global_load_dwordx2 %0, %4, off sc1\n\tglobal_load_dwordx2 %1, %5, off sc1\n\tglobal_load_dwordx2 %2, %6, off sc1\n\tglobal_load_dwordx2 %3, %7, off sc1"
                   : "=&v"(g0), "=&v"(g1), "=&v"(g2), "=&v"(g3) : "v"(p_st), "v"(p_c0), "v"(p_c1), "v"(p_d) : "memory");
    if (s == 6) {
      WAITV(DFTPF == 2 ? 2 : 0);
      asm volatile("" : "+v"(g0), "+v"(g1), "+v"(g2), "+v"(g3));
      auto ld_gran = [&](const char* p) { return __hip_atomic_load((const GAS unsigned long long*)p, __ATOMIC_RELAXED, __HIP_MEMORY_SCOPE_AGENT); };
      for (unsigned sp = 0; sp < SPIN_MAX; ++sp) {
        if (__all((unsigned)(g0 >> 32) == tag1 && (unsigned)(g1 >> 32) == tag1 && (unsigned)(g2 >> 32) == tag1 && (unsigned)(g3 >> 32) == tag1)) break;
        __builtin_amdgcn_s_sleep(2);
        g0 = ld_gran(p_st); g1 = ld_gran(p_c0); g2 = ld_gran(p_c1); g3 = ld_gran(p_d);
      }
      if (lane < 32) *(LAS f32x2*)(lds + L_STATS + (32 * h + lane) * 8) = f32x2{bf_lo((unsigned)g0), 1.0f / bf_hi((unsigned)g0)};
      *(LAS float*)(lds + L_CS + gc * 4) = isw ? __uint_as_float((unsigned)g1) + __uint_as_float((unsigned)g2) : __uint_as_float((unsigned)g1);
      if (!TBc && h == 0 && lane < 32) *(LAS float*)(lds + L_CS + (256 + lane) * 4) = __uint_as_float((unsigned)g3);
    }
    WAITL0();
    if (s == 1) { WAITV(9); } else if (s + 2 < K1_NST) { WAITV(8); } else if (s == 5) { if (wdjob) { WAITV(6 + (DFTPF == 2 ? 2 : 0)); } else { WAITV(4 + (DFTPF == 2 ? 2 : 0)); } }
    STAMP(7 + s);
    BAR();
  }
  __builtin_amdgcn_s_setprio(0);
  if (h == 3 && wdjob) {
    const int i16 = lane & 15, g = lane >> 4, q = i16 >> 2, p = i16 & 3;
    const rsrc_t rt = mk_rsrc(ws + WS_WDT, 28 * 56 * 1024);
    lptr b0 = lds + 1 * K1_STAGE + (g >> 1) * 1024 + ((g & 1) * 4 + q) * 32 + p * 8;
#pragma unroll
    for (int kk = 0; kk < 2; ++kk)
#pragma unroll
      for (int u = 0; u < 4; ++u) {
        const bf16x8 f = ldstr(b0 + kk * 4096 + u * 256, b0 + kk * 4096 + u * 256 + 2048);
        __builtin_amdgcn_raw_buffer_store_b128(__builtin_bit_cast(u32x4, f), rt, lane * 16 + ((4 * wd_cg + u) * 56 + 2 * wd_kp + kk) * 1024, 0, STAUX);
      }
    WAITL0();
  }
  BAR();
  p1_helper_epilogue<TB ? 2 : 4>(lds, ws, mt, f0, h, lane);
}

template <int MODE, int NB, int JO>
__device__ __forceinline__ void p2_loader(lptr lds, const Args& a, int xcd, int idx, int r, int m, int n, int lw, int lane) {
  char* ws = a.ws;
  constexpr int PER = 4 + NB;
  const u32x4 rc = mk_srd((r >> 1) ? a.Ci : a.Cr, S * S * 2);
  const u32x4 rb = mk_srd(ws + WS_AB, 2 * 28 * 64 * 1024);
  const u32x4 rh = mk_srd(ws + WS_H, 128 * 56 * 1024);
  const u32x4 rw = mk_srd(ws + WS_WDT, 28 * 56 * 1024);
  const int r8 = lane >> 3, c8 = lane & 7;
  int va[2];
  va[0] = r8 * 4096 + (c8 ^ (r8 & 3)) * 16;
  va[1] = r8 * 4096 + (c8 ^ ((r8 & 3) | 4)) * 16;
  const int vl = lane * 16;
  const int k0 = 1024 * (r & 1), p = r >> 1;
  const int j0 = (lw < 2) ? 4 * lw : 8 + 3 * (lw - 2);
  const unsigned l0 = lds_addr(lds);
  const unsigned dummy = l0 + L_DUMMY + (lw & 1) * 256;
  const int sab = (128 * m + 32 * lw) * 4096 + k0 * 2;
  const int shb = ((8 * m + 2 * lw) * 56 + 14 * r) * 1024;
  const unsigned lab = l0 + 4 * lw * 1024, lbb = l0 + 16384 + j0 * 1024;
  const int sbb = ((p * 28 + 7 * n + (j0 >> 1)) * 64 + (k0 >> 5)) * 1024, swb = ((7 * n + (j0 >> 1)) * 56 + 14 * r) * 1024;
  auto issueAy = [&](int s, int slot) {
    const unsigned so = slot * P_STAGE; const int b = sab + 128 * (s & 15);
#pragma unroll
    for (int i = 0; i < 4; ++i) dma16(rc, lab + so + i * 1024, va[i & 1], b + i * 8 * 4096);
  };
  auto issueAd = [&](int sd, int slot) {
    const unsigned so = slot * P_STAGE; const int b = shb + 2048 * sd;
#pragma unroll
    for (int i = 0; i < 4; ++i) dma16c(rh, lab + so + i * 1024, vl, b + (i >> 1) * 56 * 1024 + (i & 1) * 1024);
  };
  auto issueBy = [&](int s, int slot) {
    const unsigned so = slot * P_STAGE; const int b = sbb + 2048 * (s & 15);
#pragma unroll
    for (int j = 0; j < NB; ++j) dma16c(rb, lbb + so + j * 1024, vl, b + ((j + JO) >> 1) * 65536 + ((j + JO) & 1) * 1024);
  };
  auto issueBd = [&](int sd, int slot) {
    const unsigned so = slot * P_STAGE; const int b = swb + 2048 * sd;
#pragma unroll
    for (int j = 0; j < NB; ++j) dma16c(rw, lbb + so + j * 1024, vl, b + ((j + JO) >> 1) * 57344 + ((j + JO) & 1) * 1024);
  };
  __builtin_amdgcn_s_setprio(0);
  const unsigned tag = get_tag(lds);
  if (MODE != 2 && lw == 3 && blockIdx.x < 196) {
    WAITV(0);
    if (lane == 0) st_flag(ws + WS_FWD + blockIdx.x * 64, tag);
  }
  issueAy(0, 0); issueAy(1, 1);
  if (lw == 3) {
    const int mtp = 8 * (r & 1) + (lane & 7), j = (lane >> 3) & 3;
    const bool act = lane < 32 && ((n & 1) == 0 || j == 3);
    poll_all(ws + WS_FAB + (mtp * 16 + (2 * p + (n >> 1)) * 4 + j) * 64, act, tag);
    if (lane == 0) *(volatile LAS unsigned*)(lds + L_WORDS + 8) = 1u;
  } else lds_spin(lds + L_WORDS + 8);
  STAMP(16);
  issueBy(0, 0);
  {
    const int l = (lane >> 1) < 28 ? (lane >> 1) : 27;
    const int sl = l >= 14, li = 14 * idx + l - 14 * sl;
    const char* bp = ws + WS_AB + ((size_t)(p * 28 + (li >> 4)) * 64 + 32 * (r & 1) + 2 * (4 * lw + sl) + ((li >> 3) & 1)) * 1024 + (li & 7) * 128 + (lane & 1) * 64;
    pf4c(bp, dummy); pf4c(bp + 4 * 1024, dummy);
  }
  issueBy(1, 1);
  __builtin_amdgcn_s_setprio(3);
  WAITV(NB + 2); BAR();
  if (MODE == 0 && lane == 0) {
    const unsigned cnt = __hip_atomic_fetch_add((LAS unsigned*)(lds + L_WORDS + 4), 1u, __ATOMIC_RELAXED, __HIP_MEMORY_SCOPE_WORKGROUP);
    if (cnt == 7u) st_flag(ws + WS_FH + (((xcd & 1) * 8 + (idx & 7)) * 16 + (xcd >> 1) * 4 + (idx >> 3)) * 64, tag);
  }
  issueAy(2, 2); issueBy(2, 2);
  WAITV(PER); BAR();
  issueAy(3, 3); issueBy(3, 3); issueAy(4, 4); issueBy(4, 4);
  WAITV(2 * PER); BAR();
  int slot = 0;
  auto ystep = [&](int s) { issueAy(s + 3, slot); issueBy(s + 3, slot); slot = (slot == 4) ? 0 : slot + 1; WAITV(2 * PER); BAR(); };
  auto dstep = [&](int sd) { issueAd(sd, slot); issueBd(sd, slot); slot = (slot == 4) ? 0 : slot + 1; WAITV(2 * PER); BAR(); };
#pragma unroll 1
  for (int s = 2; s < P_NY - 9; ++s) ystep(s);
  const int cg0 = (112 * n) >> 6, cg1 = (112 * n + 111) >> 6, jj = lane - 4;
  const bool fact = lane < 4 || (jj < 21 && cg0 + jj / 7 <= cg1);
  const char* fp = lane < 4 ? ws + WS_FH + (m * 16 + 4 * r + lane) * 64 : ws + WS_FWD + ((fact ? cg0 + jj / 7 : cg0) * 28 + 7 * r + (fact ? jj % 7 : 0)) * 64;
  unsigned fval = 0;
  if (lw == 3) asm volatile("global_load_dword %0, %1, off sc1" : "+v"(fval) : "v"(fp) : "memory");
#pragma unroll 1
  for (int s = P_NY - 9; s < P_NY - 6; ++s) ystep(s);
  if (lw == 3) {
    asm volatile("" : "+v"(fval));
    if (!__all(!fact || fval == tag)) poll_all(fp, fact, tag);
    if (lane == 0) *(volatile LAS unsigned*)(lds + L_WORDS + 12) = 1u;
  }
#pragma unroll 1
  for (int s = P_NY - 6; s < P_NY - 3; ++s) ystep(s);
  if (lw != 3) lds_spin(lds + L_WORDS + 12);
#pragma unroll 1
  for (int sd = 0; sd < P_ND; ++sd) dstep(sd);
  WAITV(PER); BAR();
  WAITV(0); BAR();
}

template <int MODE>
__global__ __launch_bounds__(512, 2) void fused_kernel(Args a) {
  extern __shared__ __attribute__((aligned(1024))) char smem[];
  lptr lds = (lptr)smem;
  char* ws = a.ws;
  const int tid = threadIdx.x, lane = tid & 63;
  const int wid = __builtin_amdgcn_readfirstlane(tid >> 6);
  int xcd = blockIdx.x & 7, idx = blockIdx.x >> 3;
  STAMP(0); STAMPR(30);
  if (wid == 3) {
    unsigned long long pc;
    asm volatile("s_getpc_b64 %0" : "=s"(pc));
    const unsigned ln = 13 * idx + ((lane >> 1) < 13 ? (lane >> 1) : 12);
    pf4((const char*)(pc & ~127ull) + (ln * 128 < CODE_PF ? ln * 128 : 0) + (lane & 1) * 64, lds_addr(lds) + L_DUMMY);
  }
  if (wid >= 4) __builtin_amdgcn_s_setprio(3);

  if (MODE != 2) {
    const int nq = xcd >> 1;
    const int mt = (xcd & 1) * 8 + (idx & 7), j = idx >> 3;
    const bool tb = j == 3;
    const int grp = nq * 4 + j;
    const int f0 = 448 * nq + 128 * j, a0 = 14 * nq + 2 * j;
    if (wid < 4) {
      if (tb) p1_consumer<MODE, 2, 4>(lds, a, xcd, idx, mt, grp, f0, a0, wid & 1, wid >> 1, lane);
      else p1_consumer<MODE, 4, 1>(lds, a, xcd, idx, mt, grp, f0, a0, wid & 1, wid >> 1, lane);
    } else {
      const int h = wid - 4;
      if (tb) p1_helper<MODE, true>(lds, a, xcd, idx, mt, grp, f0, a0, h, lane);
      else p1_helper<MODE, false>(lds, a, xcd, idx, mt, grp, f0, a0, h, lane);
    }
  }
  if (MODE == 1) return;
  if (MODE == 2) { if (tid < 4) ((LAS unsigned*)(lds + L_WORDS))[tid] = 0u; if (tid == 4) ((LAS unsigned*)(lds + L_WORDS))[4] = 0x51u; __syncthreads(); }

  launder(a, xcd, idx);
  ws = a.ws;
  const int p1_mt = (xcd & 1) * 8 + (idx & 7), p1_grp = (xcd >> 1) * 4 + (idx >> 3);
  const int r = xcd >> 1, m = 8 * (xcd & 1) + (idx & 7), n = idx >> 3;
  const int tile = m * 4 + n;
  const int i16 = lane & 15, g = lane >> 4;
  f32x4 accY[7][2], acc[7][2];
  if (wid == 7) {
    p2_loader<MODE, 3, 1>(lds, a, xcd, idx, r, m, n, 3, lane);
  } else if (wid == 6) {
    p2_loader<MODE, 3, 0>(lds, a, xcd, idx, r, m, n, 2, lane);
  } else if (wid >= 4) {
    p2_loader<MODE, 4, 0>(lds, a, xcd, idx, r, m, n, wid - 4, lane);
  } else {
    const int w = wid;
    const int rowt = 32 * w + 8 * (i16 >> 2) + (i16 & 3);
    const int vt0 = rowt * 128 + ((g) ^ (i16 & 7)) * 16, vt1 = rowt * 128 + ((g + 4) ^ (i16 & 7)) * 16;
    const int ve = 16384 + lane * 16;
    const int vh = (4 * w) * 1024 + lane * 16;
    bf16x8 T0[4], T1[4], E[14];
    auto ldTy = [&](bf16x8 (&T)[4], lptr st) {
      T[0] = ldsr128(st + vt0); T[1] = ldsr128(st + vt0 + 512); T[2] = ldsr128(st + vt1); T[3] = ldsr128(st + vt1 + 512);
    };
    auto ldTd = [&](bf16x8 (&T)[4], lptr st) {
      T[0] = ldsr128(st + vh); T[1] = ldsr128(st + vh + 2048); T[2] = ldsr128(st + vh + 1024); T[3] = ldsr128(st + vh + 3072);
    };
    auto ldE = [&](int j, lptr st) { E[j] = ldsr128(st + ve + ((j % 7) * 2 + j / 7) * 1024); };
    auto mm = [&](int j, bf16x8 (&T)[4]) {
      mfma_ip(acc[j % 7][0], E[j], T[(j / 7) * 2]);
      mfma_ip(acc[j % 7][1], E[j], T[(j / 7) * 2 + 1]);
    };
#define GHEAD 3
    constexpr int GH = GHEAD;
    auto ldT1 = [&](bf16x8 (&T)[4], lptr st, int i, int NT) {
      if (NT == 0) T[i] = ldsr128(st + (i >= 2 ? vt1 : vt0) + (i & 1) * 512);
      else T[i] = ldsr128(st + vh + (i & 1) * 2048 + (i >> 1) * 1024);
    };
    auto rest = [&](bf16x8 (&Tc)[4], bf16x8 (&Tn)[4], lptr sn, int NT) {
#pragma unroll
      for (int j = GH; j < 14; ++j) {
        const int k = j - GH;
        mfma_ip(acc[j % 7][0], E[j], Tc[(j / 7) * 2]); SBAR();
        if (NT != 2 && k < 4 + GH) { if (k < 4) ldT1(Tn, sn, k, NT); else ldE(k - 4, sn); SBAR(); }
        mfma_ip(acc[j % 7][1], E[j], Tc[(j / 7) * 2 + 1]); SBAR();
        if (NT != 2) ldE(j, sn); else FILL();
        SBAR();
      }
    };
    auto head = [&](bf16x8 (&Tc)[4]) {
#pragma unroll
      for (int j = 0; j < GH; ++j) { mm(j, Tc); FILL(); }
      SBAR();
    };
#pragma unroll
    for (int u = 0; u < 7; ++u) { acc[u][0] = f32x4{0, 0, 0, 0}; acc[u][1] = f32x4{0, 0, 0, 0}; }
#pragma unroll
    for (int u = 0; u < 7; ++u) { asm volatile("" : "+v"(acc[u][0])); asm volatile("" : "+v"(acc[u][1])); }
    PAD_MFMA_IN();
    if (w != r) {
      const rsrc_t rp = mk_rsrc(ws + WS_PD + (size_t)((tile * 4 + r) * 4 + w) * 7168, 7168);
      const u32x4 sent = {0xffffffffu, 0xffffffffu, 0xffffffffu, 0xffffffffu};
#pragma unroll
      for (int u = 0; u < 7; ++u) {
        __builtin_amdgcn_raw_buffer_store_b128(sent, rp, u * 1024 + lane * 16, 0, 16);
      }
    }
    STAMP(16); STAMPR(28);
    BAR();
    STAMP(17); STAMPR(27);
    ldTy(T0, lds);
#pragma unroll
    for (int j = 0; j < 14; ++j) ldE(j, lds);
    SBAR();
    head(T0);
    WAITL0(); BAR();
    int slot = 1;
#pragma unroll 1
    for (int s = 0; s < P_NY - 2; s += 2) {
      lptr sn = lds + slot * P_STAGE; slot = (slot == 4) ? 0 : slot + 1;
      rest(T0, T1, sn, 0); head(T1);
      WAITL0(); BAR();
      sn = lds + slot * P_STAGE; slot = (slot == 4) ? 0 : slot + 1;
      rest(T1, T0, sn, 0); head(T0);
      WAITL0(); BAR();
      if (YREP > 1 && s == 14) { STAMP(23); }
      if (MODE == 0 && s == 0) {
        WAITV(0);
        if (lane == 0) {
          const unsigned cnt = __hip_atomic_fetch_add((LAS unsigned*)(lds + L_WORDS + 4), 1u, __ATOMIC_RELAXED, __HIP_MEMORY_SCOPE_WORKGROUP);
          if (cnt == 7u) st_flag(ws + WS_FH + (p1_mt * 16 + p1_grp) * 64, get_tag(lds));
        }
      }
    }
    {
      lptr sn = lds + slot * P_STAGE; slot = (slot == 4) ? 0 : slot + 1;
      rest(T0, T1, sn, 0); head(T1);
      WAITL0(); BAR();
      sn = lds + slot * P_STAGE; slot = (slot == 4) ? 0 : slot + 1;
      rest(T1, T0, sn, 1);
    }
    PAD_MFMA_OUT();
    STAMP(18);
    {
      const float sg = (r >> 1) ? -1.0f : 1.0f;
#pragma unroll
      for (int u = 0; u < 7; ++u) { accY[u][0] = acc[u][0] * sg; accY[u][1] = acc[u][1] * sg; acc[u][0] = f32x4{0, 0, 0, 0}; acc[u][1] = f32x4{0, 0, 0, 0}; }
      if (w != r) {
        const rsrc_t rp = mk_rsrc(ws + WS_PY + (size_t)((tile * 4 + r) * 4 + w) * 7168, 7168);
#pragma unroll
        for (int u = 0; u < 7; ++u) {
          const u32x4 o = {pk_bf16(accY[u][0][0], accY[u][0][1]), pk_bf16(accY[u][0][2], accY[u][0][3]),
                           pk_bf16(accY[u][1][0], accY[u][1][1]), pk_bf16(accY[u][1][2], accY[u][1][3])};
          __builtin_amdgcn_raw_buffer_store_b128(o, rp, u * 1024 + lane * 16, 0, 16);
        }
      }
    }
#pragma unroll
    for (int u = 0; u < 7; ++u) { asm volatile("" : "+v"(acc[u][0])); asm volatile("" : "+v"(acc[u][1])); }
    PAD_MFMA_IN();
    head(T0);
    WAITL0(); BAR();
#pragma unroll 1
    for (int s = 0; s < P_ND - 1; s += 2) {
      lptr sn = lds + slot * P_STAGE; slot = (slot == 4) ? 0 : slot + 1;
      rest(T0, T1, sn, 1); head(T1);
      WAITL0(); BAR();
      if (s == 0 && w != r) {
        WAITV(0);
        const unsigned tag = get_tag(lds);
        if (lane == 0) st_flag(ws + WS_FY + ((tile * 4 + r) * 4 + w) * 64, tag);
      }
      sn = lds + slot * P_STAGE; slot = (slot == 4) ? 0 : slot + 1;
      rest(T1, T0, sn, 1); head(T0);
      WAITL0(); if (s + 2 < P_ND - 1) BAR();
    }
    rest(T0, T1, lds, 2);
    PAD_MFMA_OUT();
    STAMP(19); STAMPR(29);
    if (w != r) {
      const rsrc_t rp = mk_rsrc(ws + WS_PD + (size_t)((tile * 4 + r) * 4 + w) * 7168, 7168);
#pragma unroll
      for (int u = 0; u < 7; ++u) {
        u32x4 o = {pk_bf16(acc[u][0][0], acc[u][0][1]), pk_bf16(acc[u][0][2], acc[u][0][3]),
                   pk_bf16(acc[u][1][0], acc[u][1][1]), pk_bf16(acc[u][1][2], acc[u][1][3])};
#pragma unroll
        for (int k = 0; k < 4; ++k) o[k] = o[k] < 0xfffffffeu ? o[k] : 0xfffffffeu;
        __builtin_amdgcn_raw_buffer_store_b128(o, rp, u * 1024 + lane * 16, 0, 16);
      }
    } else {
#pragma unroll
      for (int uu = 0; uu < 7; ++uu) {
        *(LAS f32x4*)(lds + (uu * 4 + 0) * 1024 + lane * 16) = accY[uu][0];
        *(LAS f32x4*)(lds + (uu * 4 + 1) * 1024 + lane * 16) = accY[uu][1];
        *(LAS f32x4*)(lds + (uu * 4 + 2) * 1024 + lane * 16) = acc[uu][0];
        *(LAS f32x4*)(lds + (uu * 4 + 3) * 1024 + lane * 16) = acc[uu][1];
      }
      WAITL0();
    }
    BAR();
  }
  if (wid >= 4) {
    __builtin_amdgcn_s_setprio(0);
    const int hw = wid - 4;
    const int nu = hw < 3 ? 2 : 1;
    const unsigned tag = get_tag(lds);
    const int tok0 = 128 * m + 32 * r + 8 * (i16 >> 2) + (i16 & 3);
    const rsrc_t ry = mk_rsrc(ws + WS_PY, 64 * 16 * 7168), rd = mk_rsrc(ws + WS_PD, 64 * 16 * 7168);
    u32x2 x0[2], x1[2];
    u32x4 fy[2][3], fd[2][3];
#pragma unroll
    for (int k = 0; k < 2; ++k) {
      const int u = 2 * hw + (k < nu ? k : 0);
      const int ecol = 112 * n + 16 * u + 4 * g;
      x0[k] = *(const u32x2*)(a.x + (size_t)tok0 * D + ecol); x1[k] = *(const u32x2*)(a.x + (size_t)(tok0 + 4) * D + ecol);
    }
    {
      const int rr = (r + 1 + (lane % 3)) & 3;
      poll_all(ws + WS_FY + ((tile * 4 + rr) * 4 + r) * 64, lane < 3, tag);
    }
#pragma unroll
    for (int k = 0; k < 2; ++k)
#pragma unroll
      for (int i = 0; i < 3; ++i)
        fy[k][i] = __builtin_amdgcn_raw_buffer_load_b128(ry, ((tile * 4 + ((r + 1 + i) & 3)) * 4 + r) * 7168 + (2 * hw + (k < nu ? k : 0)) * 1024 + lane * 16, 0, 16);
    STAMP(20);
    if (MODE == 0 && blockIdx.x == 0 && hw == 3) {
#pragma unroll 1
      for (int i = 0; i < 4; ++i) poll_all(ws + WS_ARRIVE + (64 * i + lane) * 64, true, tag);
      if (lane == 0) st_flag(ws + WS_NONCE, tag);
    }
    for (unsigned sp = 0; sp < (SPIN_MAX >> 3); ++sp) {
      const unsigned v = lane < 3 ? __builtin_amdgcn_raw_buffer_load_b32(rd, ((tile * 4 + ((r + 1 + lane) & 3)) * 4 + r) * 7168 + 6 * 1024 + 63 * 16 + 12, 0, 16) : 0u;
      if (__all(v != 0xffffffffu)) break;
      __builtin_amdgcn_s_sleep(1);
    }
    for (unsigned sp = 0; sp < (SPIN_MAX >> 3); ++sp) {
      bool ok = true;
#pragma unroll
      for (int k = 0; k < 2; ++k)
#pragma unroll
        for (int i = 0; i < 3; ++i) {
          fd[k][i] = __builtin_amdgcn_raw_buffer_load_b128(rd, ((tile * 4 + ((r + 1 + i) & 3)) * 4 + r) * 7168 + (2 * hw + (k < nu ? k : 0)) * 1024 + lane * 16, 0, 16);
        }
#pragma unroll
      for (int k = 0; k < 2; ++k)
#pragma unroll
        for (int i = 0; i < 3; ++i)
          ok = ok && fd[k][i][0] != 0xffffffffu && fd[k][i][1] != 0xffffffffu && fd[k][i][2] != 0xffffffffu && fd[k][i][3] != 0xffffffffu;
      if (__all(ok)) break;
      __builtin_amdgcn_s_sleep(2);
    }
    STAMP(21); STAMPR(29);
    BAR();
#pragma unroll
    for (int k = 0; k < 2; ++k) {
      if (k < nu) {
        const int u = 2 * hw + k;
        const int ecol = 112 * n + 16 * u + 4 * g;
        f32x4 y0 = *(LAS f32x4*)(lds + (u * 4 + 0) * 1024 + lane * 16), y1 = *(LAS f32x4*)(lds + (u * 4 + 1) * 1024 + lane * 16);
        f32x4 d0 = *(LAS f32x4*)(lds + (u * 4 + 2) * 1024 + lane * 16), d1 = *(LAS f32x4*)(lds + (u * 4 + 3) * 1024 + lane * 16);
#pragma unroll
        for (int i = 0; i < 3; ++i) {
          y0 += f32x4{bf_lo(fy[k][i][0]), bf_hi(fy[k][i][0]), bf_lo(fy[k][i][1]), bf_hi(fy[k][i][1])};
          y1 += f32x4{bf_lo(fy[k][i][2]), bf_hi(fy[k][i][2]), bf_lo(fy[k][i][3]), bf_hi(fy[k][i][3])};
          d0 += f32x4{bf_lo(fd[k][i][0]), bf_hi(fd[k][i][0]), bf_lo(fd[k][i][1]), bf_hi(fd[k][i][1])};
          d1 += f32x4{bf_lo(fd[k][i][2]), bf_hi(fd[k][i][2]), bf_lo(fd[k][i][3]), bf_hi(fd[k][i][3])};
        }
        const u32x2 o0 = {pk_bf16(bf_lo(x0[k][0]) + y0[0] * silu_f(d0[0]), bf_hi(x0[k][0]) + y0[1] * silu_f(d0[1])),
                          pk_bf16(bf_lo(x0[k][1]) + y0[2] * silu_f(d0[2]), bf_hi(x0[k][1]) + y0[3] * silu_f(d0[3]))};
        const u32x2 o1 = {pk_bf16(bf_lo(x1[k][0]) + y1[0] * silu_f(d1[0]), bf_hi(x1[k][0]) + y1[1] * silu_f(d1[1])),
                          pk_bf16(bf_lo(x1[k][1]) + y1[2] * silu_f(d1[2]), bf_hi(x1[k][1]) + y1[3] * silu_f(d1[3]))};
        const rsrc_t ro = mk_rsrc(a.out, S * D * 2);
        __builtin_amdgcn_raw_buffer_store_b64(o0, ro, (tok0 * D + ecol) * 2, 0, OUTAUX);
        __builtin_amdgcn_raw_buffer_store_b64(o1, ro, ((tok0 + 4) * D + ecol) * 2, 0, OUTAUX);
      }
    }
  }
}

#define SPLIT 0
extern "C" void kernel_launch(void* const* d_in, const int* in_sizes, int n_in, void* d_out, int out_size, void* d_ws, size_t ws_size,
                              hipStream_t stream) {
  Args a;
  a.x = (const u16*)d_in[0]; a.Cr = (const u16*)d_in[1]; a.Ci = (const u16*)d_in[2]; a.Dr = (const u16*)d_in[3]; a.Di = (const u16*)d_in[4];
  a.Wg = (const u16*)d_in[6]; a.Wu = (const u16*)d_in[7]; a.Wd = (const u16*)d_in[8];
  a.ws = (char*)d_ws; a.out = (u16*)d_out;
  (void)hipFuncSetAttribute((const void*)fused_kernel<0>, hipFuncAttributeMaxDynamicSharedMemorySize, L_TOTAL);
  fused_kernel<0><<<256, 512, L_TOTAL, stream>>>(a);
}
```

```cpp
#include <hip/hip_runtime.h>
#include <hip/hip_bf16.h>
#include <stdint.h>

typedef unsigned short u16;
typedef __bf16 bf16x8 __attribute__((ext_vector_type(8)));
typedef short s16x8 __attribute__((ext_vector_type(8)));
typedef float f32x4 __attribute__((ext_vector_type(4)));
typedef float f32x2 __attribute__((ext_vector_type(2)));
typedef unsigned u32x4 __attribute__((ext_vector_type(4)));
typedef unsigned u32x2 __attribute__((ext_vector_type(2)));
#define LAS __attribute__((address_space(3)))
#define GAS __attribute__((address_space(1)))
typedef LAS char* lptr;
typedef __amdgpu_buffer_rsrc_t rsrc_t;

constexpr int S = 2048, D = 448, F = 1792;

constexpr size_t WS_NONCE  = 0;
constexpr size_t WS_ARRIVE = 4096;
constexpr size_t WS_FAB    = 32768;
constexpr size_t WS_FH     = 49152;
constexpr size_t WS_FWD    = 65536;
constexpr size_t WS_FY     = 81920;
constexpr size_t WS_FD     = 147456;
constexpr size_t WS_ST     = 212992;
constexpr size_t WS_AB     = 262144;
constexpr size_t WS_H      = WS_AB + (size_t)2 * 28 * 64 * 1024;
constexpr size_t WS_WDT    = WS_H + (size_t)128 * 56 * 1024;
constexpr size_t WS_PY     = WS_WDT + (size_t)28 * 56 * 1024;
constexpr size_t WS_PD     = WS_PY + (size_t)64 * 16 * 7 * 1024;

#define STAMP(i)
#define STAMPR(i)
#define SBAR() __builtin_amdgcn_sched_barrier(0)
#define BAR() __builtin_amdgcn_s_barrier()
#define WAITV(n) asm volatile("s_waitcnt vmcnt(%0)" ::"n"(n) : "memory")
#define WAITL0() __builtin_amdgcn_s_waitcnt(0xC07F)
#define PF0 2
#define STAUX 16
#define SPIN_MAX (1u << 18)

__device__ __forceinline__ rsrc_t mk_rsrc(const void* p, unsigned bytes) {
  return __builtin_amdgcn_make_buffer_rsrc((void*)p, 0, bytes, 0x00020000);
}
__device__ __forceinline__ u32x4 mk_srd(const void* p, unsigned bytes) {
  const unsigned long long a = (unsigned long long)p;
  return u32x4{(unsigned)a, (unsigned)(a >> 32) & 0xffffu, bytes, 0x00020000u};
}
__device__ __forceinline__ unsigned lds_addr(lptr p) { return (unsigned)(size_t)p; }
__device__ __forceinline__ void dma16(const u32x4& srd, unsigned dst, int voff, int soff) {
  asm volatile("s_mov_b32 m0, %0\n\ts_nop 0\n\tbuffer_load_dwordx4 %1, %2, %3 offen lds" ::"s"(dst), "v"(voff), "s"(srd), "s"(soff) : "memory");
}
__device__ __forceinline__ void pf4(const char* p, unsigned dst) {
  asm volatile("s_mov_b32 m0, %0\n\ts_nop 0\n\tglobal_load_lds_dword %1, off" ::"s"(dst), "v"(p) : "memory");
}
__device__ __forceinline__ bf16x8 ldsr128(lptr p) { return *(const LAS bf16x8*)p; }
__device__ __forceinline__ bf16x8 ldstr(lptr p0, lptr p1) {
  typedef short v4i16_t __attribute__((ext_vector_type(4)));
  v4i16_t lo = __builtin_amdgcn_ds_read_tr16_b64_v4i16((LAS v4i16_t*)p0);
  v4i16_t hi = __builtin_amdgcn_ds_read_tr16_b64_v4i16((LAS v4i16_t*)p1);
  s16x8 v = __builtin_shufflevector(lo, hi, 0, 1, 2, 3, 4, 5, 6, 7);
  return __builtin_bit_cast(bf16x8, v);
}
__device__ __forceinline__ f32x4 mfma(bf16x8 a, bf16x8 b, f32x4 c) { return __builtin_amdgcn_mfma_f32_16x16x32_bf16(a, b, c, 0, 0, 0); }
__device__ __forceinline__ void mfma_ip(f32x4& c, const bf16x8& a, const bf16x8& b) {
  asm volatile("v_mfma_f32_16x16x32_bf16 %0, %1, %2, %0" : "+v"(c) : "v"(a), "v"(b));
}
#define FILL() asm volatile("s_nop 0")
#define PAD_MFMA_OUT() asm volatile("s_nop 7\n\ts_nop 3")
#define PAD_MFMA_IN() asm volatile("s_nop 3")
__device__ __forceinline__ unsigned pk_bf16(float lo, float hi) {
  typedef __bf16 bf16x2 __attribute__((ext_vector_type(2)));
  f32x2 v = {lo, hi};
  bf16x2 b = __builtin_convertvector(v, bf16x2);
  return __builtin_bit_cast(unsigned, b);
}
__device__ __forceinline__ float bf_lo(unsigned v) { return __uint_as_float(v << 16); }
__device__ __forceinline__ float bf_hi(unsigned v) { return __uint_as_float(v & 0xffff0000u); }
__device__ __forceinline__ float rbf(float f) { return bf_lo(pk_bf16(f, 0.f)); }
__device__ __forceinline__ float silu_f(float x) {
  float e = __builtin_amdgcn_exp2f(x * -1.4426950409f);
  return x * __builtin_amdgcn_rcpf(1.0f + e);
}
__device__ __forceinline__ float wave_sum(float v) {
  v += __builtin_bit_cast(float, __builtin_amdgcn_update_dpp(0, __builtin_bit_cast(int, v), 0x111, 0xf, 0xf, true));
  v += __builtin_bit_cast(float, __builtin_amdgcn_update_dpp(0, __builtin_bit_cast(int, v), 0x112, 0xf, 0xf, true));
  v += __builtin_bit_cast(float, __builtin_amdgcn_update_dpp(0, __builtin_bit_cast(int, v), 0x114, 0xf, 0xf, true));
  v += __builtin_bit_cast(float, __builtin_amdgcn_update_dpp(0, __builtin_bit_cast(int, v), 0x118, 0xf, 0xf, true));
  v += __builtin_bit_cast(float, __builtin_amdgcn_update_dpp(0, __builtin_bit_cast(int, v), 0x142, 0xa, 0xf, true));
  v += __builtin_bit_cast(float, __builtin_amdgcn_update_dpp(0, __builtin_bit_cast(int, v), 0x143, 0xc, 0xf, true));
  return __builtin_bit_cast(float, __builtin_amdgcn_readlane(__builtin_bit_cast(int, v), 63));
}
__device__ __forceinline__ unsigned ld_flag(const void* p) { return __hip_atomic_load((const GAS unsigned*)p, __ATOMIC_RELAXED, __HIP_MEMORY_SCOPE_AGENT); }
__device__ __forceinline__ void st_flag(void* p, unsigned v) { __hip_atomic_store((GAS unsigned*)p, v, __ATOMIC_RELAXED, __HIP_MEMORY_SCOPE_AGENT); }
__device__ __forceinline__ unsigned get_tag(lptr lds);
__device__ __forceinline__ void poll_all(const char* p, bool active, unsigned tag) {
  for (unsigned sp = 0; sp < SPIN_MAX; ++sp) {
    const unsigned v = active ? ld_flag(p) : tag;
    if (__all(v == tag)) break;
    __builtin_amdgcn_s_sleep(2);
  }
}
__device__ __forceinline__ void lds_spin(lptr p) {
  for (unsigned sp = 0; sp < SPIN_MAX * 4; ++sp) {
    if (*(volatile LAS unsigned*)p != 0u) break;
    __builtin_amdgcn_s_sleep(2);
  }
}

constexpr int K1_STAGE = 53248, K1_NST = 7, K1_WO = 16384;
constexpr int L_MISC = 3 * K1_STAGE;
constexpr int L_STATS = L_MISC;
constexpr int L_CS = L_MISC + 1024;
constexpr int L_WORDS = L_CS + 18 * 64;
constexpr int L_DUMMY = L_WORDS + 128;
constexpr int L_TOTAL = L_DUMMY + 512;
#define YREP 1
constexpr int P_STAGE = 30720, P_NY = 16 * YREP, P_ND = 7, P_NST = P_NY + P_ND;
static_assert(L_TOTAL <= 163840 && 5 * P_STAGE <= L_MISC, "LDS");

__device__ __forceinline__ unsigned get_tag(lptr lds) { return *(volatile LAS unsigned*)(lds + L_WORDS + 16); }

struct Args {
  const u16 *x, *Cr, *Ci, *Dr, *Di, *Wg, *Wu, *Wd;
  char* ws;
  u16* out;
};

template <int MODE, int NG, int NA>
__device__ __forceinline__ void p1_consumer(lptr lds, char* __restrict__ ws, int mt, int grp, int f0, int a0, int wm, int wn, int lane, const char* cp) {
  constexpr int NBK = 2 * NG, NCQ = NBK / 4, WSZ = NBK * 8 * 256, KSO = 4 * NCQ * 1024;
  constexpr int GO = K1_WO, UO = K1_WO + WSZ, DO = K1_WO + 2 * WSZ;
  const int i16 = lane & 15, g = lane >> 4, q = i16 >> 2, p = i16 & 3;
  const int rowx = 64 * wm + 8 * (i16 >> 2) + (i16 & 3);
  const int vx0 = rowx * 128 + ((g) ^ (i16 & 7)) * 16;
  const int vx1 = rowx * 128 + ((g + 4) ^ (i16 & 7)) * 16;
  const int fw = ((q >> 1) & 1) | ((g & 1) << 1);
  int vw[NG];
#pragma unroll
  for (int j = 0; j < NG; ++j) { const int nb = NG * wn + j; vw[j] = g * NCQ * 1024 + (nb >> 2) * 1024 + q * 128 + (((nb & 3) ^ fw) * 32) + p * 8; }
  const int vd0 = DO + (16 * NA * wn + i16) * 128 + ((g) ^ (i16 & 7)) * 16;
  const int vd1 = DO + (16 * NA * wn + i16) * 128 + ((g + 4) ^ (i16 & 7)) * 16;

  f32x4 G[NG][4], U[NG][4], AB[NA][4];
#pragma unroll
  for (int j = 0; j < NG; ++j)
#pragma unroll
    for (int t = 0; t < 4; ++t) { G[j][t] = f32x4{0, 0, 0, 0}; U[j][t] = f32x4{0, 0, 0, 0}; }
#pragma unroll
  for (int j = 0; j < NA; ++j)
#pragma unroll
    for (int t = 0; t < 4; ++t) AB[j][t] = f32x4{0, 0, 0, 0};

  bf16x8 X0[4], X1[4], WG[NG], WU[NG], WD[NA];
  auto ldx = [&](bf16x8 (&X)[4], lptr st, int ks) {
#pragma unroll
    for (int t = 0; t < 4; ++t) X[t] = ldsr128(st + (ks ? vx1 : vx0) + (32 * (t >> 1) + 4 * (t & 1)) * 128);
  };
  auto ldg = [&](int j, lptr st, int ks) { lptr b = st + GO + ks * KSO + vw[j]; WG[j] = ldstr(b, b + 512); };
  auto ldu = [&](int j, lptr st, int ks) { lptr b = st + UO + ks * KSO + vw[j]; WU[j] = ldstr(b, b + 512); };
  auto ldd = [&](int j, lptr st, int ks) { WD[j] = ldsr128(st + (ks ? vd1 : vd0) + j * 16 * 128); };
  auto grp4 = [&](f32x4 (&C)[4], const bf16x8& W, bf16x8 (&X)[4], bool wfirst, auto mid) {
    if (wfirst) { mfma_ip(C[0], W, X[0]); mfma_ip(C[1], W, X[1]); } else { mfma_ip(C[0], X[0], W); mfma_ip(C[1], X[1], W); }
    SBAR(); mid(); SBAR();
    if (wfirst) { mfma_ip(C[2], W, X[2]); mfma_ip(C[3], W, X[3]); } else { mfma_ip(C[2], X[2], W); mfma_ip(C[3], X[3], W); }
    SBAR();
  };
  auto fill = [&]() { FILL(); };
  auto ldx1 = [&](bf16x8 (&X)[4], lptr st, int ks, int t) { X[t] = ldsr128(st + (ks ? vx1 : vx0) + (32 * (t >> 1) + 4 * (t & 1)) * 128); };
  auto blockA = [&](lptr st) {
#pragma unroll
    for (int j = 0; j < NG; ++j) {
      grp4(G[j], WG[j], X0, true, [&]() { if (2 * j < 4) ldx1(X1, st, 1, 2 * j); else FILL(); }); ldg(j, st, 1); SBAR();
      grp4(U[j], WU[j], X0, true, [&]() { if (2 * j + 1 < 4) ldx1(X1, st, 1, 2 * j + 1); else FILL(); }); ldu(j, st, 1); SBAR();
    }
#pragma unroll
    for (int j = 0; j < NA; ++j) { grp4(AB[j], WD[j], X0, false, fill); ldd(j, st, 1); SBAR(); }
  };
  auto blockB_head = [&]() { grp4(G[0], WG[0], X1, true, fill); FILL(); grp4(U[0], WU[0], X1, true, fill); };
  auto blockB_rest = [&](lptr sn, bool reload) {
    int k = 0;
    auto mid = [&]() {
      if (!reload) FILL();
      else if (k == 0) ldg(0, sn, 0);
      else if (k == 1) ldu(0, sn, 0);
      else if (k < 6) ldx1(X0, sn, 0, k - 2);
      else FILL();
      ++k;
    };
#pragma unroll
    for (int j = 1; j < NG; ++j) {
      grp4(G[j], WG[j], X1, true, mid); if (reload) { ldg(j, sn, 0); SBAR(); } else FILL();
      grp4(U[j], WU[j], X1, true, mid); if (reload) { ldu(j, sn, 0); SBAR(); } else FILL();
    }
#pragma unroll
    for (int j = 0; j < NA; ++j) { grp4(AB[j], WD[j], X1, false, mid); if (reload) { ldd(j, sn, 0); SBAR(); } else FILL(); }
  };

#pragma unroll
  for (int j = 0; j < NG; ++j)
#pragma unroll
    for (int t = 0; t < 4; ++t) { asm volatile("" : "+v"(G[j][t])); asm volatile("" : "+v"(U[j][t])); }
#pragma unroll
  for (int j = 0; j < NA; ++j)
#pragma unroll
    for (int t = 0; t < 4; ++t) asm volatile("" : "+v"(AB[j][t]));
  PAD_MFMA_IN();
  BAR();
  STAMP(2);
  ldx(X0, lds, 0);
#pragma unroll
  for (int j = 0; j < NG; ++j) { ldg(j, lds, 0); ldu(j, lds, 0); }
#pragma unroll
  for (int j = 0; j < NA; ++j) ldd(j, lds, 0);
  SBAR();
  blockA(lds);
  blockB_head();
  WAITL0(); BAR();
  int slot = 1;
#pragma unroll 1
  for (int s = 0; s < K1_NST - 1; ++s) {
    lptr sn = lds + slot * K1_STAGE;
    slot = (slot == 2) ? 0 : slot + 1;
    if (s >= PF0 && s < PF0 + 4) { pf4(cp, lds_addr(lds) + L_DUMMY); cp += 2 * 4096; }
    blockB_rest(sn, true);
    blockA(sn);
    blockB_head();
    WAITL0();
    if (s == 2) { STAMP(8); }
    BAR();
    if (s == 2) { STAMP(9); }
    if (s == 1) { STAMP(10); }
  }
  blockB_rest(lds, false);
  PAD_MFMA_OUT();
  STAMP(3);
  BAR();
  const lptr cs = lds + L_CS;
  const lptr stats = lds + L_STATS;
  {
    const rsrc_t rab = mk_rsrc(ws + WS_AB, 2 * 28 * 64 * 1024);
#pragma unroll
    for (int pi = 0; pi < 2; ++pi) {
      const lptr sp = stats + (64 * wm + 32 * pi + 8 * g) * 8;
      const f32x4 s0 = *(LAS f32x4*)(sp), s1 = *(LAS f32x4*)(sp + 16), s2 = *(LAS f32x4*)(sp + 32), s3 = *(LAS f32x4*)(sp + 48);
#pragma unroll
      for (int j = 0; j < NA; ++j) {
        const float c = *(LAS float*)(cs + (2 * NBK + NA * wn + j) * 64 + i16 * 4);
        const f32x4 a = AB[j][2 * pi], b = AB[j][2 * pi + 1];
        const int ks = mt * 4 + wm * 2 + pi;
        u32x4 o;
        o[0] = pk_bf16((a[0] - s0[0] * c) * s0[1], (a[1] - s0[2] * c) * s0[3]);
        o[1] = pk_bf16((a[2] - s1[0] * c) * s1[1], (a[3] - s1[2] * c) * s1[3]);
        o[2] = pk_bf16((b[0] - s2[0] * c) * s2[1], (b[1] - s2[2] * c) * s2[3]);
        o[3] = pk_bf16((b[2] - s3[0] * c) * s3[1], (b[3] - s3[2] * c) * s3[3]);
        __builtin_amdgcn_raw_buffer_store_b128(o, rab, lane * 16 + ((a0 + NA * wn + j) * 64 + ks) * 1024, 0, STAUX);
      }
    }
  }
  const rsrc_t rh = mk_rsrc(ws + WS_H, 128 * 56 * 1024);
  const int tt0 = mt * 8 + wm * 4;
  f32x2 st[4];
#pragma unroll
  for (int t = 0; t < 4; ++t) st[t] = *(LAS f32x2*)(stats + (64 * wm + 32 * (t >> 1) + 8 * (i16 >> 2) + 4 * (t & 1) + (i16 & 3)) * 8);
#pragma unroll
  for (int pp = 0; pp < NG / 2; ++pp) {
    const int ks = ((f0 >> 4) + NG * wn + 2 * pp) >> 1;
    f32x4 cg[2], cu[2];
#pragma unroll
    for (int j = 0; j < 2; ++j) {
      cg[j] = *(LAS f32x4*)(cs + (NG * wn + 2 * pp + j) * 64 + g * 16);
      cu[j] = *(LAS f32x4*)(cs + (NBK + NG * wn + 2 * pp + j) * 64 + g * 16);
    }
#pragma unroll
    for (int th = 0; th < 2; ++th) {
      f32x2 gg[8], uu[8], z[8];
#pragma unroll
      for (int i = 0; i < 8; ++i) {
        const int t = 2 * th + (i >> 2), j = (i >> 1) & 1, hh = i & 1;
        const f32x2 c2 = {cg[j][2 * hh], cg[j][2 * hh + 1]};
        const f32x2 a2 = {G[2 * pp + j][t][2 * hh], G[2 * pp + j][t][2 * hh + 1]};
        gg[i] = (a2 - c2 * st[t].x) * st[t].y;
      }
#pragma unroll
      for (int i = 0; i < 8; ++i) z[i] = gg[i] * ((gg[i] * gg[i]) * -0.1029432397f + -2.302208198f);
#pragma unroll
      for (int i = 0; i < 8; ++i) { z[i].x = __builtin_amdgcn_exp2f(z[i].x); z[i].y = __builtin_amdgcn_exp2f(z[i].y); }
#pragma unroll
      for (int i = 0; i < 8; ++i) {
        const int t = 2 * th + (i >> 2), j = (i >> 1) & 1, hh = i & 1;
        const f32x2 c2 = {cu[j][2 * hh], cu[j][2 * hh + 1]};
        const f32x2 a2 = {U[2 * pp + j][t][2 * hh], U[2 * pp + j][t][2 * hh + 1]};
        uu[i] = ((a2 - c2 * st[t].x) * st[t].y) * gg[i];
        z[i] = z[i] + 1.0f;
      }
#pragma unroll
      for (int i = 0; i < 8; ++i) { z[i].x = __builtin_amdgcn_rcpf(z[i].x); z[i].y = __builtin_amdgcn_rcpf(z[i].y); }
#pragma unroll
      for (int i = 0; i < 8; ++i) uu[i] = uu[i] * z[i];
#pragma unroll
      for (int tl = 0; tl < 2; ++tl) {
        const u32x4 o = {pk_bf16(uu[4 * tl].x, uu[4 * tl].y), pk_bf16(uu[4 * tl + 1].x, uu[4 * tl + 1].y),
                         pk_bf16(uu[4 * tl + 2].x, uu[4 * tl + 2].y), pk_bf16(uu[4 * tl + 3].x, uu[4 * tl + 3].y)};
        __builtin_amdgcn_raw_buffer_store_b128(o, rh, lane * 16 + ((tt0 + 2 * th + tl) * 56 + ks) * 1024, 0, STAUX);
      }
      if (pp == 0 && th == 0) {
        WAITV(2);
        if (lane == 0) {
          const unsigned n = __hip_atomic_fetch_add((LAS unsigned*)(lds + L_WORDS), 1u, __ATOMIC_RELAXED, __HIP_MEMORY_SCOPE_WORKGROUP);
          if (n == 3u) st_flag(ws + WS_FAB + (mt * 16 + grp) * 64, get_tag(lds));
        }
        STAMP(5);
      }
    }
  }
  STAMP(4);
  if (MODE == 1) {
    WAITV(0);
    if (lane == 0) {
      const unsigned n = __hip_atomic_fetch_add((LAS unsigned*)(lds + L_WORDS + 4), 1u, __ATOMIC_RELAXED, __HIP_MEMORY_SCOPE_WORKGROUP);
      if (n == 3u) st_flag(ws + WS_FH + (mt * 16 + grp) * 64, get_tag(lds));
    }
  }
}

template <int NBK, int NWU, int NDU>
struct ColSum {
  f32x4 cw[NWU > 0 ? NWU : 1], cd[NDU > 0 ? NDU : 1];
  __device__ __forceinline__ void init() {
#pragma unroll
    for (int j = 0; j < (NWU > 0 ? NWU : 1); ++j) cw[j] = f32x4{0, 0, 0, 0};
#pragma unroll
    for (int j = 0; j < (NDU > 0 ? NDU : 1); ++j) cd[j] = f32x4{0, 0, 0, 0};
  }
  bf16x8 w[2][NWU > 0 ? NWU : 1], d[2][NDU > 0 ? NDU : 1];
  __device__ __forceinline__ void load(lptr st, int up, int w0, int d0, int lane, int ks) {
    constexpr int NCQ = NBK / 4, WSZ = NBK * 8 * 256, KSO = 4 * NCQ * 1024, DO = K1_WO + 2 * WSZ;
    const int i16 = lane & 15, g = lane >> 4, q = i16 >> 2, p = i16 & 3;
    const int fw = ((q >> 1) & 1) | ((g & 1) << 1);
    const int vb = K1_WO + up * WSZ + (w0 >> 2) * 1024 + g * NCQ * 1024 + q * 128 + p * 8;
#pragma unroll
    for (int j = 0; j < NWU; ++j) { lptr b = st + vb + ((j ^ fw) * 32) + ks * KSO; w[ks][j] = ldstr(b, b + 512); }
#pragma unroll
    for (int j = 0; j < NDU; ++j) d[ks][j] = ldsr128(st + DO + (16 * (d0 + j) + i16) * 128 + ((g + 4 * ks) ^ (i16 & 7)) * 16);
  }
  __device__ __forceinline__ void mma(int ks) {
    const u32x4 ones_u = {0x3f803f80u, 0x3f803f80u, 0x3f803f80u, 0x3f803f80u};
    const bf16x8 ONES = __builtin_bit_cast(bf16x8, ones_u);
#pragma unroll
    for (int j = 0; j < NWU; ++j) cw[j] = mfma(w[ks][j], ONES, cw[j]);
#pragma unroll
    for (int j = 0; j < NDU; ++j) cd[j] = mfma(ONES, d[ks][j], cd[j]);
  }
  __device__ __forceinline__ void store(lptr lds, int up, int w0, int d0, int lane) {
    const int i16 = lane & 15, g = lane >> 4;
#pragma unroll
    for (int j = 0; j < NWU; ++j)
      if (i16 == 0) *(LAS f32x4*)(lds + L_CS + (up * NBK + w0 + j) * 64 + g * 16) = cw[j];
#pragma unroll
    for (int j = 0; j < NDU; ++j)
      if (g == 0) *(LAS float*)(lds + L_CS + (2 * NBK + d0 + j) * 64 + i16 * 4) = cd[j][0];
  }
};

template <int MODE, bool TB, int NWU, int NDU>
__device__ __forceinline__ void p1_helper(lptr lds, const Args& a, int xcd, int idx, int mt, int grp, int f0, int a0, int h, int lane, unsigned nonce) {
  char* ws = a.ws;
  constexpr int NBK = TB ? 4 : 8, WSZ = NBK * 8 * 256;
  constexpr int NWP = TB ? 4 : 8, NDP = TB ? 4 : 1, PER = 4 + NWP + NDP;
  const int mh = xcd & 1, nq = xcd >> 1;
  const int up = h >> 1;
  const u32x4 rx = mk_srd(a.x, S * D * 2);
  const u32x4 rw = mk_srd(up ? a.Wu : a.Wg, D * F * 2);
  const u32x4 rd = mk_srd(a0 >= 28 ? a.Di : a.Dr, D * D * 2);
  const int e0 = (a0 >= 28 ? a0 - 28 : a0) * 16;
  const int r8 = lane >> 3, c8 = lane & 7;
  int vx[2];
  vx[0] = r8 * 896 + (c8 ^ (r8 & 3)) * 16;
  vx[1] = r8 * 896 + (c8 ^ ((r8 & 3) | 4)) * 16;
  const int vd = r8 * 896 + (c8 ^ r8) * 16;
  int vw[2];
  {
    const int f0_ = (r8 >> 1) & 1;
    vw[0] = r8 * (F * 2) + f0 * 2 + ((((c8 >> 1) ^ f0_) * 2) + (c8 & 1)) * 16;
    vw[1] = r8 * (F * 2) + f0 * 2 + ((((c8 >> 1) ^ (f0_ | 2)) * 2) + (c8 & 1)) * 16;
  }
  const unsigned l0 = lds_addr(lds);
  const unsigned dummy = l0 + L_DUMMY + (h & 1) * 256;
  auto piece = [&](int s, int slot, int i) {
    const unsigned st = l0 + slot * K1_STAGE;
    if (i < 4) { const int pc = 4 * h + i; dma16(rx, st + pc * 1024, vx[i & 1], (mt * 128 + 8 * pc) * 896 + s * 128); }
    else if (i < 4 + NWP) {
      const int j = i - 4, pj = NWP * (h & 1) + j, kb = TB ? pj : pj >> 1, cq = TB ? 0 : pj & 1;
      dma16(rw, st + K1_WO + up * WSZ + pj * 1024, vw[TB ? (j & 1) : ((j >> 1) & 1)], (s * 64 + 8 * kb) * (F * 2) + cq * 128);
    } else { const int pc = NDP * h + (i - 4 - NWP); dma16(rd, st + K1_WO + 2 * WSZ + pc * 1024, vd, (e0 + 8 * pc) * 896 + s * 128); }
  };
  auto issue_a = [&](int s, int slot) {
#pragma unroll
    for (int i = 0; i < 8; ++i) piece(s, slot, i);
  };
  auto issue_b = [&](int s, int slot) {
#pragma unroll
    for (int i = 8; i < PER; ++i) piece(s, slot, i);
  };
  const int ln2 = lane >> 1, hb = (lane & 1) * 64;
  const char* pfx = (const char*)a.x + (size_t)(1024 * mh + 32 * idx + ln2) * 896 + hb;
  const char* pfw;
  {
    const int j = ln2 < 28 ? ln2 : 27;
    const int mat = j >= 14, jj = j - 14 * mat, rr = jj >= 7, ln = jj - 7 * rr;
    pfw = (const char*)(mat ? a.Wu : a.Wg) + (size_t)(2 * idx + rr) * (F * 2) + 896 * nq + 128 * ln + hb;
  }
  const int srow = mt * 128 + 8 * grp + 2 * h;
  u32x4 sx0, sx1;
  {
    const u16* sp = a.x + (size_t)srow * D + (lane < 56 ? lane : 55) * 8;
    asm volatile("global_load_dwordx4 %0, %2, off\n\tglobal_load_dwordx4 %1, %2, off offset:896" : "=&v"(sx0), "=&v"(sx1) : "v"(sp) : "memory");
  }
  STAMP(1);
  if (h < 3) { pf4(pfx + h * 128, dummy); pf4(pfw + (size_t)h * (64 * F * 2), dummy); }
  else {
    const char* db = (const char*)(nq >= 2 ? a.Di : a.Dr) + (size_t)(224 * (nq & 1) + 7 * idx) * 896 + hb;
    pf4(db + ln2 * 128, dummy);
    pf4(db + (ln2 < 17 ? 32 + ln2 : 48) * 128, dummy);
  }
  STAMP(3);
  issue_a(0, 0); issue_b(0, 0);
  STAMP(5);
  constexpr int WD_SCR = 2 * K1_STAGE + (TB ? 45056 : 40960);
  const bool wdjob = blockIdx.x < 196;
  const int wd_kp = blockIdx.x % 28, wd_cg = blockIdx.x / 28;
  if (wdjob) {
    const u32x4 rwd = mk_srd(a.Wd, F * D * 2);
    const int rs = (lane & 15) >> 1, half = lane & 1, b = lane >> 4;
    const int vq = rs * 896 + (64 * wd_cg + 16 * b + 8 * half) * 2;
#pragma unroll
    for (int i = 0; i < 2; ++i) dma16(rwd, l0 + WD_SCR + (2 * h + i) * 1024, vq, (64 * wd_kp + 8 * (2 * h + i)) * 896);
  }
  STAMP(4);
  if (h == 3 && lane < 4) ((LAS unsigned*)(lds + L_WORDS))[lane] = 0u;
  ColSum<NBK, NWU, NDU> cs;
  cs.init();
  const int cup = TB ? (h & 1) : up, w0 = TB ? 0 : 4 * (h & 1), d0 = TB ? 4 * (h & 1) : (h & 1);
  auto slice = [&](int k, lptr st) {
    SBAR();
    if (k < 2) cs.load(st, cup, w0, d0, lane, k); else cs.mma(k - 2);
    SBAR();
  };
  STAMP(6);
  if (wdjob) { WAITV(PER + 4); } else { WAITV(PER + 2); }
  asm volatile("" : "+v"(sx0), "+v"(sx1));
  auto row_stats = [&](const u32x4& v) {
    float f[8];
#pragma unroll
    for (int i = 0; i < 4; ++i) { f[2 * i] = lane < 56 ? bf_lo(v[i]) : 0.f; f[2 * i + 1] = lane < 56 ? bf_hi(v[i]) : 0.f; }
    const float mean = wave_sum(((f[0] + f[1]) + (f[2] + f[3])) + ((f[4] + f[5]) + (f[6] + f[7]))) * (1.0f / D);
    float q = 0.f;
#pragma unroll
    for (int i = 0; i < 8; ++i) { const float d_ = f[i] - mean; q = __builtin_fmaf(d_, d_, q); }
    const float var = wave_sum(lane < 56 ? q : 0.f) * (1.0f / D);
    const float sd = rbf(__builtin_sqrtf(rbf(rbf(var) + 1e-5f)));
    return pk_bf16(mean, sd);
  };
  unsigned pay0 = row_stats(sx0), pay1 = row_stats(sx1);
  asm volatile("" : "+v"(pay0), "+v"(pay1));
  WAITL0(); WAITV(0);
  STAMP(7); BAR();
  STAMP(2);
  const unsigned tag1 = get_tag(lds);
  if (lane < 2)
    __hip_atomic_store((GAS unsigned long long*)(ws + WS_ST + (size_t)(srow + lane) * 8), ((unsigned long long)tag1 << 32) | (lane ? pay1 : pay0), __ATOMIC_RELAXED, __HIP_MEMORY_SCOPE_AGENT);
  slice(0, lds); slice(1, lds);
#pragma unroll
  for (int i = 0; i < PER; ++i) { piece(1, 1, i); if (i == 2 || i == 3) slice(i, lds); }
  if (h == 3 && wdjob) {
    const int i16 = lane & 15, g = lane >> 4, q = i16 >> 2, p = i16 & 3;
    const rsrc_t rt = mk_rsrc(ws + WS_WDT, 28 * 56 * 1024);
    lptr b0 = lds + WD_SCR + (g >> 1) * 1024 + ((g & 1) * 4 + q) * 32 + p * 8;
#pragma unroll
    for (int kk = 0; kk < 2; ++kk)
#pragma unroll
      for (int u = 0; u < 4; ++u) {
        const bf16x8 f = ldstr(b0 + kk * 4096 + u * 256, b0 + kk * 4096 + u * 256 + 2048);
        __builtin_amdgcn_raw_buffer_store_b128(__builtin_bit_cast(u32x4, f), rt, lane * 16 + ((4 * wd_cg + u) * 56 + 2 * wd_kp + kk) * 1024, 0, STAUX);
      }
    WAITL0();
  }
  issue_a(2, 2);
  pf4(pfx + (3 + h) * 128, dummy); pf4(pfw + (size_t)(3 + h) * (64 * F * 2), dummy);
  STAMP(28);
  WAITL0(); WAITV(10); BAR();
  int slot = 1;
#pragma unroll 1
  for (int s = 1; s < K1_NST; ++s) {
    const int s1_ = (slot == 2) ? 0 : slot + 1, s2_ = (slot == 0) ? 2 : slot - 1;
    lptr st = lds + slot * K1_STAGE;
    slice(0, st);
    if (s + 1 < K1_NST) {
#pragma unroll
      for (int i = 8; i < PER; ++i) { piece(s + 1, s1_, i); if (i == 8) slice(1, st); else if (i == 9) slice(2, st); else if (i == 10) slice(3, st); }
    } else { slice(1, st); slice(2, st); slice(3, st); }
    if (s + 2 < K1_NST) issue_a(s + 2, s2_);
    if (s == 4) {
      if (h == 0 && lane == 0) st_flag(ws + WS_ARRIVE + blockIdx.x * 64, tag1);
      if (h == 3 && wdjob && lane == 0) st_flag(ws + WS_FWD + blockIdx.x * 64, tag1);
    }
    if (s == 5) {
      const GAS unsigned long long* gp = (const GAS unsigned long long*)(ws + WS_ST + (size_t)(mt * 128 + 32 * h + (lane & 31)) * 8);
      unsigned long long gv = 0;
      for (unsigned sp = 0; sp < SPIN_MAX; ++sp) {
        gv = __hip_atomic_load(gp, __ATOMIC_RELAXED, __HIP_MEMORY_SCOPE_AGENT);
        if (__all((unsigned)(gv >> 32) == tag1)) break;
        __builtin_amdgcn_s_sleep(2);
      }
      if (lane < 32) *(LAS f32x2*)(lds + L_STATS + (32 * h + lane) * 8) = f32x2{bf_lo((unsigned)gv), 1.0f / bf_hi((unsigned)gv)};
    }
    WAITL0();
    if (s + 2 < K1_NST) { WAITV(8); }
    if (s == 3) { STAMP(8); }
    BAR();
    if (s == 3) { STAMP(9); }
    if (s == 2) { STAMP(10); }
    slot = s1_;
  }
  cs.store(lds, cup, w0, d0, lane);
  WAITL0(); BAR();
}

template <int MODE, int NB>
__device__ __forceinline__ void p2_loader(lptr lds, const Args& a, int xcd, int idx, int r, int m, int n, int lw, int lane, unsigned nonce) {
  char* ws = a.ws;
  constexpr int PER = 4 + NB;
  const u32x4 rc = mk_srd((r >> 1) ? a.Ci : a.Cr, S * S * 2);
  const u32x4 rb = mk_srd(ws + WS_AB, 2 * 28 * 64 * 1024);
  const u32x4 rh = mk_srd(ws + WS_H, 128 * 56 * 1024);
  const u32x4 rw = mk_srd(ws + WS_WDT, 28 * 56 * 1024);
  const int r8 = lane >> 3, c8 = lane & 7;
  int va[2];
  va[0] = r8 * 4096 + (c8 ^ (r8 & 3)) * 16;
  va[1] = r8 * 4096 + (c8 ^ ((r8 & 3) | 4)) * 16;
  const int vl = lane * 16;
  const int k0 = 1024 * (r & 1), p = r >> 1;
  const int j0 = (lw < 2) ? 4 * lw : 8 + 3 * (lw - 2);
  const unsigned l0 = lds_addr(lds);
  const unsigned dummy = l0 + L_DUMMY + (lw & 1) * 256;
  auto issueA = [&](int s, int slot) {
    const unsigned st = l0 + slot * P_STAGE;
    if (s < P_NY) {
#pragma unroll
      for (int i = 0; i < 4; ++i) { const int pc = 4 * lw + i; dma16(rc, st + pc * 1024, va[i & 1], (128 * m + 8 * pc) * 4096 + (k0 + 64 * (s & 15)) * 2); }
    } else {
#pragma unroll
      for (int i = 0; i < 4; ++i) { const int pc = 4 * lw + i; dma16(rh, st + pc * 1024, vl, ((8 * m + (pc >> 1)) * 56 + 14 * r + 2 * (s - P_NY) + (pc & 1)) * 1024); }
    }
  };
  auto issueB = [&](int s, int slot) {
    const unsigned st = l0 + slot * P_STAGE + 16384;
    if (s < P_NY) {
#pragma unroll
      for (int j = 0; j < NB; ++j) { const int jj = j0 + j; dma16(rb, st + jj * 1024, vl, ((p * 28 + 7 * n + (jj >> 1)) * 64 + (k0 >> 5) + 2 * (s & 15) + (jj & 1)) * 1024); }
    } else {
#pragma unroll
      for (int j = 0; j < NB; ++j) { const int jj = j0 + j; dma16(rw, st + jj * 1024, vl, ((7 * n + (jj >> 1)) * 56 + 14 * r + 2 * (s - P_NY) + (jj & 1)) * 1024); }
    }
  };
  __builtin_amdgcn_s_setprio(0);
  const unsigned tag = get_tag(lds);
  issueA(0, 0); issueA(1, 1);
  if (lw == 3) {
    const int mtp = 8 * (r & 1) + (lane & 7), j = (lane >> 3) & 3;
    const bool act = lane < 32 && ((n & 1) == 0 || j == 3);
    poll_all(ws + WS_FAB + (mtp * 16 + (2 * p + (n >> 1)) * 4 + j) * 64, act, tag);
    if (lane == 0) *(volatile LAS unsigned*)(lds + L_WORDS + 8) = 1u;
  } else lds_spin(lds + L_WORDS + 8);
  STAMP(16);
  {
    const int l = (lane >> 1) < 28 ? (lane >> 1) : 27;
    const int sl = l >= 14, li = 14 * idx + l - 14 * sl;
    const char* bp = ws + WS_AB + ((size_t)(p * 28 + (li >> 4)) * 64 + 32 * (r & 1) + 2 * (4 * lw + sl) + ((li >> 3) & 1)) * 1024 + (li & 7) * 128 + (lane & 1) * 64;
    pf4(bp, dummy); pf4(bp + 4 * 1024, dummy);
  }
  issueB(0, 0); issueB(1, 1);
  __builtin_amdgcn_s_setprio(3);
  WAITV(NB); BAR();
  issueA(2, 2); issueB(2, 2);
  WAITV(PER); BAR();
  issueA(3, 3); issueB(3, 3);
  const int cg0 = (112 * n) >> 6, cg1 = (112 * n + 111) >> 6, jj = lane - 4;
  const bool fact = lane < 4 || (jj < 21 && cg0 + jj / 7 <= cg1);
  const char* fp = lane < 4 ? ws + WS_FH + (m * 16 + 4 * r + lane) * 64 : ws + WS_FWD + ((fact ? cg0 + jj / 7 : cg0) * 28 + 7 * r + (fact ? jj % 7 : 0)) * 64;
  unsigned fval = 0;
  int slot = 4;
#pragma unroll 1
  for (int s = 1; s < P_NST; ++s) {
    if (lw == 3 && s == 7) asm volatile("global_load_dword %0, %1, off sc1" : "+v"(fval) : "v"(fp) : "memory");
    if (s == 10) {
      if (lw == 3) {
        asm volatile("" : "+v"(fval));
        if (!__all(!fact || fval == tag)) poll_all(fp, fact, tag);
        if (lane == 0) *(volatile LAS unsigned*)(lds + L_WORDS + 12) = 1u;
      }
    }
    if (s + 3 == P_NY && lw != 3) lds_spin(lds + L_WORDS + 12);
    if (s + 3 < P_NST && s >= 2) { issueA(s + 3, slot); issueB(s + 3, slot); }
    if (s == 1) { issueA(4, 4); issueB(4, 4); }
    if (s + 3 < P_NST) { WAITV(2 * PER); } else if (s + 2 < P_NST) { WAITV(PER); } else { WAITV(0); }
    slot = (slot == 4) ? 0 : slot + 1;
    if (s + 1 < P_NST) BAR();
  }
}

template <int MODE>
__global__ __launch_bounds__(512, 2) void fused_kernel(Args a) {
  extern __shared__ __attribute__((aligned(1024))) char smem[];
  lptr lds = (lptr)smem;
  char* ws = a.ws;
  const int tid = threadIdx.x, lane = tid & 63;
  const int wid = __builtin_amdgcn_readfirstlane(tid >> 6);
  const int xcd = blockIdx.x & 7, idx = blockIdx.x >> 3;
  STAMP(0); STAMPR(30);
  const unsigned nonce = 0;
  if (wid == 0) {
    const unsigned tg = (MODE == 0) ? ld_flag(ws + WS_NONCE) * 0x9E3779B1u + 0x7F4A7C15u : 0x51u;
    if (lane == 0 && MODE != 2) *(volatile LAS unsigned*)(lds + L_WORDS + 16) = tg;
    WAITL0();
  }
  if (wid >= 4) __builtin_amdgcn_s_setprio(3);

  if (MODE != 2) {
    const int mh = xcd & 1, nq = xcd >> 1;
    const int mt = mh * 8 + (idx & 7), j = idx >> 3;
    const bool tb = j == 3;
    const int grp = nq * 4 + j;
    const int f0 = 448 * nq + 128 * j, a0 = 14 * nq + 2 * j;
    if (wid < 4) {
      const char* cp = (const char*)((nq >> 1) ? a.Ci : a.Cr) + (size_t)(1024 * mh + 32 * idx + 8 * wid + (lane >> 5)) * 4096 + 2048 * (nq & 1) + (lane & 31) * 64;
      if (tb) p1_consumer<MODE, 2, 4>(lds, ws, mt, grp, f0, a0, wid & 1, wid >> 1, lane, cp);
      else p1_consumer<MODE, 4, 1>(lds, ws, mt, grp, f0, a0, wid & 1, wid >> 1, lane, cp);
    } else {
      const int h = wid - 4;
      if (tb) {
        if (h < 2) p1_helper<MODE, true, 4, 0>(lds, a, xcd, idx, mt, grp, f0, a0, h, lane, nonce);
        else p1_helper<MODE, true, 0, 4>(lds, a, xcd, idx, mt, grp, f0, a0, h, lane, nonce);
      } else {
        if (h < 2) p1_helper<MODE, false, 4, 1>(lds, a, xcd, idx, mt, grp, f0, a0, h, lane, nonce);
        else p1_helper<MODE, false, 4, 0>(lds, a, xcd, idx, mt, grp, f0, a0, h, lane, nonce);
      }
    }
  }
  if (MODE == 1) return;
  if (MODE == 2) { if (tid < 4) ((LAS unsigned*)(lds + L_WORDS))[tid] = 0u; if (tid == 4) ((LAS unsigned*)(lds + L_WORDS))[4] = 0x51u; __syncthreads(); }

  const int p1_mt = (xcd & 1) * 8 + (idx & 7), p1_grp = (xcd >> 1) * 4 + (idx >> 3);
  const int r = xcd >> 1, m = 8 * (xcd & 1) + (idx & 7), n = idx >> 3;
  const int tile = m * 4 + n;
  const int i16 = lane & 15, g = lane >> 4;
  f32x4 accY[7][2], acc[7][2];
  if (wid >= 6) {
    p2_loader<MODE, 3>(lds, a, xcd, idx, r, m, n, wid - 4, lane, nonce);
  } else if (wid >= 4) {
    p2_loader<MODE, 4>(lds, a, xcd, idx, r, m, n, wid - 4, lane, nonce);
  } else {
    const int w = wid;
    const int rowt = 32 * w + 8 * (i16 >> 2) + (i16 & 3);
    const int vt0 = rowt * 128 + ((g) ^ (i16 & 7)) * 16, vt1 = rowt * 128 + ((g + 4) ^ (i16 & 7)) * 16;
    const int ve = 16384 + lane * 16;
    const int vh = (4 * w) * 1024 + lane * 16;
    bf16x8 T0[4], T1[4], E[14];
    auto ldTy = [&](bf16x8 (&T)[4], lptr st) {
      T[0] = ldsr128(st + vt0); T[1] = ldsr128(st + vt0 + 512); T[2] = ldsr128(st + vt1); T[3] = ldsr128(st + vt1 + 512);
    };
    auto ldTd = [&](bf16x8 (&T)[4], lptr st) {
      T[0] = ldsr128(st + vh); T[1] = ldsr128(st + vh + 2048); T[2] = ldsr128(st + vh + 1024); T[3] = ldsr128(st + vh + 3072);
    };
    auto ldE = [&](int j, lptr st) { E[j] = ldsr128(st + ve + ((j % 7) * 2 + j / 7) * 1024); };
    auto mm = [&](int j, bf16x8 (&T)[4]) {
      mfma_ip(acc[j % 7][0], E[j], T[(j / 7) * 2]);
      mfma_ip(acc[j % 7][1], E[j], T[(j / 7) * 2 + 1]);
    };
#define GHEAD 3
    constexpr int GH = GHEAD;
    auto ldT1 = [&](bf16x8 (&T)[4], lptr st, int i, int NT) {
      if (NT == 0) T[i] = ldsr128(st + (i >= 2 ? vt1 : vt0) + (i & 1) * 512);
      else T[i] = ldsr128(st + vh + (i & 1) * 2048 + (i >> 1) * 1024);
    };
    auto rest = [&](bf16x8 (&Tc)[4], bf16x8 (&Tn)[4], lptr sn, int NT) {
#pragma unroll
      for (int j = GH; j < 14; ++j) {
        const int k = j - GH;
        mfma_ip(acc[j % 7][0], E[j], Tc[(j / 7) * 2]); SBAR();
        if (NT != 2 && k < 4 + GH) { if (k < 4) ldT1(Tn, sn, k, NT); else ldE(k - 4, sn); SBAR(); }
        mfma_ip(acc[j % 7][1], E[j], Tc[(j / 7) * 2 + 1]); SBAR();
        if (NT != 2) ldE(j, sn); else FILL();
        SBAR();
      }
    };
    auto head = [&](bf16x8 (&Tc)[4]) {
#pragma unroll
      for (int j = 0; j < GH; ++j) { mm(j, Tc); FILL(); }
      SBAR();
    };
#pragma unroll
    for (int u = 0; u < 7; ++u) { acc[u][0] = f32x4{0, 0, 0, 0}; acc[u][1] = f32x4{0, 0, 0, 0}; }
#pragma unroll
    for (int u = 0; u < 7; ++u) { asm volatile("" : "+v"(acc[u][0])); asm volatile("" : "+v"(acc[u][1])); }
    PAD_MFMA_IN();
    if (w != r) {
      const rsrc_t rp = mk_rsrc(ws + WS_PD + (size_t)((tile * 4 + r) * 4 + w) * 7168, 7168);
      const u32x4 sent = {0xffffffffu, 0xffffffffu, 0xffffffffu, 0xffffffffu};
#pragma unroll
      for (int u = 0; u < 7; ++u) {
        __builtin_amdgcn_raw_buffer_store_b128(sent, rp, u * 1024 + lane * 16, 0, 16);
      }
    }
    STAMP(16);
    BAR();
    STAMP(17);
    ldTy(T0, lds);
#pragma unroll
    for (int j = 0; j < 14; ++j) ldE(j, lds);
    SBAR();
    head(T0);
    WAITL0(); BAR();
    int slot = 1;
#pragma unroll 1
    for (int s = 0; s < P_NY - 2; s += 2) {
      lptr sn = lds + slot * P_STAGE; slot = (slot == 4) ? 0 : slot + 1;
      rest(T0, T1, sn, 0); head(T1);
      WAITL0(); BAR();
      sn = lds + slot * P_STAGE; slot = (slot == 4) ? 0 : slot + 1;
      rest(T1, T0, sn, 0); head(T0);
      WAITL0(); BAR();
      if (YREP > 1 && s == 14) { STAMP(23); }
      if (MODE == 0 && s == 0) {
        WAITV(0);
        if (lane == 0) {
          const unsigned cnt = __hip_atomic_fetch_add((LAS unsigned*)(lds + L_WORDS + 4), 1u, __ATOMIC_RELAXED, __HIP_MEMORY_SCOPE_WORKGROUP);
          if (cnt == 3u) st_flag(ws + WS_FH + (p1_mt * 16 + p1_grp) * 64, get_tag(lds));
        }
      }
    }
    {
      lptr sn = lds + slot * P_STAGE; slot = (slot == 4) ? 0 : slot + 1;
      rest(T0, T1, sn, 0); head(T1);
      WAITL0(); BAR();
      sn = lds + slot * P_STAGE; slot = (slot == 4) ? 0 : slot + 1;
      rest(T1, T0, sn, 1);
    }
    PAD_MFMA_OUT();
    STAMP(18);
    {
      const float sg = (r >> 1) ? -1.0f : 1.0f;
#pragma unroll
      for (int u = 0; u < 7; ++u) { accY[u][0] = acc[u][0] * sg; accY[u][1] = acc[u][1] * sg; acc[u][0] = f32x4{0, 0, 0, 0}; acc[u][1] = f32x4{0, 0, 0, 0}; }
      if (w != r) {
        const rsrc_t rp = mk_rsrc(ws + WS_PY + (size_t)((tile * 4 + r) * 4 + w) * 7168, 7168);
#pragma unroll
        for (int u = 0; u < 7; ++u) {
          const u32x4 o = {pk_bf16(accY[u][0][0], accY[u][0][1]), pk_bf16(accY[u][0][2], accY[u][0][3]),
                           pk_bf16(accY[u][1][0], accY[u][1][1]), pk_bf16(accY[u][1][2], accY[u][1][3])};
          __builtin_amdgcn_raw_buffer_store_b128(o, rp, u * 1024 + lane * 16, 0, 16);
        }
      }
    }
#pragma unroll
    for (int u = 0; u < 7; ++u) { asm volatile("" : "+v"(acc[u][0])); asm volatile("" : "+v"(acc[u][1])); }
    PAD_MFMA_IN();
    head(T0);
    WAITL0(); BAR();
#pragma unroll 1
    for (int s = 0; s < P_ND - 1; s += 2) {
      lptr sn = lds + slot * P_STAGE; slot = (slot == 4) ? 0 : slot + 1;
      rest(T0, T1, sn, 1); head(T1);
      WAITL0(); BAR();
      if (s == 0 && w != r) {
        WAITV(0);
        const unsigned tag = get_tag(lds);
        if (lane == 0) st_flag(ws + WS_FY + ((tile * 4 + r) * 4 + w) * 64, tag);
      }
      sn = lds + slot * P_STAGE; slot = (slot == 4) ? 0 : slot + 1;
      rest(T1, T0, sn, 1); head(T0);
      WAITL0(); if (s + 2 < P_ND - 1) BAR();
    }
    rest(T0, T1, lds, 2);
    PAD_MFMA_OUT();
    STAMP(19);
    if (w != r) {
      const rsrc_t rp = mk_rsrc(ws + WS_PD + (size_t)((tile * 4 + r) * 4 + w) * 7168, 7168);
#pragma unroll
      for (int u = 0; u < 7; ++u) {
        u32x4 o = {pk_bf16(acc[u][0][0], acc[u][0][1]), pk_bf16(acc[u][0][2], acc[u][0][3]),
                   pk_bf16(acc[u][1][0], acc[u][1][1]), pk_bf16(acc[u][1][2], acc[u][1][3])};
#pragma unroll
        for (int k = 0; k < 4; ++k) o[k] = o[k] < 0xfffffffeu ? o[k] : 0xfffffffeu;
        __builtin_amdgcn_raw_buffer_store_b128(o, rp, u * 1024 + lane * 16, 0, 16);
      }
    } else {
#pragma unroll
      for (int uu = 0; uu < 7; ++uu) {
        *(LAS f32x4*)(lds + (uu * 4 + 0) * 1024 + lane * 16) = accY[uu][0];
        *(LAS f32x4*)(lds + (uu * 4 + 1) * 1024 + lane * 16) = accY[uu][1];
        *(LAS f32x4*)(lds + (uu * 4 + 2) * 1024 + lane * 16) = acc[uu][0];
        *(LAS f32x4*)(lds + (uu * 4 + 3) * 1024 + lane * 16) = acc[uu][1];
      }
      WAITL0();
    }
    BAR();
  }
  if (wid >= 4) {
    __builtin_amdgcn_s_setprio(0);
    const int hw = wid - 4;
    const int nu = hw < 3 ? 2 : 1;
    const unsigned tag = get_tag(lds);
    const int tok0 = 128 * m + 32 * r + 8 * (i16 >> 2) + (i16 & 3);
    const rsrc_t ry = mk_rsrc(ws + WS_PY, 64 * 16 * 7168), rd = mk_rsrc(ws + WS_PD, 64 * 16 * 7168);
    u32x2 x0[2], x1[2];
    u32x4 fy[2][3], fd[2][3];
#pragma unroll
    for (int k = 0; k < 2; ++k) {
      const int u = 2 * hw + (k < nu ? k : 0);
      const int ecol = 112 * n + 16 * u + 4 * g;
      x0[k] = *(const u32x2*)(a.x + (size_t)tok0 * D + ecol); x1[k] = *(const u32x2*)(a.x + (size_t)(tok0 + 4) * D + ecol);
    }
    {
      const int rr = (r + 1 + (lane % 3)) & 3;
      poll_all(ws + WS_FY + ((tile * 4 + rr) * 4 + r) * 64, lane < 3, tag);
    }
#pragma unroll
    for (int k = 0; k < 2; ++k)
#pragma unroll
      for (int i = 0; i < 3; ++i)
        fy[k][i] = __builtin_amdgcn_raw_buffer_load_b128(ry, ((tile * 4 + ((r + 1 + i) & 3)) * 4 + r) * 7168 + (2 * hw + (k < nu ? k : 0)) * 1024 + lane * 16, 0, 16);
    STAMP(20);
    if (MODE == 0 && blockIdx.x == 0 && hw == 3) {
#pragma unroll 1
      for (int i = 0; i < 4; ++i) poll_all(ws + WS_ARRIVE + (64 * i + lane) * 64, true, tag);
      if (lane == 0) st_flag(ws + WS_NONCE, tag);
    }
    for (unsigned sp = 0; sp < (SPIN_MAX >> 3); ++sp) {
      const unsigned v = lane < 3 ? __builtin_amdgcn_raw_buffer_load_b32(rd, ((tile * 4 + ((r + 1 + lane) & 3)) * 4 + r) * 7168 + 6 * 1024 + 63 * 16 + 12, 0, 16) : 0u;
      if (__all(v != 0xffffffffu)) break;
      __builtin_amdgcn_s_sleep(1);
    }
    for (unsigned sp = 0; sp < (SPIN_MAX >> 3); ++sp) {
      bool ok = true;
#pragma unroll
      for (int k = 0; k < 2; ++k)
#pragma unroll
        for (int i = 0; i < 3; ++i) {
          fd[k][i] = __builtin_amdgcn_raw_buffer_load_b128(rd, ((tile * 4 + ((r + 1 + i) & 3)) * 4 + r) * 7168 + (2 * hw + (k < nu ? k : 0)) * 1024 + lane * 16, 0, 16);
        }
#pragma unroll
      for (int k = 0; k < 2; ++k)
#pragma unroll
        for (int i = 0; i < 3; ++i)
          ok = ok && fd[k][i][0] != 0xffffffffu && fd[k][i][1] != 0xffffffffu && fd[k][i][2] != 0xffffffffu && fd[k][i][3] != 0xffffffffu;
      if (__all(ok)) break;
      __builtin_amdgcn_s_sleep(2);
    }
    STAMP(21);
    BAR();
#pragma unroll
    for (int k = 0; k < 2; ++k) {
      if (k < nu) {
        const int u = 2 * hw + k;
        const int ecol = 112 * n + 16 * u + 4 * g;
        f32x4 y0 = *(LAS f32x4*)(lds + (u * 4 + 0) * 1024 + lane * 16), y1 = *(LAS f32x4*)(lds + (u * 4 + 1) * 1024 + lane * 16);
        f32x4 d0 = *(LAS f32x4*)(lds + (u * 4 + 2) * 1024 + lane * 16), d1 = *(LAS f32x4*)(lds + (u * 4 + 3) * 1024 + lane * 16);
#pragma unroll
        for (int i = 0; i < 3; ++i) {
          y0 += f32x4{bf_lo(fy[k][i][0]), bf_hi(fy[k][i][0]), bf_lo(fy[k][i][1]), bf_hi(fy[k][i][1])};
          y1 += f32x4{bf_lo(fy[k][i][2]), bf_hi(fy[k][i][2]), bf_lo(fy[k][i][3]), bf_hi(fy[k][i][3])};
          d0 += f32x4{bf_lo(fd[k][i][0]), bf_hi(fd[k][i][0]), bf_lo(fd[k][i][1]), bf_hi(fd[k][i][1])};
          d1 += f32x4{bf_lo(fd[k][i][2]), bf_hi(fd[k][i][2]), bf_lo(fd[k][i][3]), bf_hi(fd[k][i][3])};
        }
        const u32x2 o0 = {pk_bf16(bf_lo(x0[k][0]) + y0[0] * silu_f(d0[0]), bf_hi(x0[k][0]) + y0[1] * silu_f(d0[1])),
                          pk_bf16(bf_lo(x0[k][1]) + y0[2] * silu_f(d0[2]), bf_hi(x0[k][1]) + y0[3] * silu_f(d0[3]))};
        const u32x2 o1 = {pk_bf16(bf_lo(x1[k][0]) + y1[0] * silu_f(d1[0]), bf_hi(x1[k][0]) + y1[1] * silu_f(d1[1])),
                          pk_bf16(bf_lo(x1[k][1]) + y1[2] * silu_f(d1[2]), bf_hi(x1[k][1]) + y1[3] * silu_f(d1[3]))};
        *(u32x2*)(a.out + (size_t)tok0 * D + ecol) = o0;
        *(u32x2*)(a.out + (size_t)(tok0 + 4) * D + ecol) = o1;
      }
    }
  }
}

#define SPLIT 0
extern "C" void kernel_launch(void* const* d_in, const int* in_sizes, int n_in, void* d_out, int out_size, void* d_ws, size_t ws_size,
                              hipStream_t stream) {
  Args a;
  a.x = (const u16*)d_in[0]; a.Cr = (const u16*)d_in[1]; a.Ci = (const u16*)d_in[2]; a.Dr = (const u16*)d_in[3]; a.Di = (const u16*)d_in[4];
  a.Wg = (const u16*)d_in[6]; a.Wu = (const u16*)d_in[7]; a.Wd = (const u16*)d_in[8];
  a.ws = (char*)d_ws; a.out = (u16*)d_out;
  (void)hipFuncSetAttribute((const void*)fused_kernel<0>, hipFuncAttributeMaxDynamicSharedMemorySize, L_TOTAL);
  fused_kernel<0><<<256, 512, L_TOTAL, stream>>>(a);
}
```
